```python
import torch

def _softmax(x, axis=-1):
    return torch.softmax(x, dim=axis)
def _gelu(x):
    return torch.nn.functional.gelu(x, approximate="tanh")
def _silu(x):
    return torch.nn.functional.silu(x)
def _sigmoid(x):
    return torch.sigmoid(x)
def _layernorm(x, axis=-1, eps=1e-5):
    m = x.mean(dim=axis, keepdim=True)
    v = x.var(dim=axis, keepdim=True, unbiased=False)
    return (x - m) / torch.sqrt(v + eps)
def _scatter_add(tgt, idx, src, axis=0):
    return tgt.clone().index_add_(axis, idx, src)
def _segment_sum(src, seg, n):
    out = torch.zeros((n,) + src.shape[1:], dtype=src.dtype)
    return out.index_add_(0, seg, src)
def _pad(x, pw):
    flat = tuple(v for pair in reversed(pw) for v in pair)
    return torch.nn.functional.pad(x, flat)

kernel_name = 'recurrent_scan__compressed_kv_cla__swiglu_ffn__decode'

def setup_inputs():

    _w = (lambda a: (lambda t: t.to(torch.bfloat16) if t.is_floating_point() else t)(torch.as_tensor(a)))
    def _A_setup():
        B, H, Sq, Sk, D, M = 4, 32, 1, 4096, 128, 512
        Dc = 4 * D
        return {
            "x": _w((lambda *s: torch.randn(*s, dtype=torch.bfloat16) * 0.05)(B, Sq, M)),
            "c_kv": _w((lambda *s: torch.randn(*s, dtype=torch.bfloat16) * 0.8)(B, Sk, Dc)),
            "w_q": _w((lambda *s: torch.randn(*s, dtype=torch.bfloat16) * 1.6)(M, H, D) * (M ** -0.5)),
            "w_uk": _w((lambda *s: torch.randn(*s, dtype=torch.bfloat16) * 1.6)(Dc, H, D) * (Dc ** -0.5)),
            "w_uv": _w((lambda *s: torch.randn(*s, dtype=torch.bfloat16) * 0.8)(Dc, H, D) * (Dc ** -0.5)),
            "w_o": _w((lambda *s: torch.randn(*s, dtype=torch.bfloat16) * 0.8)(H, D, M) * ((H * D) ** -0.5)),
        }
    _A_in = _A_setup()

    def _B_setup():
        B, S, D, F = 4, 1, 512, 1792
        return {
            "x": _w((lambda *s: torch.randn(*s, dtype=torch.bfloat16) * 0.05)(B, S, D)),
            "w_gate": _w((lambda *s: torch.randn(*s, dtype=torch.bfloat16) * 0.2)(D, F)),
            "w_up": _w((lambda *s: torch.randn(*s, dtype=torch.bfloat16) * 0.2)(D, F)),
            "w_down": _w((lambda *s: torch.randn(*s, dtype=torch.bfloat16) * 0.2)(F, D)),
        }
    _B_in = _B_setup()

    return {
        "A_x": _A_in["x"],
        "A_c_kv": _A_in["c_kv"],
        "A_w_q": _A_in["w_q"],
        "A_w_uk": _A_in["w_uk"],
        "A_w_uv": _A_in["w_uv"],
        "A_w_o": _A_in["w_o"],
        "B_x": _B_in["x"],
        "B_w_gate": _B_in["w_gate"],
        "B_w_up": _B_in["w_up"],
        "B_w_down": _B_in["w_down"],
    }

def reference(A_x, A_c_kv, A_w_q, A_w_uk, A_w_uv, A_w_o, B_x, B_w_gate, B_w_up, B_w_down):
    def _A_ref(x, c_kv, w_q, w_uk, w_uv, w_o):
        B, Sq, M = x.shape[0], x.shape[1], x.shape[2]
        Sk = c_kv.shape[1]
        H, D = w_q.shape[1], w_q.shape[2]
        q = torch.einsum("bqm,mhd->bhqd", x, w_q)
        k = torch.einsum("bkc,chd->bhkd", c_kv, w_uk)
        v = torch.einsum("bkc,chd->bhkd", c_kv, w_uv)
        scores = torch.einsum("bhqd,bhkd->bhqk", q, k) * (D ** -0.5)
        off = Sk - Sq
        qi = torch.arange(Sq)[:, None]
        kj = torch.arange(Sk)[None, :]
        cmask = kj <= (qi + off)
        scores = torch.where(cmask[None, None], scores, -1e9)
        p = _softmax(scores, axis=-1)
        ctx = torch.einsum("bhqk,bhkd->bhqd", p, v)
        return torch.einsum("bhqd,hdm->bqm", ctx, w_o)

    def _B_ref(x, w_gate, w_up, w_down):
        g = _silu(x @ w_gate)
        u = x @ w_up
        return (g * u) @ w_down

    x = A_x
    xn = _layernorm(x, axis=-1)
    s = _A_ref(xn, A_c_kv, A_w_q, A_w_uk, A_w_uv, A_w_o)
    g = _silu(_B_ref(xn, B_w_gate, B_w_up, B_w_down))
    return x + s * g
```

```cpp
#include <hip/hip_runtime.h>
#include <hip/hip_bf16.h>
#include <stdint.h>

typedef unsigned short u16;
__device__ __forceinline__ float bf2f(u16 b) { return __uint_as_float(((unsigned)b) << 16); }
__device__ __forceinline__ u16 f2bf(float f) { return __builtin_bit_cast(u16, (__bf16)f); }
__device__ __forceinline__ float rbf(float f) { return bf2f(f2bf(f)); }

constexpr int B = 4, H = 32, SK = 4096, D = 128, M = 512, DC = 512, F = 1792;

__device__ void layernorm_lds(const u16* __restrict__ x, float* xn, float* red) {
  int tid = threadIdx.x;
  int w = tid >> 6, l = tid & 63;
  float v[8]; float s = 0.f;
  for (int i = 0; i < 8; ++i) { v[i] = bf2f(x[w * M + l * 8 + i]); s += v[i]; }
  for (int o = 32; o; o >>= 1) s += __shfl_xor(s, o);
  float mean = s * (1.f / M);
  float ss = 0.f;
  for (int i = 0; i < 8; ++i) { float d = v[i] - mean; ss += d * d; }
  for (int o = 32; o; o >>= 1) ss += __shfl_xor(ss, o);
  float var = rbf(ss * (1.f / M));
  float mb = rbf(mean);
  float den = rbf(sqrtf(rbf(var + 1e-5f)));
  for (int i = 0; i < 8; ++i) xn[w * M + l * 8 + i] = rbf(rbf(v[i] - mb) / den);
  __syncthreads();
}

__global__ void k1(const u16* x, const u16* wq, const u16* wg, const u16* wu, float* q, float* hid) {
  __shared__ float xn[B * M]; __shared__ float red[4][4][64][2];
  layernorm_lds(x, xn, nullptr);
  int tid = threadIdx.x, c = tid & 63, kq = tid >> 6;
  if (blockIdx.x < 64) {
    int n = blockIdx.x * 64 + c;
    float a[4] = {0, 0, 0, 0};
    for (int m = kq * 128; m < kq * 128 + 128; ++m) { float w = bf2f(wq[(size_t)m * 4096 + n]); for (int b = 0; b < 4; ++b) a[b] += xn[b * M + m] * w; }
    for (int b = 0; b < 4; ++b) red[kq][b][c][0] = a[b];
    __syncthreads();
    if (kq == 0) for (int b = 0; b < 4; ++b) q[b * 4096 + n] = rbf(red[0][b][c][0] + red[1][b][c][0] + red[2][b][c][0] + red[3][b][c][0]);
  } else {
    int n = (blockIdx.x - 64) * 64 + c;
    float a[4] = {0, 0, 0, 0}, u[4] = {0, 0, 0, 0};
    for (int m = kq * 128; m < kq * 128 + 128; ++m) { float w = bf2f(wg[(size_t)m * F + n]), w2 = bf2f(wu[(size_t)m * F + n]); for (int b = 0; b < 4; ++b) { a[b] += xn[b * M + m] * w; u[b] += xn[b * M + m] * w2; } }
    for (int b = 0; b < 4; ++b) { red[kq][b][c][0] = a[b]; red[kq][b][c][1] = u[b]; }
    __syncthreads();
    if (kq == 0) for (int b = 0; b < 4; ++b) {
      float g = rbf(red[0][b][c][0] + red[1][b][c][0] + red[2][b][c][0] + red[3][b][c][0]);
      float uu = rbf(red[0][b][c][1] + red[1][b][c][1] + red[2][b][c][1] + red[3][b][c][1]);
      float sg = rbf(g / (1.f + __expf(-g)));
      hid[b * F + n] = rbf(sg * uu);
    }
  }
}
__global__ void k2(const float* q, const u16* wuk, float* qa) {
  int h = blockIdx.x, c = blockIdx.y * 64 + threadIdx.x;
  float a[4] = {0, 0, 0, 0};
  for (int d = 0; d < D; ++d) { float w = bf2f(wuk[((size_t)c * H + h) * D + d]); for (int b = 0; b < 4; ++b) a[b] += q[b * 4096 + h * D + d] * w; }
  for (int b = 0; b < 4; ++b) qa[(b * H + h) * DC + c] = rbf(a[b]);
}
__global__ void k3(const float* qa, const u16* ckv, float* po, float* pm, float* pl) {
  __shared__ float s[H][64]; __shared__ float mx[H];
  int sp = blockIdx.x, b = blockIdx.y, tid = threadIdx.x;
  const u16* kv = ckv + ((size_t)b * SK + sp * 64) * DC;
  for (int i = tid; i < H * 64; i += 256) { int h = i >> 6, k = i & 63; float a = 0; for (int c = 0; c < DC; ++c) a += qa[(b * H + h) * DC + c] * bf2f(kv[k * DC + c]); s[h][k] = a * 0.08838834764831845f; }
  __syncthreads();
  if (tid < H) { float m = -INFINITY; for (int k = 0; k < 64; ++k) m = fmaxf(m, s[tid][k]); float l = 0; for (int k = 0; k < 64; ++k) { float p = __expf(s[tid][k] - m); l += p; s[tid][k] = rbf(p); } pm[(b * H + tid) * 64 + sp] = m; pl[(b * H + tid) * 64 + sp] = l; }
  __syncthreads();
  for (int i = tid; i < H * DC; i += 256) { int h = i >> 9, c = i & 511; float a = 0; for (int k = 0; k < 64; ++k) a += s[h][k] * bf2f(kv[k * DC + c]); po[((size_t)(b * H + h) * 64 + sp) * DC + c] = a; }
}
__global__ void k4(const float* po, const float* pm, const float* pl, const u16* wuv, float* ctx) {
  __shared__ float lat[DC];
  int h = blockIdx.x, b = blockIdx.y, tid = threadIdx.x;
  float M_ = -INFINITY; for (int s = 0; s < 64; ++s) M_ = fmaxf(M_, pm[(b * H + h) * 64 + s]);
  float L = 0, a = 0; for (int s = 0; s < 64; ++s) { float w = __expf(pm[(b * H + h) * 64 + s] - M_); L += w * pl[(b * H + h) * 64 + s]; a += w * po[((size_t)(b * H + h) * 64 + s) * DC + tid]; }
  lat[tid] = rbf(a / L);
  __syncthreads();
  if (tid < D) { float c = 0; for (int k = 0; k < DC; ++k) c += lat[k] * bf2f(wuv[((size_t)k * H + h) * D + tid]); ctx[b * 4096 + h * D + tid] = rbf(c); }
}
__global__ void k5(const u16* x, const float* ctx, const float* hid, const u16* wo, const u16* wd, u16* out) {
  __shared__ float red[4][4][64][2];
  int tid = threadIdx.x, c = tid & 63, kq = tid >> 6, n = blockIdx.x * 64 + c;
  float a[4] = {0, 0, 0, 0}, y[4] = {0, 0, 0, 0};
  for (int k = kq * 1024; k < kq * 1024 + 1024; ++k) { float w = bf2f(wo[(size_t)k * M + n]); for (int b = 0; b < 4; ++b) a[b] += ctx[b * 4096 + k] * w; }
  for (int k = kq * 448; k < kq * 448 + 448; ++k) { float w = bf2f(wd[(size_t)k * M + n]); for (int b = 0; b < 4; ++b) y[b] += hid[b * F + k] * w; }
  for (int b = 0; b < 4; ++b) { red[kq][b][c][0] = a[b]; red[kq][b][c][1] = y[b]; }
  __syncthreads();
  if (kq == 0) for (int b = 0; b < 4; ++b) {
    float s = rbf(red[0][b][c][0] + red[1][b][c][0] + red[2][b][c][0] + red[3][b][c][0]);
    float yy = rbf(red[0][b][c][1] + red[1][b][c][1] + red[2][b][c][1] + red[3][b][c][1]);
    float g = rbf(yy / (1.f + __expf(-yy)));
    out[b * M + n] = f2bf(bf2f(x[b * M + n]) + rbf(s * g));
  }
}

extern "C" void kernel_launch(void* const* d_in, const int* in_sizes, int n_in, void* d_out, int out_size, void* d_ws, size_t ws_size, hipStream_t stream) {
  const u16* x = (const u16*)d_in[0]; const u16* ckv = (const u16*)d_in[1]; const u16* wq = (const u16*)d_in[2]; const u16* wuk = (const u16*)d_in[3];
  const u16* wuv = (const u16*)d_in[4]; const u16* wo = (const u16*)d_in[5]; const u16* wg = (const u16*)d_in[7]; const u16* wu = (const u16*)d_in[8]; const u16* wd = (const u16*)d_in[9];
  float* ws = (float*)d_ws;
  float* q = ws; float* hid = q + 4 * 4096; float* qa = hid + 4 * 1792; float* pm = qa + 4 * 32 * 512; float* pl = pm + 4 * 32 * 64; float* ctx = pl + 4 * 32 * 64; float* po = ctx + 4 * 4096;
  k1<<<92, 256, 0, stream>>>(x, wq, wg, wu, q, hid);
  k2<<<dim3(32, 8), 64, 0, stream>>>(q, wuk, qa);
  k3<<<dim3(64, 4), 256, 0, stream>>>(qa, ckv, po, pm, pl);
  k4<<<dim3(32, 4), 512, 0, stream>>>(po, pm, pl, wuv, ctx);
  k5<<<8, 256, 0, stream>>>(x, ctx, hid, wo, wd, (u16*)d_out);
}
```

```cpp
#include <hip/hip_runtime.h>
#include <hip/hip_bf16.h>
#include <stdint.h>

typedef unsigned short u16;
__device__ __forceinline__ float bf2f(u16 b) { return __uint_as_float(((unsigned)b) << 16); }
__device__ __forceinline__ u16 f2bf(float f) { return __builtin_bit_cast(u16, (__bf16)f); }
__device__ __forceinline__ float rbf(float f) { return f; }
__device__ __forceinline__ float rbf2(float f) { return bf2f(f2bf(f)); }

constexpr int B = 4, H = 32, SK = 4096, D = 128, M = 512, DC = 512, F = 1792;

__device__ void layernorm_lds(const u16* __restrict__ x, float* xn, float* red) {
  int tid = threadIdx.x;
  int w = tid >> 6, l = tid & 63;
  float v[8]; float s = 0.f;
  for (int i = 0; i < 8; ++i) { v[i] = bf2f(x[w * M + l * 8 + i]); s += v[i]; }
  for (int o = 32; o; o >>= 1) s += __shfl_xor(s, o);
  float mean = s * (1.f / M);
  float ss = 0.f;
  for (int i = 0; i < 8; ++i) { float d = v[i] - mean; ss += d * d; }
  for (int o = 32; o; o >>= 1) ss += __shfl_xor(ss, o);
  float var = rbf2(ss * (1.f / M));
  float mb = rbf2(mean);
  float den = rbf2(sqrtf(rbf2(var + 1e-5f)));
  for (int i = 0; i < 8; ++i) xn[w * M + l * 8 + i] = rbf2(rbf2(v[i] - mb) / den);
  __syncthreads();
}

__global__ void k1(const u16* x, const u16* wq, const u16* wg, const u16* wu, float* q, float* hid) {
  __shared__ float xn[B * M]; __shared__ float red[4][4][64][2];
  layernorm_lds(x, xn, nullptr);
  int tid = threadIdx.x, c = tid & 63, kq = tid >> 6;
  if (blockIdx.x < 64) {
    int n = blockIdx.x * 64 + c;
    float a[4] = {0, 0, 0, 0};
    for (int m = kq * 128; m < kq * 128 + 128; ++m) { float w = bf2f(wq[(size_t)m * 4096 + n]); for (int b = 0; b < 4; ++b) a[b] += xn[b * M + m] * w; }
    for (int b = 0; b < 4; ++b) red[kq][b][c][0] = a[b];
    __syncthreads();
    if (kq == 0) for (int b = 0; b < 4; ++b) q[b * 4096 + n] = rbf(red[0][b][c][0] + red[1][b][c][0] + red[2][b][c][0] + red[3][b][c][0]);
  } else {
    int n = (blockIdx.x - 64) * 64 + c;
    float a[4] = {0, 0, 0, 0}, u[4] = {0, 0, 0, 0};
    for (int m = kq * 128; m < kq * 128 + 128; ++m) { float w = bf2f(wg[(size_t)m * F + n]), w2 = bf2f(wu[(size_t)m * F + n]); for (int b = 0; b < 4; ++b) { a[b] += xn[b * M + m] * w; u[b] += xn[b * M + m] * w2; } }
    for (int b = 0; b < 4; ++b) { red[kq][b][c][0] = a[b]; red[kq][b][c][1] = u[b]; }
    __syncthreads();
    if (kq == 0) for (int b = 0; b < 4; ++b) {
      float g = rbf(red[0][b][c][0] + red[1][b][c][0] + red[2][b][c][0] + red[3][b][c][0]);
      float uu = rbf(red[0][b][c][1] + red[1][b][c][1] + red[2][b][c][1] + red[3][b][c][1]);
      float sg = rbf(g / (1.f + __expf(-g)));
      hid[b * F + n] = rbf(sg * uu);
    }
  }
}
__global__ void k2(const float* q, const u16* wuk, float* qa) {
  int h = blockIdx.x, c = blockIdx.y * 64 + threadIdx.x;
  float a[4] = {0, 0, 0, 0};
  for (int d = 0; d < D; ++d) { float w = bf2f(wuk[((size_t)c * H + h) * D + d]); for (int b = 0; b < 4; ++b) a[b] += q[b * 4096 + h * D + d] * w; }
  for (int b = 0; b < 4; ++b) qa[(b * H + h) * DC + c] = rbf(a[b]);
}
__global__ void k3(const float* qa, const u16* ckv, float* po, float* pm, float* pl) {
  __shared__ float s[H][64]; __shared__ float mx[H];
  int sp = blockIdx.x, b = blockIdx.y, tid = threadIdx.x;
  const u16* kv = ckv + ((size_t)b * SK + sp * 64) * DC;
  for (int i = tid; i < H * 64; i += 256) { int h = i >> 6, k = i & 63; float a = 0; for (int c = 0; c < DC; ++c) a += qa[(b * H + h) * DC + c] * bf2f(kv[k * DC + c]); s[h][k] = a * 0.08838834764831845f; }
  __syncthreads();
  if (tid < H) { float m = -INFINITY; for (int k = 0; k < 64; ++k) m = fmaxf(m, s[tid][k]); float l = 0; for (int k = 0; k < 64; ++k) { float p = __expf(s[tid][k] - m); l += p; s[tid][k] = rbf(p); } pm[(b * H + tid) * 64 + sp] = m; pl[(b * H + tid) * 64 + sp] = l; }
  __syncthreads();
  for (int i = tid; i < H * DC; i += 256) { int h = i >> 9, c = i & 511; float a = 0; for (int k = 0; k < 64; ++k) a += s[h][k] * bf2f(kv[k * DC + c]); po[((size_t)(b * H + h) * 64 + sp) * DC + c] = a; }
}
__global__ void k4(const float* po, const float* pm, const float* pl, const u16* wuv, float* ctx) {
  __shared__ float lat[DC];
  int h = blockIdx.x, b = blockIdx.y, tid = threadIdx.x;
  float M_ = -INFINITY; for (int s = 0; s < 64; ++s) M_ = fmaxf(M_, pm[(b * H + h) * 64 + s]);
  float L = 0, a = 0; for (int s = 0; s < 64; ++s) { float w = __expf(pm[(b * H + h) * 64 + s] - M_); L += w * pl[(b * H + h) * 64 + s]; a += w * po[((size_t)(b * H + h) * 64 + s) * DC + tid]; }
  lat[tid] = rbf(a / L);
  __syncthreads();
  if (tid < D) { float c = 0; for (int k = 0; k < DC; ++k) c += lat[k] * bf2f(wuv[((size_t)k * H + h) * D + tid]); ctx[b * 4096 + h * D + tid] = rbf(c); }
}
__global__ void k5(const u16* x, const float* ctx, const float* hid, const u16* wo, const u16* wd, u16* out) {
  __shared__ float red[4][4][64][2];
  int tid = threadIdx.x, c = tid & 63, kq = tid >> 6, n = blockIdx.x * 64 + c;
  float a[4] = {0, 0, 0, 0}, y[4] = {0, 0, 0, 0};
  for (int k = kq * 1024; k < kq * 1024 + 1024; ++k) { float w = bf2f(wo[(size_t)k * M + n]); for (int b = 0; b < 4; ++b) a[b] += ctx[b * 4096 + k] * w; }
  for (int k = kq * 448; k < kq * 448 + 448; ++k) { float w = bf2f(wd[(size_t)k * M + n]); for (int b = 0; b < 4; ++b) y[b] += hid[b * F + k] * w; }
  for (int b = 0; b < 4; ++b) { red[kq][b][c][0] = a[b]; red[kq][b][c][1] = y[b]; }
  __syncthreads();
  if (kq == 0) for (int b = 0; b < 4; ++b) {
    float s = rbf(red[0][b][c][0] + red[1][b][c][0] + red[2][b][c][0] + red[3][b][c][0]);
    float yy = rbf(red[0][b][c][1] + red[1][b][c][1] + red[2][b][c][1] + red[3][b][c][1]);
    float g = rbf(yy / (1.f + __expf(-yy)));
    out[b * M + n] = f2bf(bf2f(x[b * M + n]) + rbf(s * g));
  }
}

extern "C" void kernel_launch(void* const* d_in, const int* in_sizes, int n_in, void* d_out, int out_size, void* d_ws, size_t ws_size, hipStream_t stream) {
  const u16* x = (const u16*)d_in[0]; const u16* ckv = (const u16*)d_in[1]; const u16* wq = (const u16*)d_in[2]; const u16* wuk = (const u16*)d_in[3];
  const u16* wuv = (const u16*)d_in[4]; const u16* wo = (const u16*)d_in[5]; const u16* wg = (const u16*)d_in[7]; const u16* wu = (const u16*)d_in[8]; const u16* wd = (const u16*)d_in[9];
  float* ws = (float*)d_ws;
  float* q = ws; float* hid = q + 4 * 4096; float* qa = hid + 4 * 1792; float* pm = qa + 4 * 32 * 512; float* pl = pm + 4 * 32 * 64; float* ctx = pl + 4 * 32 * 64; float* po = ctx + 4 * 4096;
  k1<<<92, 256, 0, stream>>>(x, wq, wg, wu, q, hid);
  k2<<<dim3(32, 8), 64, 0, stream>>>(q, wuk, qa);
  k3<<<dim3(64, 4), 256, 0, stream>>>(qa, ckv, po, pm, pl);
  k4<<<dim3(32, 4), 512, 0, stream>>>(po, pm, pl, wuv, ctx);
  k5<<<8, 256, 0, stream>>>(x, ctx, hid, wo, wd, (u16*)d_out);
}
```

```cpp
#include <hip/hip_runtime.h>
#include <stdint.h>

typedef unsigned u32;
typedef unsigned short u16;
typedef u32 u32x4 __attribute__((ext_vector_type(4)));
typedef u32 u32x2 __attribute__((ext_vector_type(2)));
typedef float f32x4 __attribute__((ext_vector_type(4)));
typedef short s16x4 __attribute__((ext_vector_type(4)));
typedef __bf16 bf16x8 __attribute__((ext_vector_type(8)));
typedef __bf16 bf16x2 __attribute__((ext_vector_type(2)));
#define LAS __attribute__((address_space(3)))
typedef __amdgpu_buffer_rsrc_t rsrc_t;

#define DIAG 0

constexpr u32 EPOCH_OFF = 0;
constexpr u32 QA_OFF = 4096;
constexpr u32 ST_OFF = QA_OFF + 4 * 32 * 2048;
constexpr u32 E3_OFF = ST_OFF + 32 * 4 * 32 * 8;
constexpr u32 E5S_OFF = E3_OFF + 256 * 32768;
constexpr u32 E5Y_OFF = E5S_OFF + 256 * 256 * 64;
constexpr u32 WS_END = E5Y_OFF + 256 * 256 * 64;
constexpr u32 DBG_OFF = WS_END;
constexpr u32 WS_TOTAL = DBG_OFF + 256 * 32 * 8;

constexpr u32 L_R = 0;
constexpr u32 L_U = 131072;
constexpr u32 L_WDN = L_U + 16384;
constexpr u32 L_M = 155648;
constexpr u32 L_XN = L_M;
constexpr u32 L_P = L_M;
constexpr u32 L_QB = L_M + 4096;
constexpr u32 L_CTX = L_QB;
constexpr u32 L_SM = L_M + 5120;
constexpr u32 L_SL = L_M + 5632;
constexpr u32 L_LAT = L_M + 6144;
constexpr u32 L_F1P = L_M + 6656;
constexpr u32 L_HF = L_M + 7680;
constexpr u32 L_FIN = L_M + 7808;
constexpr u32 LDS_BYTES = 163840;
static_assert(L_FIN + 384 <= LDS_BYTES, "lds");

constexpr u32 SPIN_LIMIT = 1u << 16;

__device__ __forceinline__ float bf2f_lo(u32 v) { return __uint_as_float(v << 16); }
__device__ __forceinline__ float bf2f_hi(u32 v) { return __uint_as_float(v & 0xffff0000u); }
__device__ __forceinline__ u32 pack2(float lo, float hi) { bf16x2 v = {(__bf16)lo, (__bf16)hi}; return __builtin_bit_cast(u32, v); }
__device__ __forceinline__ float rbf(float f) { return __uint_as_float(((u32)__builtin_bit_cast(u16, (__bf16)f)) << 16); }
__device__ __forceinline__ f32x4 mfma32(u32x4 a, u32x4 b, f32x4 c) {
  return __builtin_amdgcn_mfma_f32_16x16x32_bf16(__builtin_bit_cast(bf16x8, a), __builtin_bit_cast(bf16x8, b), c, 0, 0, 0);
}
__device__ __forceinline__ f32x4 mfma16(u32x2 a, u32x2 b, f32x4 c) {
  return __builtin_amdgcn_mfma_f32_16x16x16bf16_1k(__builtin_bit_cast(s16x4, a), __builtin_bit_cast(s16x4, b), c, 0, 0, 0);
}
template <int CTRL> __device__ __forceinline__ float dpp(float x) {
  return __builtin_bit_cast(float, __builtin_amdgcn_mov_dpp(__builtin_bit_cast(int, x), CTRL, 0xf, 0xf, true));
}
constexpr int XOR1 = 0xB1, XOR2 = 0x4E, XOR7 = 0x141, XOR15 = 0x140, XOR8 = 0x128;
__device__ __forceinline__ float x16_sum(float x) { auto s = __builtin_amdgcn_permlane16_swap(__float_as_uint(x), __float_as_uint(x), false, false); return __uint_as_float(s[0]) + __uint_as_float(s[1]); }
__device__ __forceinline__ float x32_sum(float x) { auto s = __builtin_amdgcn_permlane32_swap(__float_as_uint(x), __float_as_uint(x), false, false); return __uint_as_float(s[0]) + __uint_as_float(s[1]); }
__device__ __forceinline__ float x16_max(float x) { auto s = __builtin_amdgcn_permlane16_swap(__float_as_uint(x), __float_as_uint(x), false, false); return fmaxf(__uint_as_float(s[0]), __uint_as_float(s[1])); }
__device__ __forceinline__ float x32_max(float x) { auto s = __builtin_amdgcn_permlane32_swap(__float_as_uint(x), __float_as_uint(x), false, false); return fmaxf(__uint_as_float(s[0]), __uint_as_float(s[1])); }
__device__ __forceinline__ float wave_sum(float v) {
  v += dpp<XOR1>(v); v += dpp<XOR2>(v); v += dpp<XOR7>(v); v += dpp<XOR15>(v); v = x16_sum(v); v = x32_sum(v); return v;
}
__device__ __forceinline__ float half_sum32(float v) {
  v += dpp<XOR1>(v); v += dpp<XOR2>(v); v += dpp<XOR7>(v); v += dpp<XOR15>(v); v = x16_sum(v); return v;
}
__device__ __forceinline__ float half_max32(float v) {
  v = fmaxf(v, dpp<XOR1>(v)); v = fmaxf(v, dpp<XOR2>(v)); v = fmaxf(v, dpp<XOR7>(v)); v = fmaxf(v, dpp<XOR15>(v)); v = x16_max(v); return v;
}

#define BAR() asm volatile("s_waitcnt lgkmcnt(0)\n\ts_barrier" ::: "memory")
#define VMWAIT(n) asm volatile("s_waitcnt vmcnt(" #n ")" ::: "memory")
template <int N> __device__ __forceinline__ void vmwait() { asm volatile("s_waitcnt vmcnt(%0)" :: "i"(N) : "memory"); }

__device__ __forceinline__ void ld128(u32x4& d, u32 voff, rsrc_t rs, u32 soff) {
  asm volatile("buffer_load_dwordx4 %0, %1, %2, %3 offen" : "=v"(d) : "v"(voff), "s"(rs), "s"(soff) : "memory");
}
__device__ __forceinline__ void ld128_sc1(u32x4& d, u32 voff, rsrc_t rs, u32 soff) {
  asm volatile("buffer_load_dwordx4 %0, %1, %2, %3 offen sc1" : "=v"(d) : "v"(voff), "s"(rs), "s"(soff) : "memory");
}
__device__ __forceinline__ void ld64_sc1(u32x2& d, u32 voff, rsrc_t rs, u32 soff) {
  asm volatile("buffer_load_dwordx2 %0, %1, %2, %3 offen sc1" : "=v"(d) : "v"(voff), "s"(rs), "s"(soff) : "memory");
}
__device__ __forceinline__ void ld32_sc1(u32& d, u32 voff, rsrc_t rs, u32 soff) {
  asm volatile("buffer_load_dword %0, %1, %2, %3 offen sc1" : "=v"(d) : "v"(voff), "s"(rs), "s"(soff) : "memory");
}
__device__ __forceinline__ void st128_sc1(u32x4 d, u32 voff, rsrc_t rs) {
  asm volatile("buffer_store_dwordx4 %0, %1, %2, 0 offen sc1\n\ts_nop 1" :: "v"(d), "v"(voff), "s"(rs) : "memory");
}
__device__ __forceinline__ void st64_sc1(u32x2 d, u32 voff, rsrc_t rs) {
  asm volatile("buffer_store_dwordx2 %0, %1, %2, 0 offen sc1\n\ts_nop 1" :: "v"(d), "v"(voff), "s"(rs) : "memory");
}
__device__ __forceinline__ void st32_sc1(u32 d, u32 voff, rsrc_t rs) {
  asm volatile("buffer_store_dword %0, %1, %2, 0 offen sc1\n\ts_nop 1" :: "v"(d), "v"(voff), "s"(rs) : "memory");
}
__device__ __forceinline__ void dma16(u32 lds_dst, u32 voff, rsrc_t rs, u32 soff) {
  asm volatile("s_mov_b32 m0, %0\n\ts_nop 0\n\tbuffer_load_dwordx4 %1, %2, %3 offen lds" :: "s"(lds_dst), "v"(voff), "s"(rs), "s"(soff) : "memory");
}
#define TIE4(a) asm volatile("" : "+v"(a))

__device__ __forceinline__ rsrc_t mk(const void* p, u32 bytes) { return __builtin_amdgcn_make_buffer_rsrc((void*)p, 0, bytes, 0x00020000); }

#define STAMP(i) do {} while (0)

__global__ void __launch_bounds__(512, 2)
fused_decode(const u16* __restrict__ x, const u16* __restrict__ ckv, const u16* __restrict__ wq, const u16* __restrict__ wuk,
             const u16* __restrict__ wuv, const u16* __restrict__ wo, const u16* __restrict__ wg, const u16* __restrict__ wu,
             const u16* __restrict__ wd, u16* __restrict__ out, unsigned char* __restrict__ ws) {
  __shared__ __attribute__((aligned(16))) unsigned char lds[LDS_BYTES];
  LAS unsigned char* L = (LAS unsigned char*)lds;
  const u32 lbase = (u32)(size_t)L;
  const int tid = threadIdx.x;
  const int w = __builtin_amdgcn_readfirstlane(tid >> 6);
  const int l = tid & 63, g = l >> 4, idx = l & 15;
  const int bid = blockIdx.x, xc = bid & 7, r = bid >> 3;
  const int h = xc * 4 + (r >> 3), j = r & 7;
  const int P = xc * 16 + (r >> 1), ab = P >> 5, as = P & 31, ag = r & 1;
  const int u = xc + 8 * (r >> 3);
  const bool ffn_ok = u < 28;
  const int uc = ffn_ok ? u : 27;
  const int f0 = 64 * uc + 8 * j;
  const int pf = u * 8 + j;
  STAMP(0);

  const rsrc_t rs_x = mk(x, 4096), rs_kv = mk(ckv, 4u * 4096 * 1024), rs_wq = mk(wq, 4u << 20), rs_wuk = mk(wuk, 4u << 20),
               rs_wuv = mk(wuv, 4u << 20), rs_wo = mk(wo, 4u << 20), rs_wg = mk(wg, 512 * 1792 * 2), rs_wu = mk(wu, 512 * 1792 * 2),
               rs_wd = mk(wd, 512 * 1792 * 2), rs_ws = mk(ws, WS_TOTAL);

  u32 epoch_v; u32x4 xv; u32x4 wqf[16]; u32x4 wukf[4];
  ld32_sc1(epoch_v, 0u, rs_ws, 0u);
  ld128(xv, (u32)((w & 3) * 1024 + l * 16), rs_x, 0u);
  {
    const u32 vo = (u32)((64 * w + 16 * g) * 8192 + h * 256 + idx * 16);
#pragma unroll
    for (int t = 0; t < 16; ++t) ld128(wqf[t], vo, rs_wq, (u32)(t * 8192));
    const u32 vo2 = (u32)((64 * j + 8 * w + (idx & 7)) * 8192 + h * 256 + g * 16);
#pragma unroll
    for (int kk = 0; kk < 4; ++kk) ld128(wukf[kk], vo2, rs_wuk, (u32)(kk * 64));
  }
  const u32 kvrow0 = (u32)((ab * 4096 + as * 128 + 16 * w) * 1024);
#define KV_DMA(i0) do { _Pragma("unroll") for (int i = (i0); i < (i0) + 4; ++i) \
    dma16(lbase + L_R + (u32)((16 * w + i) * 1024), (u32)((l ^ (2 * (i & 7))) * 16), rs_kv, kvrow0 + (u32)(i * 1024)); } while (0)

  vmwait<20>();
  asm volatile("" : "+v"(epoch_v), "+v"(xv));
  const u32 epoch = (u32)__builtin_amdgcn_readfirstlane(epoch_v) + 1u;
  STAMP(1);
  if (w < 4) {
    float v[8];
    v[0] = bf2f_lo(xv.x); v[1] = bf2f_hi(xv.x); v[2] = bf2f_lo(xv.y); v[3] = bf2f_hi(xv.y);
    v[4] = bf2f_lo(xv.z); v[5] = bf2f_hi(xv.z); v[6] = bf2f_lo(xv.w); v[7] = bf2f_hi(xv.w);
    float s = ((v[0] + v[1]) + (v[2] + v[3])) + ((v[4] + v[5]) + (v[6] + v[7]));
    s = wave_sum(s);
    const float mean = s * (1.f / 512.f);
    float ss = 0.f;
#pragma unroll
    for (int i = 0; i < 8; ++i) { const float d = v[i] - mean; ss = fmaf(d, d, ss); }
    ss = wave_sum(ss);
    const float var = rbf(ss * (1.f / 512.f));
    const float mb = rbf(mean);
    const float den = rbf(__builtin_sqrtf(rbf(var + 1e-5f)));
    const float rd = 1.0f / den;
    u32x4 o;
    o.x = pack2(rbf(v[0] - mb) * rd, rbf(v[1] - mb) * rd);
    o.y = pack2(rbf(v[2] - mb) * rd, rbf(v[3] - mb) * rd);
    o.z = pack2(rbf(v[4] - mb) * rd, rbf(v[5] - mb) * rd);
    o.w = pack2(rbf(v[6] - mb) * rd, rbf(v[7] - mb) * rd);
    *(LAS u32x4*)(L + L_XN + w * 1024 + l * 16) = o;
  }
  KV_DMA(0);
  BAR();
  STAMP(2);

  u32 fx[4];
  {
    u32x4 xp[2][2];
#pragma unroll
    for (int bp = 0; bp < 2; ++bp) {
      const u32 a = L_XN + (u32)((2 * bp + (idx >> 3)) * 1024 + (64 * w + 16 * g) * 2);
      xp[bp][0] = *(LAS u32x4*)(L + a); xp[bp][1] = *(LAS u32x4*)(L + a + 16);
    }
#pragma unroll
    for (int b = 0; b < 4; ++b) fx[b] = *(LAS u32*)(L + L_XN + b * 1024 + (tid & 255) * 4);
    const int jp = idx & 7;
    u32 sel_lo[4], sel_hi[4];
#pragma unroll
    for (int i = 0; i < 4; ++i) {
      const bool on = (i == (jp >> 1));
      sel_lo[i] = on ? ((jp & 1) ? 0x01000c0cu : 0x0c0c0100u) : 0x0c0c0c0cu;
      sel_hi[i] = on ? ((jp & 1) ? 0x03020c0cu : 0x0c0c0302u) : 0x0c0c0c0cu;
    }
    f32x4 qa0 = {0.f, 0.f, 0.f, 0.f}, qa1 = {0.f, 0.f, 0.f, 0.f};
#pragma unroll
    for (int t = 0; t < 16; ++t) {
      if (t == 8) KV_DMA(4);
      if (t < 8) {
        switch (t) { case 0: vmwait<23>(); break; case 1: vmwait<22>(); break; case 2: vmwait<21>(); break; case 3: vmwait<20>(); break;
                     case 4: vmwait<19>(); break; case 5: vmwait<18>(); break; case 6: vmwait<17>(); break; default: vmwait<16>(); break; }
      } else {
        switch (t) { case 8: vmwait<19>(); break; case 9: vmwait<18>(); break; case 10: vmwait<17>(); break; case 11: vmwait<16>(); break;
                     case 12: vmwait<15>(); break; case 13: vmwait<14>(); break; case 14: vmwait<13>(); break; default: vmwait<12>(); break; }
      }
      TIE4(wqf[t]);
      const u32 p0 = xp[0][t >> 3][(t >> 1) & 3], p1 = xp[1][t >> 3][(t >> 1) & 3];
      u32x4 a0, a1;
      if (t & 1) {
        a0 = u32x4{__builtin_amdgcn_perm(p0, p0, sel_hi[0]), __builtin_amdgcn_perm(p0, p0, sel_hi[1]), __builtin_amdgcn_perm(p0, p0, sel_hi[2]), __builtin_amdgcn_perm(p0, p0, sel_hi[3])};
        a1 = u32x4{__builtin_amdgcn_perm(p1, p1, sel_hi[0]), __builtin_amdgcn_perm(p1, p1, sel_hi[1]), __builtin_amdgcn_perm(p1, p1, sel_hi[2]), __builtin_amdgcn_perm(p1, p1, sel_hi[3])};
      } else {
        a0 = u32x4{__builtin_amdgcn_perm(p0, p0, sel_lo[0]), __builtin_amdgcn_perm(p0, p0, sel_lo[1]), __builtin_amdgcn_perm(p0, p0, sel_lo[2]), __builtin_amdgcn_perm(p0, p0, sel_lo[3])};
        a1 = u32x4{__builtin_amdgcn_perm(p1, p1, sel_lo[0]), __builtin_amdgcn_perm(p1, p1, sel_lo[1]), __builtin_amdgcn_perm(p1, p1, sel_lo[2]), __builtin_amdgcn_perm(p1, p1, sel_lo[3])};
      }
      qa0 = mfma32(a0, wqf[t], qa0);
      qa1 = mfma32(a1, wqf[t], qa1);
    }
    const u32 d0 = (u32)(8 * idx + 4 * (g & 1));
    *(LAS f32x4*)(L + L_U + (u32)(((w * 4 + (g >> 1)) * 128 + d0) * 4)) = qa0;
    *(LAS f32x4*)(L + L_U + (u32)(((w * 4 + 2 + (g >> 1)) * 128 + d0) * 4)) = qa1;
  }
  STAMP(3);
  BAR();
  {
    float s = 0.f;
#pragma unroll
    for (int ww = 0; ww < 8; ++ww) s += *(LAS float*)(L + L_U + (u32)((ww * 512 + tid) * 4));
    const u32 pk = pack2(s, s);
    *(LAS u16*)(L + L_QB + tid * 2) = (u16)pk;
  }
  BAR();
  {
    vmwait<8>();
#pragma unroll
    for (int kk = 0; kk < 4; ++kk) TIE4(wukf[kk]);
    f32x4 acc = {0.f, 0.f, 0.f, 0.f};
#pragma unroll
    for (int kk = 0; kk < 4; ++kk) {
      const u32x4 bq = *(LAS u32x4*)(L + L_QB + (u32)(((idx & 3) * 128 + 32 * kk + 8 * g) * 2));
      acc = mfma32(wukf[kk], bq, acc);
    }
    constexpr float SC = 0.08838834764831845f * 1.4426950408889634f;
    u32x4 o; o.x = pack2(acc[0] * SC, acc[1] * SC); o.y = epoch; o.z = pack2(acc[2] * SC, acc[3] * SC); o.w = epoch;
    const u32 off = QA_OFF + (u32)((((idx & 3) * 32 + h) * 256 + (64 * j + 8 * w + 4 * g) / 2) * 8);
    if (idx < 4 && g < 2) st128_sc1(o, off, rs_ws);
  }
  STAMP(4);
  KV_DMA(8);
  KV_DMA(12);
  u32x4 f1a, f1b;
  {
    const u32 vo = (u32)((2 * (tid & 255)) * 3584 + f0 * 2);
    if (w < 4) { ld128(f1a, vo, rs_wg, 0u); ld128(f1b, vo, rs_wg, 3584u); }
    else       { ld128(f1a, vo, rs_wu, 0u); ld128(f1b, vo, rs_wu, 3584u); }
  }
  bool fail = false;
  {
    u32x4 q4[4];
    const u32 vo = QA_OFF + (u32)((ab * 32 + 16 * ag) * 2048 + w * 4096 + l * 16);
    for (u32 spin = 0;; ++spin) {
#pragma unroll
      for (int k = 0; k < 4; ++k) ld128_sc1(q4[k], vo, rs_ws, (u32)(k * 1024));
      VMWAIT(0);
#pragma unroll
      for (int k = 0; k < 4; ++k) TIE4(q4[k]);
      bool ok = true;
#pragma unroll
      for (int k = 0; k < 4; ++k) ok = ok && (q4[k].y == epoch) && (q4[k].w == epoch);
      if (__all(ok)) break;
      if (spin > SPIN_LIMIT) { fail = true; break; }
      __builtin_amdgcn_s_sleep(2);
    }
    STAMP(5);
#pragma unroll
    for (int k = 0; k < 4; ++k) {
      const u32 head = (u32)(2 * w + (k >> 1));
      const u32 uu = (u32)(32 * (k & 1) + (l >> 1));
      u32x2 pv; pv.x = q4[k].x; pv.y = q4[k].z;
      *(LAS u32x2*)(L + L_U + (uu * 16 + (head ^ (uu & 15))) * 16 + 8 * (l & 1)) = pv;
    }
  }
  BAR();
  f32x4 sc = {0.f, 0.f, 0.f, 0.f};
  {
    const u32 sw = (u32)(2 * (idx & 7));
    const u32 rowb = L_R + (u32)((16 * w + idx) * 1024);
#pragma unroll
    for (int kk = 0; kk < 16; ++kk) {
      const u32 uu = (u32)(4 * kk + g);
      const u32x4 bq = *(LAS u32x4*)(L + L_U + (uu * 16 + ((u32)idx ^ (uu & 15))) * 16);
      const u32x4 ak = *(LAS u32x4*)(L + rowb + ((uu ^ sw) * 16));
      sc = mfma32(ak, bq, sc);
    }
  }
  {
    float m = fmaxf(fmaxf(sc[0], sc[1]), fmaxf(sc[2], sc[3]));
    m = x16_max(m); m = x32_max(m);
    if (g == 0) *(LAS float*)(L + L_SM + (u32)((w * 16 + idx) * 4)) = m;
  }
  STAMP(6);
  BAR();
  {
#pragma unroll
    for (int i = 0; i < 2; ++i) {
      const int row = 8 * w + 4 * i + g;
      dma16(lbase + L_U + (u32)((8 * w + 4 * i) * 256), (u32)(row * 8192 + ((idx ^ (2 * (row & 7))) * 16)), rs_wuv, (u32)(64 * j * 8192 + h * 256));
    }
    dma16(lbase + L_WDN + (u32)(w * 1024), (u32)((l ^ (2 * w)) * 16), rs_wd, (u32)((f0 + w) * 1024));
  }
  float mall;
  {
    float m = -INFINITY;
#pragma unroll
    for (int ww = 0; ww < 8; ++ww) m = fmaxf(m, *(LAS float*)(L + L_SM + (u32)((ww * 16 + idx) * 4)));
    mall = m;
    const float p0 = __builtin_amdgcn_exp2f(sc[0] - m), p1 = __builtin_amdgcn_exp2f(sc[1] - m), p2 = __builtin_amdgcn_exp2f(sc[2] - m), p3 = __builtin_amdgcn_exp2f(sc[3] - m);
    float s = (p0 + p1) + (p2 + p3);
    s = x16_sum(s); s = x32_sum(s);
    if (g == 0) *(LAS float*)(L + L_SL + (u32)((w * 16 + idx) * 4)) = s;
    u32x2 pv; pv.x = pack2(p0, p1); pv.y = pack2(p2, p3);
    *(LAS u32x2*)(L + L_P + (u32)((((w >> 1) * 4 + g) * 16 + idx) * 16 + 8 * (w & 1))) = pv;
  }
  BAR();
  const int q_ = idx >> 2, p_ = idx & 3;
  u32 trb[4];
#pragma unroll
  for (int ct = 0; ct < 4; ++ct) {
    const u32 pos = (u32)((8 * w + 2 * ct + (p_ >> 1)) ^ (8 * (g & 1) + 2 * q_));
    trb[ct] = L_R + (u32)((4 * g + q_) * 1024) + pos * 16 + (u32)(8 * (p_ & 1));
  }
  {
    u32x4 pf4[4];
#pragma unroll
    for (int ks = 0; ks < 4; ++ks) pf4[ks] = *(LAS u32x4*)(L + L_P + (u32)((ks * 64 + l) * 16));
    float inv[4];
#pragma unroll
    for (int reg = 0; reg < 4; ++reg) {
      float s = 0.f;
#pragma unroll
      for (int ww = 0; ww < 8; ++ww) s += *(LAS float*)(L + L_SL + (u32)((ww * 16 + 4 * g + reg) * 4));
      inv[reg] = 1.0f / s;
    }
    f32x4 o[4];
#pragma unroll
    for (int ct = 0; ct < 4; ++ct) {
      o[ct] = f32x4{0.f, 0.f, 0.f, 0.f};
#pragma unroll
      for (int ks = 0; ks < 4; ++ks) {
        const s16x4 t0 = __builtin_amdgcn_ds_read_tr16_b64_v4i16((LAS s16x4*)(L + trb[ct] + (u32)(ks * 32768)));
        const s16x4 t1 = __builtin_amdgcn_ds_read_tr16_b64_v4i16((LAS s16x4*)(L + trb[ct] + (u32)(ks * 32768 + 16384)));
        const u32x2 a = __builtin_bit_cast(u32x2, t0), b = __builtin_bit_cast(u32x2, t1);
        o[ct] = mfma32(pf4[ks], u32x4{a.x, a.y, b.x, b.y}, o[ct]);
      }
    }
#pragma unroll
    for (int reg = 0; reg < 4; ++reg) {
      u32x4 v; v.x = pack2(o[0][reg] * inv[reg], o[1][reg] * inv[reg]); v.y = epoch; v.z = pack2(o[2][reg] * inv[reg], o[3][reg] * inv[reg]); v.w = epoch;
      const u32 hh = (u32)(16 * ag + 4 * g + reg);
      st128_sc1(v, E3_OFF + (((hh * 8 + (u32)w) * 4 + (u32)ab) * 32 + (u32)as) * 256 + (u32)(idx * 16), rs_ws);
    }
    if (w == 0 && g == 0) {
      float s = 0.f;
#pragma unroll
      for (int ww = 0; ww < 8; ++ww) s += *(LAS float*)(L + L_SL + (u32)((ww * 16 + idx) * 4));
      const float lse = mall + __builtin_amdgcn_logf(s);
      u32x2 v; v.x = __float_as_uint(lse); v.y = epoch;
      st64_sc1(v, ST_OFF + (u32)((((16 * ag + idx) * 4 + ab) * 32 + as) * 8), rs_ws);
    }
  }
  STAMP(7);
  BAR();
#pragma unroll
  for (int i = 0; i < 16; ++i)
    dma16(lbase + L_R + (u32)((16 * w + i) * 1024), (u32)((l ^ (2 * (i & 7))) * 16), rs_wo, (u32)((h * 128 + 16 * w + i) * 1024));
  {
    TIE4(f1a); TIE4(f1b);
    u32 wp[8];
    wp[0] = __builtin_amdgcn_perm(f1b.x, f1a.x, 0x05040100u); wp[1] = __builtin_amdgcn_perm(f1b.x, f1a.x, 0x07060302u);
    wp[2] = __builtin_amdgcn_perm(f1b.y, f1a.y, 0x05040100u); wp[3] = __builtin_amdgcn_perm(f1b.y, f1a.y, 0x07060302u);
    wp[4] = __builtin_amdgcn_perm(f1b.z, f1a.z, 0x05040100u); wp[5] = __builtin_amdgcn_perm(f1b.z, f1a.z, 0x07060302u);
    wp[6] = __builtin_amdgcn_perm(f1b.w, f1a.w, 0x05040100u); wp[7] = __builtin_amdgcn_perm(f1b.w, f1a.w, 0x07060302u);
    float v[32];
#pragma unroll
    for (int b = 0; b < 4; ++b)
#pragma unroll
      for (int f = 0; f < 8; ++f)
        v[b * 8 + f] = __builtin_amdgcn_fdot2_f32_bf16(__builtin_bit_cast(bf16x2, wp[f]), __builtin_bit_cast(bf16x2, fx[b]), 0.f, false);
#pragma unroll
    for (int i = 0; i < 16; ++i) {
      auto s = __builtin_amdgcn_permlane32_swap(__float_as_uint(v[i]), __float_as_uint(v[i + 16]), false, false);
      v[i] = __uint_as_float(s[0]) + __uint_as_float(s[1]);
    }
#pragma unroll
    for (int i = 0; i < 8; ++i) {
      auto s = __builtin_amdgcn_permlane16_swap(__float_as_uint(v[i]), __float_as_uint(v[i + 8]), false, false);
      v[i] = __uint_as_float(s[0]) + __uint_as_float(s[1]);
    }
    {
      const bool b3 = (l & 8) != 0;
#pragma unroll
      for (int i = 0; i < 4; ++i) { const float snd = b3 ? v[i] : v[i + 4], keep = b3 ? v[i + 4] : v[i]; v[i] = keep + dpp<XOR8>(snd); }
      const bool b2 = (l & 4) != 0;
#pragma unroll
      for (int i = 0; i < 2; ++i) { const float snd = b2 ? v[i] : v[i + 2], keep = b2 ? v[i + 2] : v[i]; v[i] = keep + dpp<XOR7>(snd); }
      const bool b1 = (l & 2) != 0;
      { const float snd = b1 ? v[0] : v[1], keep = b1 ? v[1] : v[0]; v[0] = keep + dpp<XOR2>(snd); }
      v[0] += dpp<XOR1>(v[0]);
    }
    if ((l & 1) == 0) *(LAS float*)(L + L_F1P + (u32)((w * 32 + (l >> 1)) * 4)) = v[0];
  }
  BAR();
  if (tid < 32) {
    float gsum = 0.f, usum = 0.f;
#pragma unroll
    for (int ww = 0; ww < 4; ++ww) { gsum += *(LAS float*)(L + L_F1P + (u32)((ww * 32 + tid) * 4)); usum += *(LAS float*)(L + L_F1P + (u32)(((ww + 4) * 32 + tid) * 4)); }
    const float gb = rbf(gsum), ub = rbf(usum);
    const float sg = rbf(gb / (1.0f + __expf(-gb)));
    const u32 hv = pack2(sg * ub, 0.f);
    const int b = tid >> 3, f = tid & 7;
    *(LAS u16*)(L + L_HF + (u32)((((f >> 2) * 4 + b) * 4 + (f & 3)) * 2)) = (u16)hv;
  }
  vmwait<16>();
  BAR();
  {
    u32x2 hb = *(LAS u32x2*)(L + L_HF + (u32)((((g & 1) * 4 + (idx & 3)) * 4) * 2));
    if (g >= 2) { hb.x = 0u; hb.y = 0u; }
    f32x4 y[4];
#pragma unroll
    for (int mt = 0; mt < 4; ++mt) {
      const int row = (4 * g + q_) & 7;
      const u32 pos = (u32)((8 * w + 2 * mt + (p_ >> 1)) ^ (2 * row));
      const s16x4 t0 = __builtin_amdgcn_ds_read_tr16_b64_v4i16((LAS s16x4*)(L + L_WDN + (u32)(row * 1024) + pos * 16 + (u32)(8 * (p_ & 1))));
      y[mt] = mfma16(__builtin_bit_cast(u32x2, t0), hb, f32x4{0.f, 0.f, 0.f, 0.f});
    }
    const int rep = idx >> 2;
    f32x4 v = rep == 0 ? y[0] : rep == 1 ? y[1] : rep == 2 ? y[2] : y[3];
    const u32 c0 = (u32)(32 * w + 8 * rep + 2 * g);
    if (ffn_ok) {
      u32x4 s0; s0.x = __float_as_uint(v[0]); s0.y = epoch; s0.z = __float_as_uint(v[1]); s0.w = epoch;
      u32x4 s1; s1.x = __float_as_uint(v[2]); s1.y = epoch; s1.z = __float_as_uint(v[3]); s1.w = epoch;
      st128_sc1(s0, E5Y_OFF + (c0 * 256 + (u32)pf) * 64 + (u32)((idx & 3) * 16), rs_ws);
      st128_sc1(s1, E5Y_OFF + ((c0 + 1) * 256 + (u32)pf) * 64 + (u32)((idx & 3) * 16), rs_ws);
    }
  }
  STAMP(8);
  {
    const int b = w >> 1, ch = w & 1, pc = 8 * ch + (l & 7), ss = l >> 3;
    u32x4 e4[4]; u32x2 st;
    const u32 vo = E3_OFF + (u32)(((h * 8 + j) * 4 + b) * 32 * 256 + ss * 256 + pc * 16);
    const u32 so = ST_OFF + (u32)(((h * 4 + b) * 32 + (l & 31)) * 8);
    for (u32 spin = 0;; ++spin) {
#pragma unroll
      for (int k = 0; k < 4; ++k) ld128_sc1(e4[k], vo, rs_ws, (u32)(k * 2048));
      ld64_sc1(st, so, rs_ws, 0u);
      VMWAIT(0);
#pragma unroll
      for (int k = 0; k < 4; ++k) TIE4(e4[k]);
      TIE4(st);
      bool ok = (st.y == epoch);
#pragma unroll
      for (int k = 0; k < 4; ++k) ok = ok && (e4[k].y == epoch) && (e4[k].w == epoch);
      if (__all(ok)) break;
      if (spin > SPIN_LIMIT) { fail = true; break; }
      __builtin_amdgcn_s_sleep(2);
    }
    STAMP(9);
    const float lse = __uint_as_float(st.x);
    const float M = half_max32(lse);
    const float e = __builtin_amdgcn_exp2f(lse - M);
    const float S = half_sum32(e);
    const float wgt = e / S;
    float acc[4] = {0.f, 0.f, 0.f, 0.f};
#pragma unroll
    for (int k = 0; k < 4; ++k) {
      const float ws_ = __builtin_bit_cast(float, __builtin_amdgcn_ds_bpermute((8 * k + ss) * 4, __builtin_bit_cast(int, wgt)));
      acc[0] = fmaf(ws_, bf2f_lo(e4[k].x), acc[0]); acc[1] = fmaf(ws_, bf2f_hi(e4[k].x), acc[1]);
      acc[2] = fmaf(ws_, bf2f_lo(e4[k].z), acc[2]); acc[3] = fmaf(ws_, bf2f_hi(e4[k].z), acc[3]);
    }
#pragma unroll
    for (int e2 = 0; e2 < 4; ++e2) { acc[e2] += dpp<XOR8>(acc[e2]); acc[e2] = x16_sum(acc[e2]); acc[e2] = x32_sum(acc[e2]); }
    if (l < 8) {
#pragma unroll
      for (int e2 = 0; e2 < 4; ++e2) {
        const u32 pk = pack2(acc[e2], 0.f);
        *(LAS u16*)(L + L_LAT + (u32)(((((e2 >> 1) * 4 + (pc >> 2)) * 4 + b) * 8 + 4 * (e2 & 1) + (pc & 3)) * 2)) = (u16)pk;
      }
    }
  }
  BAR();
  {
    f32x4 c = {0.f, 0.f, 0.f, 0.f};
    const u32 pos = (u32)((2 * w + (p_ >> 1)) ^ (8 * (g & 1) + 2 * q_));
    const u32 ub = L_U + (u32)((4 * g + q_) * 256) + pos * 16 + (u32)(8 * (p_ & 1));
#pragma unroll
    for (int ks = 0; ks < 2; ++ks) {
      const s16x4 t0 = __builtin_amdgcn_ds_read_tr16_b64_v4i16((LAS s16x4*)(L + ub + (u32)(ks * 8192)));
      const s16x4 t1 = __builtin_amdgcn_ds_read_tr16_b64_v4i16((LAS s16x4*)(L + ub + (u32)(ks * 8192 + 4096)));
      const u32x2 a = __builtin_bit_cast(u32x2, t0), b = __builtin_bit_cast(u32x2, t1);
      const u32x4 lb = *(LAS u32x4*)(L + L_LAT + (u32)(((ks * 4 + g) * 4 + (idx & 3)) * 16));
      c = mfma32(u32x4{a.x, a.y, b.x, b.y}, lb, c);
    }
    if (idx < 4) {
      u32x2 v; v.x = pack2(c[0], c[1]); v.y = pack2(c[2], c[3]);
      *(LAS u32x2*)(L + L_CTX + (u32)((((w >> 1) * 4 + g) * 4 + idx) * 16 + 8 * (w & 1))) = v;
    }
  }
  BAR();
  {
    u32x4 cf[4];
#pragma unroll
    for (int ks = 0; ks < 4; ++ks) cf[ks] = *(LAS u32x4*)(L + L_CTX + (u32)(((ks * 4 + g) * 4 + (idx & 3)) * 16));
    f32x4 o[4];
#pragma unroll
    for (int mt = 0; mt < 4; ++mt) {
      o[mt] = f32x4{0.f, 0.f, 0.f, 0.f};
#pragma unroll
      for (int ks = 0; ks < 4; ++ks) {
        const s16x4 t0 = __builtin_amdgcn_ds_read_tr16_b64_v4i16((LAS s16x4*)(L + trb[mt] + (u32)(ks * 32768)));
        const s16x4 t1 = __builtin_amdgcn_ds_read_tr16_b64_v4i16((LAS s16x4*)(L + trb[mt] + (u32)(ks * 32768 + 16384)));
        const u32x2 a = __builtin_bit_cast(u32x2, t0), b = __builtin_bit_cast(u32x2, t1);
        o[mt] = mfma32(u32x4{a.x, a.y, b.x, b.y}, cf[ks], o[mt]);
      }
    }
    const int rep = idx >> 2;
    f32x4 v = rep == 0 ? o[0] : rep == 1 ? o[1] : rep == 2 ? o[2] : o[3];
    const u32 c0 = (u32)(32 * w + 8 * rep + 2 * g);
    u32x4 s0; s0.x = __float_as_uint(v[0]); s0.y = epoch; s0.z = __float_as_uint(v[1]); s0.w = epoch;
    u32x4 s1; s1.x = __float_as_uint(v[2]); s1.y = epoch; s1.z = __float_as_uint(v[3]); s1.w = epoch;
    st128_sc1(s0, E5S_OFF + (c0 * 256 + (u32)bid) * 64 + (u32)((idx & 3) * 16), rs_ws);
    st128_sc1(s1, E5S_OFF + ((c0 + 1) * 256 + (u32)bid) * 64 + (u32)((idx & 3) * 16), rs_ws);
  }
  STAMP(10);
  {
    u32x4 a4[2], y4[2];
    const u32 vs = E5S_OFF + (u32)(bid * 16384 + tid * 16), vy = E5Y_OFF + (u32)(bid * 16384 + tid * 16);
    const bool y1 = tid < 384;
    for (u32 spin = 0;; ++spin) {
      ld128_sc1(a4[0], vs, rs_ws, 0u); ld128_sc1(a4[1], vs, rs_ws, 8192u);
      ld128_sc1(y4[0], vy, rs_ws, 0u); ld128_sc1(y4[1], vy, rs_ws, 8192u);
      VMWAIT(0);
      TIE4(a4[0]); TIE4(a4[1]); TIE4(y4[0]); TIE4(y4[1]);
      bool ok = (a4[0].y == epoch) && (a4[0].w == epoch) && (a4[1].y == epoch) && (a4[1].w == epoch) && (y4[0].y == epoch) && (y4[0].w == epoch);
      ok = ok && (!y1 || ((y4[1].y == epoch) && (y4[1].w == epoch)));
      if (__all(ok)) break;
      if (spin > SPIN_LIMIT) { fail = true; break; }
      __builtin_amdgcn_s_sleep(2);
    }
    STAMP(11);
    float s0 = __uint_as_float(a4[0].x) + __uint_as_float(a4[1].x), s1 = __uint_as_float(a4[0].z) + __uint_as_float(a4[1].z);
    float t0 = __uint_as_float(y4[0].x) + (y1 ? __uint_as_float(y4[1].x) : 0.f), t1 = __uint_as_float(y4[0].z) + (y1 ? __uint_as_float(y4[1].z) : 0.f);
    s0 += dpp<0x124>(s0); s1 += dpp<0x124>(s1); t0 += dpp<0x124>(t0); t1 += dpp<0x124>(t1);
    s0 += dpp<0x128>(s0); s1 += dpp<0x128>(s1); t0 += dpp<0x128>(t0); t1 += dpp<0x128>(t1);
    s0 = x16_sum(s0); s1 = x16_sum(s1); t0 = x16_sum(t0); t1 = x16_sum(t1);
    s0 = x32_sum(s0); s1 = x32_sum(s1); t0 = x32_sum(t0); t1 = x32_sum(t1);
    if (l < 4) *(LAS f32x4*)(L + L_F1P + (u32)((w * 4 + l) * 16)) = f32x4{s0, s1, t0, t1};
  }
  BAR();
  if (tid < 8) {
    const int b = tid >> 1, mi = tid & 1;
    float s = 0.f, y = 0.f;
#pragma unroll
    for (int ww = 0; ww < 8; ++ww) { s += *(LAS float*)(L + L_F1P + (u32)(((ww * 4 + b) * 4 + mi) * 4)); y += *(LAS float*)(L + L_F1P + (u32)(((ww * 4 + b) * 4 + 2 + mi) * 4)); }
    const float sb = rbf(s), yb = rbf(y);
    const float gg = rbf(yb / (1.0f + __expf(-yb)));
    const float xv_ = __uint_as_float(((u32)x[b * 512 + 2 * bid + mi]) << 16);
    float o = xv_ + rbf(sb * gg);
    if (fail) o = __uint_as_float(0x7fc00000u);
    out[b * 512 + 2 * bid + mi] = (u16)(pack2(o, 0.f));
  }
  if (bid == 0 && tid == 0) st32_sc1(epoch, EPOCH_OFF, rs_ws);
}

extern "C" void kernel_launch(void* const* d_in, const int* in_sizes, int n_in, void* d_out, int out_size, void* d_ws, size_t ws_size, hipStream_t stream) {
  const u16* x = (const u16*)d_in[0]; const u16* ckv = (const u16*)d_in[1]; const u16* wq = (const u16*)d_in[2]; const u16* wuk = (const u16*)d_in[3];
  const u16* wuv = (const u16*)d_in[4]; const u16* wo = (const u16*)d_in[5]; const u16* wg = (const u16*)d_in[7]; const u16* wu = (const u16*)d_in[8]; const u16* wd = (const u16*)d_in[9];
  fused_decode<<<256, 512, 0, stream>>>(x, ckv, wq, wuk, wuv, wo, wg, wu, wd, (u16*)d_out, (unsigned char*)d_ws);
}
```

```cpp
#include <hip/hip_runtime.h>
#include <stdint.h>

typedef unsigned u32;
typedef unsigned short u16;
typedef u32 u32x4 __attribute__((ext_vector_type(4)));
typedef u32 u32x2 __attribute__((ext_vector_type(2)));
typedef float f32x4 __attribute__((ext_vector_type(4)));
typedef short s16x4 __attribute__((ext_vector_type(4)));
typedef __bf16 bf16x8 __attribute__((ext_vector_type(8)));
typedef __bf16 bf16x2 __attribute__((ext_vector_type(2)));
#define LAS __attribute__((address_space(3)))
typedef __amdgpu_buffer_rsrc_t rsrc_t;

#define DIAG 0
#define NT_W 1

constexpr u32 EPOCH_OFF = 0;
constexpr u32 QA_OFF = 4096;
constexpr u32 ST_OFF = QA_OFF + 4 * 32 * 2048;
constexpr u32 CF_OFF = ST_OFF + 32 * 4 * 32 * 8;
constexpr u32 EF_OFF = CF_OFF + 4096;
constexpr u32 E5Y_OFF = EF_OFF + 32 * 32768;
constexpr u32 E3_OFF = E5Y_OFF + 128 * 4096;
constexpr u32 E5S_OFF = E3_OFF + 256 * 16384;
constexpr u32 WS_END = E5S_OFF + 128 * 16384;
constexpr u32 DBG_OFF = WS_END;
constexpr u32 WS_TOTAL = DBG_OFF + 256 * 64 * 8;
constexpr u32 SENT = 0xFFFFFFFFu;

constexpr u32 L_R = 0;
constexpr u32 L_U = 131072;
constexpr u32 L_WDN = L_U + 16384;
constexpr u32 L_M = 155648;
constexpr u32 L_XN = L_M;
constexpr u32 L_P = L_M;
constexpr u32 L_QB = L_M + 4096;
constexpr u32 L_CTX = L_QB;
constexpr u32 L_SM = L_M + 5120;
constexpr u32 L_SL = L_M + 5632;
constexpr u32 L_LAT = L_M + 6144;
constexpr u32 L_HF = L_M + 6656;
constexpr u32 L_FIN = L_M + 7168;
constexpr u32 L_HS = L_M;
constexpr u32 L_FP = L_WDN;
constexpr u32 L_CNT = L_M + 7680;
constexpr u32 L_XF = L_M + 7696;
constexpr u32 LDS_BYTES = 163840;

constexpr u32 SPIN_LIMIT = 1u << 16;
#define KD 6
constexpr int PD = 8;
#define KS 11
#define E2_SLEEP 8

__device__ __forceinline__ float bf2f_lo(u32 v) { return __uint_as_float(v << 16); }
__device__ __forceinline__ float bf2f_hi(u32 v) { return __uint_as_float(v & 0xffff0000u); }
__device__ __forceinline__ u32 pack2(float lo, float hi) { bf16x2 v = {(__bf16)lo, (__bf16)hi}; return __builtin_bit_cast(u32, v); }
__device__ __forceinline__ float rbf(float f) { return __uint_as_float(((u32)__builtin_bit_cast(u16, (__bf16)f)) << 16); }
__device__ __forceinline__ f32x4 mfma32(u32x4 a, u32x4 b, f32x4 c) {
  return __builtin_amdgcn_mfma_f32_16x16x32_bf16(__builtin_bit_cast(bf16x8, a), __builtin_bit_cast(bf16x8, b), c, 0, 0, 0);
}
__device__ __forceinline__ f32x4 mfma16(u32x2 a, u32x2 b, f32x4 c) {
  return __builtin_amdgcn_mfma_f32_16x16x16bf16_1k(__builtin_bit_cast(s16x4, a), __builtin_bit_cast(s16x4, b), c, 0, 0, 0);
}
template <int CTRL> __device__ __forceinline__ float dpp(float x) {
  return __builtin_bit_cast(float, __builtin_amdgcn_mov_dpp(__builtin_bit_cast(int, x), CTRL, 0xf, 0xf, true));
}
constexpr int XOR1 = 0xB1, XOR2 = 0x4E, XOR7 = 0x141, XOR15 = 0x140, XOR8 = 0x128;
__device__ __forceinline__ float x16_sum(float x) { auto s = __builtin_amdgcn_permlane16_swap(__float_as_uint(x), __float_as_uint(x), false, false); return __uint_as_float(s[0]) + __uint_as_float(s[1]); }
__device__ __forceinline__ float x32_sum(float x) { auto s = __builtin_amdgcn_permlane32_swap(__float_as_uint(x), __float_as_uint(x), false, false); return __uint_as_float(s[0]) + __uint_as_float(s[1]); }
__device__ __forceinline__ float x16_max(float x) { auto s = __builtin_amdgcn_permlane16_swap(__float_as_uint(x), __float_as_uint(x), false, false); return fmaxf(__uint_as_float(s[0]), __uint_as_float(s[1])); }
__device__ __forceinline__ float x32_max(float x) { auto s = __builtin_amdgcn_permlane32_swap(__float_as_uint(x), __float_as_uint(x), false, false); return fmaxf(__uint_as_float(s[0]), __uint_as_float(s[1])); }
__device__ __forceinline__ float wave_sum(float v) {
  v += dpp<XOR1>(v); v += dpp<XOR2>(v); v += dpp<XOR7>(v); v += dpp<XOR15>(v); v = x16_sum(v); v = x32_sum(v); return v;
}
__device__ __forceinline__ float half_sum32(float v) {
  v += dpp<XOR1>(v); v += dpp<XOR2>(v); v += dpp<XOR7>(v); v += dpp<XOR15>(v); v = x16_sum(v); return v;
}
__device__ __forceinline__ float half_max32(float v) {
  v = fmaxf(v, dpp<XOR1>(v)); v = fmaxf(v, dpp<XOR2>(v)); v = fmaxf(v, dpp<XOR7>(v)); v = fmaxf(v, dpp<XOR15>(v)); v = x16_max(v); return v;
}

#define BAR() asm volatile("s_waitcnt lgkmcnt(0)\n\ts_barrier" ::: "memory")
#define RAWBAR() asm volatile("s_barrier" ::: "memory")
#define VMWAIT(n) asm volatile("s_waitcnt vmcnt(" #n ")" ::: "memory")
#define SCHED() __builtin_amdgcn_sched_barrier(0)
template <int N> __device__ __forceinline__ void vmwait() { asm volatile("s_waitcnt vmcnt(%0)" :: "i"(N) : "memory"); }
#define PIN(a) asm volatile("" : "+v"(a))

#define NTS " nt"
__device__ __forceinline__ void ld128(u32x4& d, u32 voff, rsrc_t rs, u32 soff) {
  asm volatile("buffer_load_dwordx4 %0, %1, %2, %3 offen" : "=v"(d) : "v"(voff), "s"(rs), "s"(soff) : "memory");
}
__device__ __forceinline__ void ld128_w(u32x4& d, u32 voff, rsrc_t rs, u32 soff) {
  asm volatile("buffer_load_dwordx4 %0, %1, %2, %3 offen" NTS : "=v"(d) : "v"(voff), "s"(rs), "s"(soff) : "memory");
}
__device__ __forceinline__ void ld128_sc1(u32x4& d, u32 voff, rsrc_t rs, u32 soff) {
  asm volatile("buffer_load_dwordx4 %0, %1, %2, %3 offen sc1" : "=v"(d) : "v"(voff), "s"(rs), "s"(soff) : "memory");
}
__device__ __forceinline__ void ld64_sc1(u32x2& d, u32 voff, rsrc_t rs, u32 soff) {
  asm volatile("buffer_load_dwordx2 %0, %1, %2, %3 offen sc1" : "=v"(d) : "v"(voff), "s"(rs), "s"(soff) : "memory");
}
__device__ __forceinline__ void ld32_sc1(u32& d, u32 voff, rsrc_t rs, u32 soff) {
  asm volatile("buffer_load_dword %0, %1, %2, %3 offen sc1" : "=v"(d) : "v"(voff), "s"(rs), "s"(soff) : "memory");
}
__device__ __forceinline__ void ld32(u32& d, u32 voff, rsrc_t rs, u32 soff) {
  asm volatile("buffer_load_dword %0, %1, %2, %3 offen" : "=v"(d) : "v"(voff), "s"(rs), "s"(soff) : "memory");
}
__device__ __forceinline__ void st128_sc1(u32x4 d, u32 voff, rsrc_t rs, u32 soff) {
  asm volatile("buffer_store_dwordx4 %0, %1, %2, %3 offen sc1\n\ts_nop 1" :: "v"(d), "v"(voff), "s"(rs), "s"(soff) : "memory");
}
__device__ __forceinline__ void st64_sc1(u32x2 d, u32 voff, rsrc_t rs) {
  asm volatile("buffer_store_dwordx2 %0, %1, %2, 0 offen sc1\n\ts_nop 1" :: "v"(d), "v"(voff), "s"(rs) : "memory");
}
__device__ __forceinline__ void st32_sc1(u32 d, u32 voff, rsrc_t rs) {
  asm volatile("buffer_store_dword %0, %1, %2, 0 offen sc1\n\ts_nop 1" :: "v"(d), "v"(voff), "s"(rs) : "memory");
}
__device__ __forceinline__ void dma16(u32 lds_dst, u32 voff, rsrc_t rs, u32 soff) {
  asm volatile("s_mov_b32 m0, %0\n\ts_nop 0\n\tbuffer_load_dwordx4 %1, %2, %3 offen" NTS " lds" :: "s"(lds_dst), "v"(voff), "s"(rs), "s"(soff) : "memory");
}
__device__ __forceinline__ void dma16_def(u32 lds_dst, u32 voff, rsrc_t rs, u32 soff) {
  asm volatile("s_mov_b32 m0, %0\n\ts_nop 0\n\tbuffer_load_dwordx4 %1, %2, %3 offen lds" :: "s"(lds_dst), "v"(voff), "s"(rs), "s"(soff) : "memory");
}
#define KV_NT 1
#define KV_STAG 0
#define WO_NT 0
#define WQ_NT 0
#define dma16_kv dma16
#define dma16_wo dma16_def
#define ld128_wq ld128
__device__ __forceinline__ rsrc_t mk(const void* p, u32 bytes) { return __builtin_amdgcn_make_buffer_rsrc((void*)p, 0, bytes, 0x00020000); }
__device__ __forceinline__ u32x4 lds128(LAS unsigned char* L, u32 a) { return *(LAS u32x4*)(L + a); }
__device__ __forceinline__ u32x4 trfrag(LAS unsigned char* L, u32 a, u32 o0, u32 o1) {
  const s16x4 t0 = __builtin_amdgcn_ds_read_tr16_b64_v4i16((LAS s16x4*)(L + a + o0));
  const s16x4 t1 = __builtin_amdgcn_ds_read_tr16_b64_v4i16((LAS s16x4*)(L + a + o1));
  const u32x2 x = __builtin_bit_cast(u32x2, t0), y = __builtin_bit_cast(u32x2, t1);
  return u32x4{x.x, x.y, y.x, y.y};
}
__device__ __forceinline__ bool tags_ok(const u32x4* v, int n, u32 epoch) {
  u32 a = v[0].y & v[0].w, o = v[0].y | v[0].w;
#pragma unroll
  for (int k = 1; k < n; ++k) { a &= v[k].y & v[k].w; o |= v[k].y | v[k].w; }
  return a == epoch && o == epoch;
}

#define STAMP(i) do {} while (0)

__global__ void __launch_bounds__(512, 2)
fused_decode(const u16* __restrict__ x, const u16* __restrict__ ckv, const u16* __restrict__ wq, const u16* __restrict__ wuk,
             const u16* __restrict__ wuv, const u16* __restrict__ wo, const u16* __restrict__ wg, const u16* __restrict__ wu,
             const u16* __restrict__ wd, u16* __restrict__ out, unsigned char* __restrict__ ws) {
  __shared__ __attribute__((aligned(16))) unsigned char lds[LDS_BYTES];
  LAS unsigned char* L = (LAS unsigned char*)lds;
  const u32 lbase = (u32)(size_t)L;
  const int tid = threadIdx.x;
  const int w = __builtin_amdgcn_readfirstlane(tid >> 6);
  const int l = tid & 63, g = l >> 4, idx = l & 15;
  const int bid = blockIdx.x, xc = bid & 7, r = bid >> 3;
  const int h = xc + 8 * (r >> 3), j = r & 7;
  const int P = xc * 16 + (r & 15), ab = P >> 5, as = P & 31, ag = 1 - (r >> 4);
  const bool ffn_ok = h < 28;
  const int uc = ffn_ok ? h : 27;
  const int fmat = uc & 1, ftile = uc >> 1;
  STAMP(0);

  const rsrc_t rs_x = mk(x, 4096), rs_kv = mk(ckv, 4u * 4096 * 1024), rs_wq = mk(wq, 4u << 20), rs_wuk = mk(wuk, 4u << 20),
               rs_wuv = mk(wuv, 4u << 20), rs_wo = mk(wo, 4u << 20), rs_wg = mk(wg, 512 * 1792 * 2), rs_wu = mk(wu, 512 * 1792 * 2),
               rs_wd = mk(wd, 512 * 1792 * 2), rs_ws = mk(ws, WS_TOTAL);

  const bool chain = w < 4;
  const int sw = w & 3;
  u32x4 xv; u32x4 wqf[16]; u32x4 aux[4];
  const int wb = (w + j) & 7;
  const u32 vo_wq = (u32)((64 * wb + 16 * g) * 8192 + h * 256 + idx * 16);
  const rsrc_t rs_f1 = fmat ? rs_wu : rs_wg;
  if (chain) ld128(xv, (u32)(w * 1024 + l * 16), rs_x, 0u);
  if (tid == 0) { *(LAS u32*)(L + L_CNT) = 0u; *(LAS u32*)(L + L_XF) = 0u; }
  BAR();
#pragma unroll
  for (int t = 0; t < 4; ++t) ld128_wq(wqf[t], vo_wq, rs_wq, (u32)(t * 8192));
  const u32 vo_wuk = (u32)((64 * j + 16 * sw + idx) * 8192 + h * 256 + g * 16);
  const u32 vo_f1 = (u32)((64 * j + 16 * sw + 4 * g) * 3584 + ftile * 256 + idx * 16);
  const u32 kvrow0 = (u32)((ab * 4096 + as * 128 + 32 * sw) * 1024);
  u32 dvo[8];
#pragma unroll
  for (int i = 0; i < 8; ++i) { dvo[i] = (u32)((l ^ (2 * i)) * 16); PIN(dvo[i]); }
#define KV(i) dma16_kv(lbase + L_R + (u32)((32 * sw + (i)) * 1024), dvo[(i) & 7], rs_kv, kvrow0 + (u32)((i) * 1024))
  const int q_ = idx >> 2, p_ = idx & 3, jp = idx & 7;
  u32 a_xn0 = L_XN + (u32)((idx >> 3) * 1024 + (64 * wb + 16 * g) * 2);
  u32 a_fx = L_XN + (u32)((idx >> 3) * 1024 + (64 * j + 16 * sw + 4 * g) * 2);
  u32 sel_lo[4], sel_hi[4];
#pragma unroll
  for (int i = 0; i < 4; ++i) {
    const bool on = (i == (jp >> 1));
    sel_lo[i] = on ? ((jp & 1) ? 0x01000c0cu : 0x0c0c0100u) : 0x0c0c0c0cu;
    sel_hi[i] = on ? ((jp & 1) ? 0x03020c0cu : 0x0c0c0302u) : 0x0c0c0c0cu;
    PIN(sel_lo[i]); PIN(sel_hi[i]);
  }
  u32 a_qpw = L_U + (u32)(((wb * 4 + (g >> 1)) * 128 + 8 * idx + 4 * (g & 1)) * 4);
  u32 a_qpr = L_U + (u32)((tid & 255) * 8);
  u32 a_qbw = L_QB + (u32)((tid & 255) * 4);
  u32 a_qbr = L_QB + (u32)(((idx & 3) * 128 + 8 * g) * 2);
  u32 o_qa = QA_OFF + (u32)((((idx & 3) * 32 + h) * 256 + (64 * j + 16 * sw + 4 * g) / 2) * 8);
  PIN(a_xn0); PIN(a_fx); PIN(a_qpw); PIN(a_qpr); PIN(a_qbw); PIN(a_qbr); PIN(o_qa);

  STAMP(1);
  if (chain) {
    vmwait<4>();
    PIN(xv);
    float v[8];
    v[0] = bf2f_lo(xv.x); v[1] = bf2f_hi(xv.x); v[2] = bf2f_lo(xv.y); v[3] = bf2f_hi(xv.y);
    v[4] = bf2f_lo(xv.z); v[5] = bf2f_hi(xv.z); v[6] = bf2f_lo(xv.w); v[7] = bf2f_hi(xv.w);
    float s = ((v[0] + v[1]) + (v[2] + v[3])) + ((v[4] + v[5]) + (v[6] + v[7]));
    s = wave_sum(s);
    const float mean = s * (1.f / 512.f);
    float ss = 0.f;
#pragma unroll
    for (int i = 0; i < 8; ++i) { const float d = v[i] - mean; ss = fmaf(d, d, ss); }
    ss = wave_sum(ss);
    const float var = rbf(ss * (1.f / 512.f));
    const float mb = rbf(mean);
    const float den = rbf(__builtin_amdgcn_sqrtf(rbf(var + 1e-5f)));
    const float rd = __builtin_amdgcn_rcpf(den);
    u32x4 o;
    o.x = pack2(rbf(v[0] - mb) * rd, rbf(v[1] - mb) * rd);
    o.y = pack2(rbf(v[2] - mb) * rd, rbf(v[3] - mb) * rd);
    o.z = pack2(rbf(v[4] - mb) * rd, rbf(v[5] - mb) * rd);
    o.w = pack2(rbf(v[6] - mb) * rd, rbf(v[7] - mb) * rd);
    *(LAS u32x4*)(L + L_XN + w * 1024 + l * 16) = o;
    asm volatile("s_waitcnt lgkmcnt(0)" ::: "memory");
    if (l == 0) __hip_atomic_fetch_add((LAS u32*)(L + L_XF), 1u, __ATOMIC_RELAXED, __HIP_MEMORY_SCOPE_WORKGROUP);
  }
#pragma unroll
  for (int t = 4; t < 16; ++t) ld128_wq(wqf[t], vo_wq, rs_wq, (u32)(t * 8192));
#pragma unroll
  for (int t = 0; t < 4; ++t) { if (chain) ld128(aux[t], vo_wuk, rs_wuk, (u32)(t * 64)); else ld128_w(aux[t], vo_f1, rs_f1, (u32)(t * 3584)); }
  if (!chain) { KV(0); KV(1); KV(2); KV(3); }
  STAMP(2);

  u32x2 fx[2];
  u32 epoch;
  {
    asm volatile("s_load_dword %0, %1, 0x0\n\ts_waitcnt lgkmcnt(0)" : "=s"(epoch) : "s"(ws) : "memory");
    epoch += 1u;
    while (__hip_atomic_load((LAS u32*)(L + L_XF), __ATOMIC_RELAXED, __HIP_MEMORY_SCOPE_WORKGROUP) < 4u) __builtin_amdgcn_s_sleep(1);
    asm volatile("" ::: "memory");
    u32x4 xp[2][2];
    xp[0][0] = lds128(L, a_xn0); xp[0][1] = lds128(L, a_xn0 + 16); xp[1][0] = lds128(L, a_xn0 + 2048); xp[1][1] = lds128(L, a_xn0 + 2064);
    fx[0] = *(LAS u32x2*)(L + a_fx); fx[1] = *(LAS u32x2*)(L + a_fx + 2048);
    f32x4 qa0 = {0.f, 0.f, 0.f, 0.f}, qa1 = {0.f, 0.f, 0.f, 0.f};
#define DIAG_A(p, sel) u32x4{__builtin_amdgcn_perm(p, p, sel[0]), __builtin_amdgcn_perm(p, p, sel[1]), __builtin_amdgcn_perm(p, p, sel[2]), __builtin_amdgcn_perm(p, p, sel[3])}
#define QSTEP(t) do { \
      const u32 p0 = xp[0][(t) >> 3][((t) >> 1) & 3], p1 = xp[1][(t) >> 3][((t) >> 1) & 3]; \
      const u32* sel = ((t) & 1) ? sel_hi : sel_lo; \
      const u32x4 a0 = DIAG_A(p0, sel), a1 = DIAG_A(p1, sel); \
      vmwait<19 - (t)>(); PIN(wqf[t]); \
      qa0 = mfma32(a0, wqf[t], qa0); qa1 = mfma32(a1, wqf[t], qa1); } while (0)
    QSTEP(0); QSTEP(1); QSTEP(2); QSTEP(3); QSTEP(4); QSTEP(5); QSTEP(6); QSTEP(7);
    QSTEP(8); QSTEP(9); QSTEP(10); QSTEP(11); QSTEP(12); QSTEP(13); QSTEP(14); QSTEP(15);
    *(LAS f32x4*)(L + a_qpw) = qa0;
    *(LAS f32x4*)(L + a_qpw + 1024) = qa1;
  }
  if (chain) VMWAIT(0); else vmwait<4>();
  PIN(aux[0]); PIN(aux[1]); PIN(aux[2]); PIN(aux[3]);
  STAMP(3);
  BAR();
  if (chain) {
    u32x2 pv[8];
#pragma unroll
    for (int ww = 0; ww < 8; ++ww) pv[ww] = *(LAS u32x2*)(L + a_qpr + ww * 2048);
    float s0, s1;
    s0 = ((__uint_as_float(pv[0].x) + __uint_as_float(pv[1].x)) + (__uint_as_float(pv[2].x) + __uint_as_float(pv[3].x))) + ((__uint_as_float(pv[4].x) + __uint_as_float(pv[5].x)) + (__uint_as_float(pv[6].x) + __uint_as_float(pv[7].x)));
    s1 = ((__uint_as_float(pv[0].y) + __uint_as_float(pv[1].y)) + (__uint_as_float(pv[2].y) + __uint_as_float(pv[3].y))) + ((__uint_as_float(pv[4].y) + __uint_as_float(pv[5].y)) + (__uint_as_float(pv[6].y) + __uint_as_float(pv[7].y)));
    *(LAS u32*)(L + a_qbw) = pack2(s0, s1);
  } else {
    KV(4); KV(5); KV(6); KV(7);
    f32x4 f0a = {0.f, 0.f, 0.f, 0.f}, f1a = {0.f, 0.f, 0.f, 0.f};
#pragma unroll
    for (int t = 0; t < 4; ++t) {
      const u32 p0 = (t >> 1) ? fx[0].y : fx[0].x, p1 = (t >> 1) ? fx[1].y : fx[1].x;
      const u32* sel = (t & 1) ? sel_hi : sel_lo;
      f0a = mfma32(DIAG_A(p0, sel), aux[t], f0a); f1a = mfma32(DIAG_A(p1, sel), aux[t], f1a);
    }
    const u32 a_fpw = L_FP + (u32)(((sw * 4 + (g >> 1)) * 128 + 8 * idx + 4 * (g & 1)) * 4);
    *(LAS f32x4*)(L + a_fpw) = f0a;
    *(LAS f32x4*)(L + a_fpw + 1024) = f1a;
  }
  BAR();
  bool fail = false;
  u32 a_qir[4], a_kvr[4], trb[4], trb2[4];
  u32 a_smw, a_smr, a_pw, a_pr, o_e3w, bp_src, o_stw;
#define LATE_ADDR() do { \
    _Pragma("unroll") for (int c = 0; c < 4; ++c) { const u32 uu = (u32)(4 * c + g); \
      a_qir[c] = L_U + uu * 256 + (((u32)idx ^ uu) * 16); \
      a_kvr[c] = L_R + (u32)((16 * w + idx) * 1024) + ((uu ^ (u32)(2 * (idx & 7))) * 16); \
      const u32 pos = (u32)((8 * w + 2 * c + (p_ >> 1)) ^ (8 * (g & 1) + 2 * q_)); \
      trb[c] = L_R + (u32)((4 * g + q_) * 1024) + pos * 16 + (u32)(8 * (p_ & 1)); trb2[c] = trb[c] + 65536u; \
      PIN(a_qir[c]); PIN(a_kvr[c]); PIN(trb[c]); PIN(trb2[c]); } \
    a_smw = L_SM + (u32)((w * 16 + idx) * 4); a_smr = L_SM + (u32)(idx * 4); \
    a_pw = L_P + (u32)((((w >> 1) * 4 + g) * 16 + idx) * 16 + 8 * (w & 1)); a_pr = L_P + (u32)(l * 16); \
    o_e3w = E3_OFF + (u32)(((((16 * ag + (l >> 2)) * 8 + w) * 4 + ab) * 32 + as) * 128 + (l & 3) * 16); \
    bp_src = (u32)(((l >> 2) + 16 * (l & 3)) * 4); \
    o_stw = ST_OFF + (u32)((((16 * ag + idx) * 4 + ab) * 32 + as) * 8); \
    PIN(a_smw); PIN(a_smr); PIN(a_pw); PIN(a_pr); PIN(o_e3w); PIN(bp_src); PIN(o_stw); } while (0)
  if (chain) {
    u32x4 bq[4];
#pragma unroll
    for (int kk = 0; kk < 4; ++kk) bq[kk] = lds128(L, a_qbr + kk * 64);
    f32x4 acc = {0.f, 0.f, 0.f, 0.f}, acc2 = {0.f, 0.f, 0.f, 0.f};
    acc = mfma32(aux[0], bq[0], acc); acc2 = mfma32(aux[1], bq[1], acc2);
    acc = mfma32(aux[2], bq[2], acc); acc2 = mfma32(aux[3], bq[3], acc2);
    constexpr float SC = 0.08838834764831845f * 1.4426950408889634f;
    u32x4 o; o.x = pack2((acc[0] + acc2[0]) * SC, (acc[1] + acc2[1]) * SC); o.y = epoch; o.z = pack2((acc[2] + acc2[2]) * SC, (acc[3] + acc2[3]) * SC); o.w = epoch;
    if (idx < 4) st128_sc1(o, o_qa, rs_ws, 0u);
    STAMP(4);
    {
      const int t = tid;
      const u32 o_efw = EF_OFF + (u32)((2 * ftile + ((t & 63) >> 5)) * 32768 + ((fmat * 8 + j) * 4 + (t >> 6)) * 512 + (t & 31) * 16);
      u32x2 pv[4];
#pragma unroll
      for (int ww = 0; ww < 4; ++ww) pv[ww] = *(LAS u32x2*)(L + L_FP + t * 8 + ww * 2048);
      const float s0 = (__uint_as_float(pv[0].x) + __uint_as_float(pv[1].x)) + (__uint_as_float(pv[2].x) + __uint_as_float(pv[3].x));
      const float s1 = (__uint_as_float(pv[0].y) + __uint_as_float(pv[1].y)) + (__uint_as_float(pv[2].y) + __uint_as_float(pv[3].y));
      if (ffn_ok) st128_sc1(u32x4{__float_as_uint(s0), epoch, __float_as_uint(s1), epoch}, o_efw, rs_ws, 0u);
    }
    LATE_ADDR();
    const u32 o_e2 = QA_OFF + (u32)((ab * 32 + 16 * ag) * 2048 + w * 8192 + l * 16);
    u32 a_qiw[4];
#pragma unroll
    for (int k2 = 0; k2 < 4; ++k2) { const u32 uu = (u32)(l >> 1); a_qiw[k2] = L_U + uu * 256 + (((u32)(4 * w + k2) ^ (uu & 15)) * 16) + (u32)(8 * (l & 1)); PIN(a_qiw[k2]); }
    const u32 o_efr = EF_OFF + (u32)(uc * 32768 + (tid >> 7) * 16384 + (tid & 127) * 16);
    __builtin_amdgcn_s_sleep(E2_SLEEP);
    STAMP(13);
    u32x4 q4[8];
    for (u32 spin = 0;; ++spin) {
#pragma unroll
      for (int k = 0; k < 8; ++k) ld128_sc1(q4[k], o_e2, rs_ws, (u32)(k * 1024));
      VMWAIT(0);
#pragma unroll
      for (int k = 0; k < 8; ++k) PIN(q4[k]);
      if (__all(tags_ok(q4, 8, epoch))) break;
      if (spin > SPIN_LIMIT) { fail = true; break; }
    }
    STAMP(5);
#pragma unroll
    for (int k = 0; k < 8; ++k) {
      *(LAS u32*)(L + a_qiw[k >> 1] + (k & 1) * 8192) = q4[k].x;
      *(LAS u32*)(L + a_qiw[k >> 1] + (k & 1) * 8192 + 4) = q4[k].z;
    }
    for (u32 spin = 0; ffn_ok; ++spin) {
#pragma unroll
      for (int k = 0; k < 8; ++k) ld128_sc1(q4[k], o_efr, rs_ws, (u32)(k * 2048));
      VMWAIT(0);
#pragma unroll
      for (int k = 0; k < 8; ++k) PIN(q4[k]);
      if (__all(tags_ok(q4, 8, epoch))) break;
      if (spin > SPIN_LIMIT) { fail = true; break; }
    }
    float s0 = 0.f, s1 = 0.f;
#pragma unroll
    for (int k = 0; k < 8; ++k) { s0 += __uint_as_float(q4[k].x); s1 += __uint_as_float(q4[k].z); }
    *(LAS u32x2*)(L + L_HS + tid * 8) = u32x2{__float_as_uint(s0), __float_as_uint(s1)};
  } else {
    const u32x4 sv = {SENT, SENT, SENT, SENT};
    const u32 oc = E3_OFF + (u32)((h * 8 + j) * 16384 + (tid - 256) * 16);
    const u32 oc2 = E5S_OFF + (u32)((bid >> 1) * 16384 + (bid & 1) * 8192 + (tid - 256) * 16);
    KV(8); st128_sc1(sv, oc, rs_ws, 0u);
    KV(9); st128_sc1(sv, oc, rs_ws, 4096u);
    KV(10); st128_sc1(sv, oc, rs_ws, 8192u);
    KV(11); st128_sc1(sv, oc, rs_ws, 12288u);
    LATE_ADDR();
    vmwait<KS>(); KV(12); st128_sc1(sv, oc2, rs_ws, 0u);
    vmwait<KS>(); KV(13); st128_sc1(sv, oc2, rs_ws, 4096u);
    vmwait<KS>(); KV(14); vmwait<KS>(); KV(15); vmwait<KS>(); KV(16); vmwait<KS>(); KV(17);
    vmwait<KS>(); KV(18); vmwait<KS>(); KV(19); vmwait<KS>(); KV(20); vmwait<KS>(); KV(21);
    vmwait<KS>(); KV(22); vmwait<KS>(); KV(23); vmwait<KS>(); KV(24);
    vmwait<11>();
    {
      u32 old = 0;
      if (l == 0) old = __hip_atomic_fetch_add((LAS u32*)(L + L_CNT), 1u, __ATOMIC_RELAXED, __HIP_MEMORY_SCOPE_WORKGROUP);
      old = (u32)__builtin_amdgcn_readfirstlane(old);
      if (old == 3u && l == 0) st64_sc1(u32x2{epoch, epoch}, CF_OFF + (u32)(((h >> 4) * 128 + (h & 15) * 8 + j) * 8), rs_ws);
    }
    vmwait<KS>(); KV(25); vmwait<KS>(); KV(26); vmwait<KS>(); KV(27); vmwait<KS>(); KV(28);
    vmwait<KS>(); KV(29); vmwait<KS>(); KV(30); vmwait<KS>(); KV(31);
    VMWAIT(0);
  }
  BAR();
  u32x4 bq[16];
#pragma unroll
  for (int kk = 0; kk < 16; ++kk) bq[kk] = lds128(L, a_qir[kk & 3] + (kk >> 2) * 4096);
  if (w < 2) {
    const u32x2 g0 = *(LAS u32x2*)(L + L_HS + tid * 8), u0 = *(LAS u32x2*)(L + L_HS + 1024 + tid * 8);
    const float ga = rbf(__uint_as_float(g0.x)), gb = rbf(__uint_as_float(g0.y));
    const float ua = rbf(__uint_as_float(u0.x)), ub = rbf(__uint_as_float(u0.y));
    const float sa = rbf(ga * __builtin_amdgcn_rcpf(1.0f + __expf(-ga))), sb = rbf(gb * __builtin_amdgcn_rcpf(1.0f + __expf(-gb)));
    const int f = 2 * (tid & 31), b = tid >> 5;
    *(LAS u32*)(L + L_HF + (u32)(((((f >> 5) * 4 + ((f >> 2) & 3)) * 4 + b) * 8 + 4 * ((f >> 4) & 1) + (f & 3)) * 2)) = pack2(sa * ua, sb * ub);
  }
  f32x4 sc;
  {
    u32x4 ak[16];
#pragma unroll
    for (int kk = 0; kk < 16; ++kk) ak[kk] = lds128(L, a_kvr[kk & 3] + (kk >> 2) * 256);
    SCHED();
    f32x4 s0 = {0.f, 0.f, 0.f, 0.f}, s1 = {0.f, 0.f, 0.f, 0.f};
#pragma unroll
    for (int kk = 0; kk < 16; kk += 2) { s0 = mfma32(ak[kk], bq[kk], s0); s1 = mfma32(ak[kk + 1], bq[kk + 1], s1); }
    sc = s0 + s1;
    float m = fmaxf(fmaxf(sc[0], sc[1]), fmaxf(sc[2], sc[3]));
    m = x16_max(m); m = x32_max(m);
    if (g == 0) *(LAS float*)(L + a_smw) = m;
  }
  STAMP(6);
  BAR();
  u32x4 cf4;
  ld128_sc1(cf4, CF_OFF + (u32)(ag * 1024 + l * 16), rs_ws, 0u);
  float mall;
  {
    float mv[8];
#pragma unroll
    for (int ww = 0; ww < 8; ++ww) mv[ww] = *(LAS float*)(L + a_smr + ww * 64);
    const float m = fmaxf(fmaxf(fmaxf(mv[0], mv[1]), fmaxf(mv[2], mv[3])), fmaxf(fmaxf(mv[4], mv[5]), fmaxf(mv[6], mv[7])));
    mall = m;
    const float p0 = __builtin_amdgcn_exp2f(sc[0] - m), p1 = __builtin_amdgcn_exp2f(sc[1] - m), p2 = __builtin_amdgcn_exp2f(sc[2] - m), p3 = __builtin_amdgcn_exp2f(sc[3] - m);
    u32x2 pv; pv.x = pack2(p0, p1); pv.y = pack2(p2, p3);
    *(LAS u32x2*)(L + a_pw) = pv;
    float s = (p0 + p1) + (p2 + p3);
    s = x16_sum(s); s = x32_sum(s);
    if (g == 0) *(LAS float*)(L + a_smw + 512) = s;
  }
  for (u32 spin = 0;; ++spin) {
    VMWAIT(0);
    PIN(cf4);
    if (__all(cf4.x == epoch && cf4.y == epoch && cf4.z == epoch && cf4.w == epoch)) break;
    if (spin > SPIN_LIMIT) { fail = true; break; }
    ld128_sc1(cf4, CF_OFF + (u32)(ag * 1024 + l * 16), rs_ws, 0u);
  }
  BAR();
  {
#pragma unroll
    for (int i = 0; i < 2; ++i) { const int row = 8 * w + 4 * i + g; dma16(lbase + L_U + (u32)((8 * w + 4 * i) * 256), (u32)(row * 8192 + ((idx ^ (2 * (row & 7))) * 16)), rs_wuv, (u32)(64 * j * 8192 + h * 256)); }
    const u32 vo_wd = (u32)((l >> 3) * 1024 + (((l & 7) ^ (2 * ((l >> 4) & 3))) * 16));
    dma16(lbase + L_WDN + (u32)(w * 1024), vo_wd, rs_wd, (u32)((uc * 64 + 8 * w) * 1024 + j * 128));
  }
  {
    u32x4 pf4[4], bk[4][4];
    float sl[8];
#pragma unroll
    for (int ks = 0; ks < 4; ++ks) pf4[ks] = lds128(L, a_pr + ks * 1024);
#pragma unroll
    for (int ct = 0; ct < 4; ++ct) {
      bk[ct][0] = trfrag(L, trb[ct], 0, 16384); bk[ct][1] = trfrag(L, trb[ct], 32768, 49152);
      bk[ct][2] = trfrag(L, trb2[ct], 0, 16384); bk[ct][3] = trfrag(L, trb2[ct], 32768, 49152);
    }
#pragma unroll
    for (int ww = 0; ww < 8; ++ww) sl[ww] = *(LAS float*)(L + a_smr + 512 + ww * 64);
    SCHED();
    f32x4 o[4];
#pragma unroll
    for (int ct = 0; ct < 4; ++ct) o[ct] = f32x4{0.f, 0.f, 0.f, 0.f};
#pragma unroll
    for (int ks = 0; ks < 4; ++ks)
#pragma unroll
      for (int ct = 0; ct < 4; ++ct) o[ct] = mfma32(bk[ct][ks], pf4[ks], o[ct]);
    const float ls = ((sl[0] + sl[1]) + (sl[2] + sl[3])) + ((sl[4] + sl[5]) + (sl[6] + sl[7]));
    const float inv = __builtin_amdgcn_rcpf(ls);
    u32 pk[8];
#pragma unroll
    for (int ct = 0; ct < 4; ++ct) { pk[2 * ct] = pack2(o[ct][0] * inv, o[ct][1] * inv); pk[2 * ct + 1] = pack2(o[ct][2] * inv, o[ct][3] * inv); }
#pragma unroll
    for (int i = 0; i < 8; ++i) pk[i] = min((u32)__builtin_amdgcn_ds_bpermute((int)bp_src, (int)pk[i]), SENT - 1u);
    st128_sc1(u32x4{pk[0], pk[1], pk[2], pk[3]}, o_e3w, rs_ws, 0u);
    st128_sc1(u32x4{pk[4], pk[5], pk[6], pk[7]}, o_e3w, rs_ws, 64u);
    if (w == 0) {
      const float lse = mall + __builtin_amdgcn_logf(ls);
      u32x2 v; v.x = __float_as_uint(lse); v.y = epoch;
      if (g == 0) st64_sc1(v, o_stw, rs_ws);
    }
  }
  STAMP(7);
  BAR();
  {
    const int wr = (w + j) & 7;
#pragma unroll
    for (int i = 0; i < 8; ++i)
      dma16_wo(lbase + L_R + (u32)((8 * wr + i) * 1024), dvo[i], rs_wo, (u32)((h * 128 + 8 * wr + i) * 1024));
  }
  const int lb = w >> 1, lp4 = 4 * (w & 1) + (l & 3), lss = l >> 2;
  u32 o_e3r = E3_OFF + (u32)(((h * 8 + j) * 4 + lb) * 32 * 128 + lss * 128 + lp4 * 16);
  u32 o_str = ST_OFF + (u32)(((h * 4 + lb) * 32 + (l & 31)) * 8);
  u32 a_wuvr = L_U + (u32)((4 * g + q_) * 256) + (u32)(((2 * w + (p_ >> 1)) ^ (8 * (g & 1) + 2 * q_)) * 16) + (u32)(8 * (p_ & 1));
  u32 a_latr = L_LAT + (u32)((g * 4 + (idx & 3)) * 16);
  u32 a_ctxw = L_CTX + (u32)((((w >> 1) * 4 + g) * 4 + (idx & 3)) * 16 + 8 * (w & 1));
  u32 a_ctxr = L_CTX + (u32)((g * 4 + (idx & 3)) * 16);
  u32 o_e5s = E5S_OFF + (u32)(((16 * w + 4 * (idx >> 2) + g) * 256 + bid) * 64 + (idx & 3) * 16);
  u32 a_latw;
  {
    const int e = ((l >> 5) & 1) * 4 + ((l >> 4) & 1) * 2 + ((l >> 3) & 1), k = e >> 1;
    const int c = 16 * (2 * (lp4 >> 2) + (k >> 1)) + 4 * (lp4 & 3) + 2 * (k & 1) + (e & 1);
    a_latw = L_LAT + (u32)(((((c >> 5) * 4 + ((c >> 2) & 3)) * 4 + lb) * 8 + 4 * ((c >> 4) & 1) + (c & 3)) * 2);
  }
  PIN(o_e3r); PIN(o_str); PIN(a_wuvr); PIN(a_latr); PIN(a_ctxw); PIN(a_ctxr); PIN(o_e5s); PIN(a_latw);
  {
    const int wr = (w + j) & 7;
#pragma unroll
    for (int i = 0; i < 8; ++i)
      dma16_wo(lbase + L_R + (u32)((64 + 8 * wr + i) * 1024), dvo[i], rs_wo, (u32)((h * 128 + 64 + 8 * wr + i) * 1024));
  }
  STAMP(8);
  {
    u32x4 e4[2]; u32x2 st;
    for (u32 spin = 0;; ++spin) {
      ld64_sc1(st, o_str, rs_ws, 0u);
      ld128_sc1(e4[0], o_e3r, rs_ws, 0u); ld128_sc1(e4[1], o_e3r, rs_ws, 2048u);
      VMWAIT(0);
      PIN(e4[0]); PIN(e4[1]); PIN(st);
      const u32 mx = max(max(max(e4[0].x, e4[0].y), max(e4[0].z, e4[0].w)), max(max(e4[1].x, e4[1].y), max(e4[1].z, e4[1].w)));
      if (__all(mx != SENT && st.y == epoch)) break;
      if (spin > SPIN_LIMIT) { fail = true; break; }
    }
    STAMP(9);
    const float lse = __uint_as_float(st.x);
    const float M = half_max32(lse);
    const float e = __builtin_amdgcn_exp2f(lse - M);
    const float S = half_sum32(e);
    const float wgt = e * __builtin_amdgcn_rcpf(S);
    const float w0 = __builtin_bit_cast(float, __builtin_amdgcn_ds_bpermute(lss * 4, __builtin_bit_cast(int, wgt)));
    const float w1 = __builtin_bit_cast(float, __builtin_amdgcn_ds_bpermute((16 + lss) * 4, __builtin_bit_cast(int, wgt)));
    float v[8];
    v[0] = fmaf(w1, bf2f_lo(e4[1].x), w0 * bf2f_lo(e4[0].x)); v[1] = fmaf(w1, bf2f_hi(e4[1].x), w0 * bf2f_hi(e4[0].x));
    v[2] = fmaf(w1, bf2f_lo(e4[1].y), w0 * bf2f_lo(e4[0].y)); v[3] = fmaf(w1, bf2f_hi(e4[1].y), w0 * bf2f_hi(e4[0].y));
    v[4] = fmaf(w1, bf2f_lo(e4[1].z), w0 * bf2f_lo(e4[0].z)); v[5] = fmaf(w1, bf2f_hi(e4[1].z), w0 * bf2f_hi(e4[0].z));
    v[6] = fmaf(w1, bf2f_lo(e4[1].w), w0 * bf2f_lo(e4[0].w)); v[7] = fmaf(w1, bf2f_hi(e4[1].w), w0 * bf2f_hi(e4[0].w));
#pragma unroll
    for (int i = 0; i < 4; ++i) { auto s = __builtin_amdgcn_permlane32_swap(__float_as_uint(v[i]), __float_as_uint(v[i + 4]), false, false); v[i] = __uint_as_float(s[0]) + __uint_as_float(s[1]); }
#pragma unroll
    for (int i = 0; i < 2; ++i) { auto s = __builtin_amdgcn_permlane16_swap(__float_as_uint(v[i]), __float_as_uint(v[i + 2]), false, false); v[i] = __uint_as_float(s[0]) + __uint_as_float(s[1]); }
    { const bool b3 = (l & 8) != 0; const float snd = b3 ? v[0] : v[1], keep = b3 ? v[1] : v[0]; v[0] = keep + dpp<XOR8>(snd); }
    {
      int t1 = __builtin_amdgcn_update_dpp(0, __builtin_bit_cast(int, v[0]), 0x114, 0xf, 0xa, false);
      t1 = __builtin_amdgcn_update_dpp(t1, __builtin_bit_cast(int, v[0]), 0x104, 0xf, 0x5, false);
      v[0] += __builtin_bit_cast(float, t1);
    }
    *(LAS u16*)(L + a_latw) = (u16)pack2(v[0], v[0]);
  }
  BAR();
  u32x4 wk3[4][4];
  {
    const u32x4 wa0 = trfrag(L, a_wuvr, 0, 4096), wa1 = trfrag(L, a_wuvr, 8192, 12288);
    const u32x4 lb0 = lds128(L, a_latr), lb1 = lds128(L, a_latr + 256);
#pragma unroll
    for (int mt = 0; mt < 4; ++mt) {
      wk3[mt][0] = trfrag(L, trb[mt], 0, 16384); wk3[mt][1] = trfrag(L, trb[mt], 32768, 49152);
      wk3[mt][2] = trfrag(L, trb2[mt], 0, 16384); wk3[mt][3] = trfrag(L, trb2[mt], 32768, 49152);
    }
    f32x4 c = mfma32(wa0, lb0, f32x4{0.f, 0.f, 0.f, 0.f});
    c = mfma32(wa1, lb1, c);
    if (idx < 4) { u32x2 v; v.x = pack2(c[0], c[1]); v.y = pack2(c[2], c[3]); *(LAS u32x2*)(L + a_ctxw) = v; }
  }
  BAR();
  {
    u32x4 cf[4];
#pragma unroll
    for (int ks = 0; ks < 4; ++ks) cf[ks] = lds128(L, a_ctxr + ks * 256);
    f32x4 o[4];
#pragma unroll
    for (int mt = 0; mt < 4; ++mt) o[mt] = f32x4{0.f, 0.f, 0.f, 0.f};
#pragma unroll
    for (int ks = 0; ks < 4; ++ks)
#pragma unroll
      for (int mt = 0; mt < 4; ++mt) o[mt] = mfma32(wk3[mt][ks], cf[ks], o[mt]);
    const int rep = idx >> 2;
    f32x4 v = rep == 0 ? o[0] : rep == 1 ? o[1] : rep == 2 ? o[2] : o[3];
    u32x4 s0;
    s0.x = min(__float_as_uint(v[0]), SENT - 1u); s0.y = min(__float_as_uint(v[1]), SENT - 1u); s0.z = min(__float_as_uint(v[2]), SENT - 1u); s0.w = min(__float_as_uint(v[3]), SENT - 1u);
    st128_sc1(s0, o_e5s, rs_ws, 0u);
  }
  if (w < 4) {
    const int row = 4 * g + q_;
    const u32 a0 = L_WDN + (u32)(row * 128) + (u32)(((2 * w + (p_ >> 1)) ^ (2 * ((row >> 1) & 3))) * 16) + (u32)(8 * (p_ & 1));
    const u32x4 wa0 = trfrag(L, a0, 0, 2048), wa1 = trfrag(L, a0, 4096, 6144);
    const u32x4 hb0 = lds128(L, L_HF + (u32)((g * 4 + (idx & 3)) * 16)), hb1 = lds128(L, L_HF + 256 + (u32)((g * 4 + (idx & 3)) * 16));
    f32x4 y = mfma32(wa0, hb0, f32x4{0.f, 0.f, 0.f, 0.f});
    y = mfma32(wa1, hb1, y);
    const u32 off = E5Y_OFF + (u32)(((16 * j + 4 * w + g) * 32 + uc) * 128 + (idx & 3) * 32);
    if (ffn_ok && idx < 4) {
      st128_sc1(u32x4{__float_as_uint(y[0]), epoch, __float_as_uint(y[1]), epoch}, off, rs_ws, 0u);
      st128_sc1(u32x4{__float_as_uint(y[2]), epoch, __float_as_uint(y[3]), epoch}, off, rs_ws, 16u);
    }
  }
  STAMP(10);
  if (bid < 128) {
    u32x4 a4[2], y4;
    const u32 xfin = ((const u32*)x)[(l & 3) * 256 + bid * 2 + ((l >> 3) & 1)];
    const u32 o_r = E5S_OFF + (u32)(bid * 16384 + tid * 16);
    const u32 o_y = E5Y_OFF + (u32)(bid * 4096 + (tid & 255) * 16);
    const bool yv = tid < 224;
    for (u32 spin = 0;; ++spin) {
      ld128_sc1(y4, o_y, rs_ws, 0u);
      ld128_sc1(a4[0], o_r, rs_ws, 0u); ld128_sc1(a4[1], o_r, rs_ws, 8192u);
      VMWAIT(0);
      PIN(a4[0]); PIN(a4[1]); PIN(y4);
      const u32 mx = max(max(max(a4[0].x, a4[0].y), max(a4[0].z, a4[0].w)), max(max(a4[1].x, a4[1].y), max(a4[1].z, a4[1].w)));
      if (__all(mx != SENT && (!yv || (y4.y == epoch && y4.w == epoch)))) break;
      if (spin > SPIN_LIMIT) { fail = true; break; }
    }
    STAMP(11);
    float v[4];
    v[0] = __uint_as_float(a4[0].x) + __uint_as_float(a4[1].x); v[1] = __uint_as_float(a4[0].y) + __uint_as_float(a4[1].y);
    v[2] = __uint_as_float(a4[0].z) + __uint_as_float(a4[1].z); v[3] = __uint_as_float(a4[0].w) + __uint_as_float(a4[1].w);
#pragma unroll
    for (int i = 0; i < 2; ++i) { auto s = __builtin_amdgcn_permlane32_swap(__float_as_uint(v[i]), __float_as_uint(v[i + 2]), false, false); v[i] = __uint_as_float(s[0]) + __uint_as_float(s[1]); }
    { auto s = __builtin_amdgcn_permlane16_swap(__float_as_uint(v[0]), __float_as_uint(v[1]), false, false); v[0] = __uint_as_float(s[0]) + __uint_as_float(s[1]); }
    v[0] += dpp<0x124>(v[0]); v[0] += dpp<0x128>(v[0]);
    float y0 = yv ? __uint_as_float(y4.x) : 0.f, y1 = yv ? __uint_as_float(y4.z) : 0.f;
    y0 += dpp<XOR8>(y0); y1 += dpp<XOR8>(y1);
    y0 = x16_sum(y0); y1 = x16_sum(y1); y0 = x32_sum(y0); y1 = x32_sum(y1);
    if ((l & 12) == 0) *(LAS float*)(L + L_FIN + (u32)((w * 16 + (l >> 4) * 4 + (l & 3)) * 4)) = v[0];
    if (l < 8 && w < 4) *(LAS u32x2*)(L + L_HS + (u32)((w * 8 + l) * 8)) = u32x2{__float_as_uint(y0), __float_as_uint(y1)};
    BAR();
    if (w == 0) {
      const int mi = (l >> 2) & 3, b = l & 3;
      float sv[8], yv4[4];
#pragma unroll
      for (int ww = 0; ww < 8; ++ww) sv[ww] = *(LAS float*)(L + L_FIN + (u32)((ww * 16 + mi * 4 + b) * 4));
#pragma unroll
      for (int ww = 0; ww < 4; ++ww) yv4[ww] = *(LAS float*)(L + L_HS + (u32)(((ww * 8 + b * 2 + (mi >> 1)) * 2 + (mi & 1)) * 4));
      const float s = ((sv[0] + sv[1]) + (sv[2] + sv[3])) + ((sv[4] + sv[5]) + (sv[6] + sv[7]));
      const float y = (yv4[0] + yv4[1]) + (yv4[2] + yv4[3]);
      const float sb = rbf(s), yb = rbf(y);
      const float gg = rbf(yb * __builtin_amdgcn_rcpf(1.0f + __expf(-yb)));
      const float xv_ = (mi & 1) ? bf2f_hi(xfin) : bf2f_lo(xfin);
      float o = xv_ + rbf(sb * gg);
      if (fail) o = __uint_as_float(0x7fc00000u);
      if (l < 16) out[b * 512 + 4 * bid + mi] = (u16)(pack2(o, 0.f));
      if (bid == 0 && l == 0) st32_sc1(epoch, EPOCH_OFF, rs_ws);
    }
  }
}

extern "C" void kernel_launch(void* const* d_in, const int* in_sizes, int n_in, void* d_out, int out_size, void* d_ws, size_t ws_size, hipStream_t stream) {
  const u16* x = (const u16*)d_in[0]; const u16* ckv = (const u16*)d_in[1]; const u16* wq = (const u16*)d_in[2]; const u16* wuk = (const u16*)d_in[3];
  const u16* wuv = (const u16*)d_in[4]; const u16* wo = (const u16*)d_in[5]; const u16* wg = (const u16*)d_in[7]; const u16* wu = (const u16*)d_in[8]; const u16* wd = (const u16*)d_in[9];
  fused_decode<<<256, 512, 0, stream>>>(x, ckv, wq, wuk, wuv, wo, wg, wu, wd, (u16*)d_out, (unsigned char*)d_ws);
}
```

```cpp
#include <hip/hip_runtime.h>
#include <stdint.h>

typedef unsigned u32;
typedef unsigned short u16;
typedef u32 u32x4 __attribute__((ext_vector_type(4)));
typedef u32 u32x2 __attribute__((ext_vector_type(2)));
typedef float f32x4 __attribute__((ext_vector_type(4)));
typedef short s16x4 __attribute__((ext_vector_type(4)));
typedef __bf16 bf16x8 __attribute__((ext_vector_type(8)));
typedef __bf16 bf16x2 __attribute__((ext_vector_type(2)));
#define LAS __attribute__((address_space(3)))
typedef __amdgpu_buffer_rsrc_t rsrc_t;

#define DIAG 1
#define PRINT_STAMPS 1
#include <stdio.h>
#define NT_W 1

constexpr u32 EPOCH_OFF = 0;
constexpr u32 QA_OFF = 4096;
constexpr u32 ST_OFF = QA_OFF + 4 * 32 * 2048;
constexpr u32 CF_OFF = ST_OFF + 32 * 4 * 32 * 8;
constexpr u32 EF_OFF = CF_OFF + 4096;
constexpr u32 E5Y_OFF = EF_OFF + 32 * 32768;
constexpr u32 E3_OFF = E5Y_OFF + 128 * 4096;
constexpr u32 E5S_OFF = E3_OFF + 256 * 16384;
constexpr u32 WS_END = E5S_OFF + 128 * 16384;
constexpr u32 DBG_OFF = WS_END;
constexpr u32 WS_TOTAL = DBG_OFF + 256 * 64 * 8;
constexpr u32 SENT = 0xFFFFFFFFu;

constexpr u32 L_R = 0;
constexpr u32 L_U = 131072;
constexpr u32 L_WDN = L_U + 16384;
constexpr u32 L_M = 155648;
constexpr u32 L_XN = L_M;
constexpr u32 L_P = L_M;
constexpr u32 L_QB = L_M + 4096;
constexpr u32 L_CTX = L_QB;
constexpr u32 L_SM = L_M + 5120;
constexpr u32 L_SL = L_M + 5632;
constexpr u32 L_LAT = L_M + 6144;
constexpr u32 L_HF = L_M + 6656;
constexpr u32 L_FIN = L_M + 7168;
constexpr u32 L_HS = L_M;
constexpr u32 L_FP = L_WDN;
constexpr u32 L_CNT = L_M + 7680;
constexpr u32 L_XF = L_M + 7696;
constexpr u32 LDS_BYTES = 163840;

constexpr u32 SPIN_LIMIT = 1u << 16;
#define KD 6
constexpr int PD = 8;
#define KS 11
#define E2_SLEEP 8

__device__ __forceinline__ float bf2f_lo(u32 v) { return __uint_as_float(v << 16); }
__device__ __forceinline__ float bf2f_hi(u32 v) { return __uint_as_float(v & 0xffff0000u); }
__device__ __forceinline__ u32 pack2(float lo, float hi) { bf16x2 v = {(__bf16)lo, (__bf16)hi}; return __builtin_bit_cast(u32, v); }
__device__ __forceinline__ float rbf(float f) { return __uint_as_float(((u32)__builtin_bit_cast(u16, (__bf16)f)) << 16); }
__device__ __forceinline__ f32x4 mfma32(u32x4 a, u32x4 b, f32x4 c) {
  return __builtin_amdgcn_mfma_f32_16x16x32_bf16(__builtin_bit_cast(bf16x8, a), __builtin_bit_cast(bf16x8, b), c, 0, 0, 0);
}
__device__ __forceinline__ f32x4 mfma16(u32x2 a, u32x2 b, f32x4 c) {
  return __builtin_amdgcn_mfma_f32_16x16x16bf16_1k(__builtin_bit_cast(s16x4, a), __builtin_bit_cast(s16x4, b), c, 0, 0, 0);
}
template <int CTRL> __device__ __forceinline__ float dpp(float x) {
  return __builtin_bit_cast(float, __builtin_amdgcn_mov_dpp(__builtin_bit_cast(int, x), CTRL, 0xf, 0xf, true));
}
constexpr int XOR1 = 0xB1, XOR2 = 0x4E, XOR7 = 0x141, XOR15 = 0x140, XOR8 = 0x128;
__device__ __forceinline__ float x16_sum(float x) { auto s = __builtin_amdgcn_permlane16_swap(__float_as_uint(x), __float_as_uint(x), false, false); return __uint_as_float(s[0]) + __uint_as_float(s[1]); }
__device__ __forceinline__ float x32_sum(float x) { auto s = __builtin_amdgcn_permlane32_swap(__float_as_uint(x), __float_as_uint(x), false, false); return __uint_as_float(s[0]) + __uint_as_float(s[1]); }
__device__ __forceinline__ float x16_max(float x) { auto s = __builtin_amdgcn_permlane16_swap(__float_as_uint(x), __float_as_uint(x), false, false); return fmaxf(__uint_as_float(s[0]), __uint_as_float(s[1])); }
__device__ __forceinline__ float x32_max(float x) { auto s = __builtin_amdgcn_permlane32_swap(__float_as_uint(x), __float_as_uint(x), false, false); return fmaxf(__uint_as_float(s[0]), __uint_as_float(s[1])); }
__device__ __forceinline__ float wave_sum(float v) {
  v += dpp<XOR1>(v); v += dpp<XOR2>(v); v += dpp<XOR7>(v); v += dpp<XOR15>(v); v = x16_sum(v); v = x32_sum(v); return v;
}
__device__ __forceinline__ float half_sum32(float v) {
  v += dpp<XOR1>(v); v += dpp<XOR2>(v); v += dpp<XOR7>(v); v += dpp<XOR15>(v); v = x16_sum(v); return v;
}
__device__ __forceinline__ float half_max32(float v) {
  v = fmaxf(v, dpp<XOR1>(v)); v = fmaxf(v, dpp<XOR2>(v)); v = fmaxf(v, dpp<XOR7>(v)); v = fmaxf(v, dpp<XOR15>(v)); v = x16_max(v); return v;
}

#define BAR() asm volatile("s_waitcnt lgkmcnt(0)\n\ts_barrier" ::: "memory")
#define RAWBAR() asm volatile("s_barrier" ::: "memory")
#define VMWAIT(n) asm volatile("s_waitcnt vmcnt(" #n ")" ::: "memory")
#define SCHED() __builtin_amdgcn_sched_barrier(0)
template <int N> __device__ __forceinline__ void vmwait() { asm volatile("s_waitcnt vmcnt(%0)" :: "i"(N) : "memory"); }
#define PIN(a) asm volatile("" : "+v"(a))

#define NTS " nt"
__device__ __forceinline__ void ld128(u32x4& d, u32 voff, rsrc_t rs, u32 soff) {
  asm volatile("buffer_load_dwordx4 %0, %1, %2, %3 offen" : "=v"(d) : "v"(voff), "s"(rs), "s"(soff) : "memory");
}
__device__ __forceinline__ void ld128_w(u32x4& d, u32 voff, rsrc_t rs, u32 soff) {
  asm volatile("buffer_load_dwordx4 %0, %1, %2, %3 offen" NTS : "=v"(d) : "v"(voff), "s"(rs), "s"(soff) : "memory");
}
__device__ __forceinline__ void ld128_sc1(u32x4& d, u32 voff, rsrc_t rs, u32 soff) {
  asm volatile("buffer_load_dwordx4 %0, %1, %2, %3 offen sc1" : "=v"(d) : "v"(voff), "s"(rs), "s"(soff) : "memory");
}
__device__ __forceinline__ void ld64_sc1(u32x2& d, u32 voff, rsrc_t rs, u32 soff) {
  asm volatile("buffer_load_dwordx2 %0, %1, %2, %3 offen sc1" : "=v"(d) : "v"(voff), "s"(rs), "s"(soff) : "memory");
}
__device__ __forceinline__ void ld32_sc1(u32& d, u32 voff, rsrc_t rs, u32 soff) {
  asm volatile("buffer_load_dword %0, %1, %2, %3 offen sc1" : "=v"(d) : "v"(voff), "s"(rs), "s"(soff) : "memory");
}
__device__ __forceinline__ void ld32(u32& d, u32 voff, rsrc_t rs, u32 soff) {
  asm volatile("buffer_load_dword %0, %1, %2, %3 offen" : "=v"(d) : "v"(voff), "s"(rs), "s"(soff) : "memory");
}
__device__ __forceinline__ void st128_sc1(u32x4 d, u32 voff, rsrc_t rs, u32 soff) {
  asm volatile("buffer_store_dwordx4 %0, %1, %2, %3 offen sc1\n\ts_nop 1" :: "v"(d), "v"(voff), "s"(rs), "s"(soff) : "memory");
}
__device__ __forceinline__ void st64_sc1(u32x2 d, u32 voff, rsrc_t rs) {
  asm volatile("buffer_store_dwordx2 %0, %1, %2, 0 offen sc1\n\ts_nop 1" :: "v"(d), "v"(voff), "s"(rs) : "memory");
}
__device__ __forceinline__ void st32_sc1(u32 d, u32 voff, rsrc_t rs) {
  asm volatile("buffer_store_dword %0, %1, %2, 0 offen sc1\n\ts_nop 1" :: "v"(d), "v"(voff), "s"(rs) : "memory");
}
__device__ __forceinline__ void dma16(u32 lds_dst, u32 voff, rsrc_t rs, u32 soff) {
  asm volatile("s_mov_b32 m0, %0\n\ts_nop 0\n\tbuffer_load_dwordx4 %1, %2, %3 offen" NTS " lds" :: "s"(lds_dst), "v"(voff), "s"(rs), "s"(soff) : "memory");
}
__device__ __forceinline__ void dma16_def(u32 lds_dst, u32 voff, rsrc_t rs, u32 soff) {
  asm volatile("s_mov_b32 m0, %0\n\ts_nop 0\n\tbuffer_load_dwordx4 %1, %2, %3 offen lds" :: "s"(lds_dst), "v"(voff), "s"(rs), "s"(soff) : "memory");
}
#define KV_NT 1
#define KV_STAG 0
#define WO_NT 1
#define WQ_NT 1
#define dma16_kv dma16
#define dma16_wo dma16
#define ld128_wq ld128_w
__device__ __forceinline__ rsrc_t mk(const void* p, u32 bytes) { return __builtin_amdgcn_make_buffer_rsrc((void*)p, 0, bytes, 0x00020000); }
__device__ __forceinline__ u32x4 lds128(LAS unsigned char* L, u32 a) { return *(LAS u32x4*)(L + a); }
__device__ __forceinline__ u32x4 trfrag(LAS unsigned char* L, u32 a, u32 o0, u32 o1) {
  const s16x4 t0 = __builtin_amdgcn_ds_read_tr16_b64_v4i16((LAS s16x4*)(L + a + o0));
  const s16x4 t1 = __builtin_amdgcn_ds_read_tr16_b64_v4i16((LAS s16x4*)(L + a + o1));
  const u32x2 x = __builtin_bit_cast(u32x2, t0), y = __builtin_bit_cast(u32x2, t1);
  return u32x4{x.x, x.y, y.x, y.y};
}
__device__ __forceinline__ bool tags_ok(const u32x4* v, int n, u32 epoch) {
  u32 a = v[0].y & v[0].w, o = v[0].y | v[0].w;
#pragma unroll
  for (int k = 1; k < n; ++k) { a &= v[k].y & v[k].w; o |= v[k].y | v[k].w; }
  return a == epoch && o == epoch;
}

#define STAMP(i) do { unsigned long long t_; asm volatile("s_memtime %0\n\ts_waitcnt lgkmcnt(0)" : "=s"(t_) :: "memory"); stamps[i] = t_; } while (0)

__global__ void __launch_bounds__(512, 2)
fused_decode(const u16* __restrict__ x, const u16* __restrict__ ckv, const u16* __restrict__ wq, const u16* __restrict__ wuk,
             const u16* __restrict__ wuv, const u16* __restrict__ wo, const u16* __restrict__ wg, const u16* __restrict__ wu,
             const u16* __restrict__ wd, u16* __restrict__ out, unsigned char* __restrict__ ws) {
  __shared__ __attribute__((aligned(16))) unsigned char lds[LDS_BYTES];
  LAS unsigned char* L = (LAS unsigned char*)lds;
  const u32 lbase = (u32)(size_t)L;
  const int tid = threadIdx.x;
  const int w = __builtin_amdgcn_readfirstlane(tid >> 6);
  const int l = tid & 63, g = l >> 4, idx = l & 15;
  const int bid = blockIdx.x, xc = bid & 7, r = bid >> 3;
  const int h = xc + 8 * (r >> 3), j = r & 7;
  const int P = xc * 16 + (r & 15), ab = P >> 5, as = P & 31, ag = 1 - (r >> 4);
  const bool ffn_ok = h < 28;
  const int uc = ffn_ok ? h : 27;
  const int fmat = uc & 1, ftile = uc >> 1;
  unsigned long long stamps[24];
#pragma unroll
  for (int i = 0; i < 24; ++i) stamps[i] = 0;
  unsigned long long clk0 = __builtin_amdgcn_s_memrealtime();
  STAMP(0);

  const rsrc_t rs_x = mk(x, 4096), rs_kv = mk(ckv, 4u * 4096 * 1024), rs_wq = mk(wq, 4u << 20), rs_wuk = mk(wuk, 4u << 20),
               rs_wuv = mk(wuv, 4u << 20), rs_wo = mk(wo, 4u << 20), rs_wg = mk(wg, 512 * 1792 * 2), rs_wu = mk(wu, 512 * 1792 * 2),
               rs_wd = mk(wd, 512 * 1792 * 2), rs_ws = mk(ws, WS_TOTAL);

  const bool chain = w < 4;
  const int sw = w & 3;
  u32x4 xv; u32x4 wqf[16]; u32x4 aux[4];
  const int wb = (w + j) & 7;
  const u32 vo_wq = (u32)((64 * wb + 16 * g) * 8192 + h * 256 + idx * 16);
  const rsrc_t rs_f1 = fmat ? rs_wu : rs_wg;
  if (chain) ld128(xv, (u32)(w * 1024 + l * 16), rs_x, 0u);
  if (tid == 0) { *(LAS u32*)(L + L_CNT) = 0u; *(LAS u32*)(L + L_XF) = 0u; }
  BAR();
#pragma unroll
  for (int t = 0; t < 4; ++t) ld128_wq(wqf[t], vo_wq, rs_wq, (u32)(t * 8192));
  const u32 vo_wuk = (u32)((64 * j + 16 * sw + idx) * 8192 + h * 256 + g * 16);
  const u32 vo_f1 = (u32)((64 * j + 16 * sw + 4 * g) * 3584 + ftile * 256 + idx * 16);
  const u32 kvrow0 = (u32)((ab * 4096 + as * 128 + 32 * sw) * 1024);
  u32 dvo[8];
#pragma unroll
  for (int i = 0; i < 8; ++i) { dvo[i] = (u32)((l ^ (2 * i)) * 16); PIN(dvo[i]); }
#define KV(i) dma16_kv(lbase + L_R + (u32)((32 * sw + (i)) * 1024), dvo[(i) & 7], rs_kv, kvrow0 + (u32)((i) * 1024))
  const int q_ = idx >> 2, p_ = idx & 3, jp = idx & 7;
  u32 a_xn0 = L_XN + (u32)((idx >> 3) * 1024 + (64 * wb + 16 * g) * 2);
  u32 a_fx = L_XN + (u32)((idx >> 3) * 1024 + (64 * j + 16 * sw + 4 * g) * 2);
  u32 sel_lo[4], sel_hi[4];
#pragma unroll
  for (int i = 0; i < 4; ++i) {
    const bool on = (i == (jp >> 1));
    sel_lo[i] = on ? ((jp & 1) ? 0x01000c0cu : 0x0c0c0100u) : 0x0c0c0c0cu;
    sel_hi[i] = on ? ((jp & 1) ? 0x03020c0cu : 0x0c0c0302u) : 0x0c0c0c0cu;
    PIN(sel_lo[i]); PIN(sel_hi[i]);
  }
  u32 a_qpw = L_U + (u32)(((wb * 4 + (g >> 1)) * 128 + 8 * idx + 4 * (g & 1)) * 4);
  u32 a_qpr = L_U + (u32)((tid & 255) * 8);
  u32 a_qbw = L_QB + (u32)((tid & 255) * 4);
  u32 a_qbr = L_QB + (u32)(((idx & 3) * 128 + 8 * g) * 2);
  u32 o_qa = QA_OFF + (u32)((((idx & 3) * 32 + h) * 256 + (64 * j + 16 * sw + 4 * g) / 2) * 8);
  PIN(a_xn0); PIN(a_fx); PIN(a_qpw); PIN(a_qpr); PIN(a_qbw); PIN(a_qbr); PIN(o_qa);

  STAMP(1);
  if (chain) {
    vmwait<4>();
    PIN(xv);
    float v[8];
    v[0] = bf2f_lo(xv.x); v[1] = bf2f_hi(xv.x); v[2] = bf2f_lo(xv.y); v[3] = bf2f_hi(xv.y);
    v[4] = bf2f_lo(xv.z); v[5] = bf2f_hi(xv.z); v[6] = bf2f_lo(xv.w); v[7] = bf2f_hi(xv.w);
    float s = ((v[0] + v[1]) + (v[2] + v[3])) + ((v[4] + v[5]) + (v[6] + v[7]));
    s = wave_sum(s);
    const float mean = s * (1.f / 512.f);
    float ss = 0.f;
#pragma unroll
    for (int i = 0; i < 8; ++i) { const float d = v[i] - mean; ss = fmaf(d, d, ss); }
    ss = wave_sum(ss);
    const float var = rbf(ss * (1.f / 512.f));
    const float mb = rbf(mean);
    const float den = rbf(__builtin_amdgcn_sqrtf(rbf(var + 1e-5f)));
    const float rd = __builtin_amdgcn_rcpf(den);
    u32x4 o;
    o.x = pack2(rbf(v[0] - mb) * rd, rbf(v[1] - mb) * rd);
    o.y = pack2(rbf(v[2] - mb) * rd, rbf(v[3] - mb) * rd);
    o.z = pack2(rbf(v[4] - mb) * rd, rbf(v[5] - mb) * rd);
    o.w = pack2(rbf(v[6] - mb) * rd, rbf(v[7] - mb) * rd);
    *(LAS u32x4*)(L + L_XN + w * 1024 + l * 16) = o;
    asm volatile("s_waitcnt lgkmcnt(0)" ::: "memory");
    if (l == 0) __hip_atomic_fetch_add((LAS u32*)(L + L_XF), 1u, __ATOMIC_RELAXED, __HIP_MEMORY_SCOPE_WORKGROUP);
  }
#pragma unroll
  for (int t = 4; t < 16; ++t) ld128_wq(wqf[t], vo_wq, rs_wq, (u32)(t * 8192));
#pragma unroll
  for (int t = 0; t < 4; ++t) { if (chain) ld128(aux[t], vo_wuk, rs_wuk, (u32)(t * 64)); else ld128_w(aux[t], vo_f1, rs_f1, (u32)(t * 3584)); }
  if (!chain) { KV(0); KV(1); KV(2); KV(3); }
  STAMP(2);

  u32x2 fx[2];
  u32 epoch;
  {
    asm volatile("s_load_dword %0, %1, 0x0\n\ts_waitcnt lgkmcnt(0)" : "=s"(epoch) : "s"(ws) : "memory");
    epoch += 1u;
    while (__hip_atomic_load((LAS u32*)(L + L_XF), __ATOMIC_RELAXED, __HIP_MEMORY_SCOPE_WORKGROUP) < 4u) __builtin_amdgcn_s_sleep(1);
    asm volatile("" ::: "memory");
    u32x4 xp[2][2];
    xp[0][0] = lds128(L, a_xn0); xp[0][1] = lds128(L, a_xn0 + 16); xp[1][0] = lds128(L, a_xn0 + 2048); xp[1][1] = lds128(L, a_xn0 + 2064);
    fx[0] = *(LAS u32x2*)(L + a_fx); fx[1] = *(LAS u32x2*)(L + a_fx + 2048);
    f32x4 qa0 = {0.f, 0.f, 0.f, 0.f}, qa1 = {0.f, 0.f, 0.f, 0.f};
#define DIAG_A(p, sel) u32x4{__builtin_amdgcn_perm(p, p, sel[0]), __builtin_amdgcn_perm(p, p, sel[1]), __builtin_amdgcn_perm(p, p, sel[2]), __builtin_amdgcn_perm(p, p, sel[3])}
#define QSTEP(t) do { \
      const u32 p0 = xp[0][(t) >> 3][((t) >> 1) & 3], p1 = xp[1][(t) >> 3][((t) >> 1) & 3]; \
      const u32* sel = ((t) & 1) ? sel_hi : sel_lo; \
      const u32x4 a0 = DIAG_A(p0, sel), a1 = DIAG_A(p1, sel); \
      vmwait<19 - (t)>(); PIN(wqf[t]); \
      qa0 = mfma32(a0, wqf[t], qa0); qa1 = mfma32(a1, wqf[t], qa1); } while (0)
    QSTEP(0); QSTEP(1); QSTEP(2); QSTEP(3); QSTEP(4); QSTEP(5); QSTEP(6); QSTEP(7);
    QSTEP(8); QSTEP(9); QSTEP(10); QSTEP(11); QSTEP(12); QSTEP(13); QSTEP(14); QSTEP(15);
    *(LAS f32x4*)(L + a_qpw) = qa0;
    *(LAS f32x4*)(L + a_qpw + 1024) = qa1;
  }
  if (chain) VMWAIT(0); else vmwait<4>();
  PIN(aux[0]); PIN(aux[1]); PIN(aux[2]); PIN(aux[3]);
  STAMP(3);
  BAR();
  if (chain) {
    u32x2 pv[8];
#pragma unroll
    for (int ww = 0; ww < 8; ++ww) pv[ww] = *(LAS u32x2*)(L + a_qpr + ww * 2048);
    float s0, s1;
    s0 = ((__uint_as_float(pv[0].x) + __uint_as_float(pv[1].x)) + (__uint_as_float(pv[2].x) + __uint_as_float(pv[3].x))) + ((__uint_as_float(pv[4].x) + __uint_as_float(pv[5].x)) + (__uint_as_float(pv[6].x) + __uint_as_float(pv[7].x)));
    s1 = ((__uint_as_float(pv[0].y) + __uint_as_float(pv[1].y)) + (__uint_as_float(pv[2].y) + __uint_as_float(pv[3].y))) + ((__uint_as_float(pv[4].y) + __uint_as_float(pv[5].y)) + (__uint_as_float(pv[6].y) + __uint_as_float(pv[7].y)));
    *(LAS u32*)(L + a_qbw) = pack2(s0, s1);
  } else {
    KV(4); KV(5); KV(6); KV(7);
    f32x4 f0a = {0.f, 0.f, 0.f, 0.f}, f1a = {0.f, 0.f, 0.f, 0.f};
#pragma unroll
    for (int t = 0; t < 4; ++t) {
      const u32 p0 = (t >> 1) ? fx[0].y : fx[0].x, p1 = (t >> 1) ? fx[1].y : fx[1].x;
      const u32* sel = (t & 1) ? sel_hi : sel_lo;
      f0a = mfma32(DIAG_A(p0, sel), aux[t], f0a); f1a = mfma32(DIAG_A(p1, sel), aux[t], f1a);
    }
    const u32 a_fpw = L_FP + (u32)(((sw * 4 + (g >> 1)) * 128 + 8 * idx + 4 * (g & 1)) * 4);
    *(LAS f32x4*)(L + a_fpw) = f0a;
    *(LAS f32x4*)(L + a_fpw + 1024) = f1a;
  }
  BAR();
  bool fail = false;
  u32 a_qir[4], a_kvr[4], trb[4], trb2[4];
  u32 a_smw, a_smr, a_pw, a_pr, o_e3w, bp_src, o_stw;
#define LATE_ADDR() do { \
    _Pragma("unroll") for (int c = 0; c < 4; ++c) { const u32 uu = (u32)(4 * c + g); \
      a_qir[c] = L_U + uu * 256 + (((u32)idx ^ uu) * 16); \
      a_kvr[c] = L_R + (u32)((16 * w + idx) * 1024) + ((uu ^ (u32)(2 * (idx & 7))) * 16); \
      const u32 pos = (u32)((8 * w + 2 * c + (p_ >> 1)) ^ (8 * (g & 1) + 2 * q_)); \
      trb[c] = L_R + (u32)((4 * g + q_) * 1024) + pos * 16 + (u32)(8 * (p_ & 1)); trb2[c] = trb[c] + 65536u; \
      PIN(a_qir[c]); PIN(a_kvr[c]); PIN(trb[c]); PIN(trb2[c]); } \
    a_smw = L_SM + (u32)((w * 16 + idx) * 4); a_smr = L_SM + (u32)(idx * 4); \
    a_pw = L_P + (u32)((((w >> 1) * 4 + g) * 16 + idx) * 16 + 8 * (w & 1)); a_pr = L_P + (u32)(l * 16); \
    o_e3w = E3_OFF + (u32)(((((16 * ag + (l >> 2)) * 8 + w) * 4 + ab) * 32 + as) * 128 + (l & 3) * 16); \
    bp_src = (u32)(((l >> 2) + 16 * (l & 3)) * 4); \
    o_stw = ST_OFF + (u32)((((16 * ag + idx) * 4 + ab) * 32 + as) * 8); \
    PIN(a_smw); PIN(a_smr); PIN(a_pw); PIN(a_pr); PIN(o_e3w); PIN(bp_src); PIN(o_stw); } while (0)
  if (chain) {
    u32x4 bq[4];
#pragma unroll
    for (int kk = 0; kk < 4; ++kk) bq[kk] = lds128(L, a_qbr + kk * 64);
    f32x4 acc = {0.f, 0.f, 0.f, 0.f}, acc2 = {0.f, 0.f, 0.f, 0.f};
    acc = mfma32(aux[0], bq[0], acc); acc2 = mfma32(aux[1], bq[1], acc2);
    acc = mfma32(aux[2], bq[2], acc); acc2 = mfma32(aux[3], bq[3], acc2);
    constexpr float SC = 0.08838834764831845f * 1.4426950408889634f;
    u32x4 o; o.x = pack2((acc[0] + acc2[0]) * SC, (acc[1] + acc2[1]) * SC); o.y = epoch; o.z = pack2((acc[2] + acc2[2]) * SC, (acc[3] + acc2[3]) * SC); o.w = epoch;
    if (idx < 4) st128_sc1(o, o_qa, rs_ws, 0u);
    STAMP(4);
    {
      const int t = tid;
      const u32 o_efw = EF_OFF + (u32)((2 * ftile + ((t & 63) >> 5)) * 32768 + ((fmat * 8 + j) * 4 + (t >> 6)) * 512 + (t & 31) * 16);
      u32x2 pv[4];
#pragma unroll
      for (int ww = 0; ww < 4; ++ww) pv[ww] = *(LAS u32x2*)(L + L_FP + t * 8 + ww * 2048);
      const float s0 = (__uint_as_float(pv[0].x) + __uint_as_float(pv[1].x)) + (__uint_as_float(pv[2].x) + __uint_as_float(pv[3].x));
      const float s1 = (__uint_as_float(pv[0].y) + __uint_as_float(pv[1].y)) + (__uint_as_float(pv[2].y) + __uint_as_float(pv[3].y));
      if (ffn_ok) st128_sc1(u32x4{__float_as_uint(s0), epoch, __float_as_uint(s1), epoch}, o_efw, rs_ws, 0u);
    }
    LATE_ADDR();
    const u32 o_e2 = QA_OFF + (u32)((ab * 32 + 16 * ag) * 2048 + w * 8192 + l * 16);
    u32 a_qiw[4];
#pragma unroll
    for (int k2 = 0; k2 < 4; ++k2) { const u32 uu = (u32)(l >> 1); a_qiw[k2] = L_U + uu * 256 + (((u32)(4 * w + k2) ^ (uu & 15)) * 16) + (u32)(8 * (l & 1)); PIN(a_qiw[k2]); }
    const u32 o_efr = EF_OFF + (u32)(uc * 32768 + (tid >> 7) * 16384 + (tid & 127) * 16);
    __builtin_amdgcn_s_sleep(E2_SLEEP);
    STAMP(13);
    u32x4 q4[8];
    for (u32 spin = 0;; ++spin) {
#pragma unroll
      for (int k = 0; k < 8; ++k) ld128_sc1(q4[k], o_e2, rs_ws, (u32)(k * 1024));
      VMWAIT(0);
#pragma unroll
      for (int k = 0; k < 8; ++k) PIN(q4[k]);
      if (__all(tags_ok(q4, 8, epoch))) break;
      if (spin > SPIN_LIMIT) { fail = true; break; }
    }
    STAMP(5);
#pragma unroll
    for (int k = 0; k < 8; ++k) {
      *(LAS u32*)(L + a_qiw[k >> 1] + (k & 1) * 8192) = q4[k].x;
      *(LAS u32*)(L + a_qiw[k >> 1] + (k & 1) * 8192 + 4) = q4[k].z;
    }
    for (u32 spin = 0; ffn_ok; ++spin) {
#pragma unroll
      for (int k = 0; k < 8; ++k) ld128_sc1(q4[k], o_efr, rs_ws, (u32)(k * 2048));
      VMWAIT(0);
#pragma unroll
      for (int k = 0; k < 8; ++k) PIN(q4[k]);
      if (__all(tags_ok(q4, 8, epoch))) break;
      if (spin > SPIN_LIMIT) { fail = true; break; }
    }
    float s0 = 0.f, s1 = 0.f;
#pragma unroll
    for (int k = 0; k < 8; ++k) { s0 += __uint_as_float(q4[k].x); s1 += __uint_as_float(q4[k].z); }
    *(LAS u32x2*)(L + L_HS + tid * 8) = u32x2{__float_as_uint(s0), __float_as_uint(s1)};
  } else {
    const u32x4 sv = {SENT, SENT, SENT, SENT};
    const u32 oc = E3_OFF + (u32)((h * 8 + j) * 16384 + (tid - 256) * 16);
    const u32 oc2 = E5S_OFF + (u32)((bid >> 1) * 16384 + (bid & 1) * 8192 + (tid - 256) * 16);
    KV(8); st128_sc1(sv, oc, rs_ws, 0u);
    KV(9); st128_sc1(sv, oc, rs_ws, 4096u);
    KV(10); st128_sc1(sv, oc, rs_ws, 8192u);
    KV(11); st128_sc1(sv, oc, rs_ws, 12288u);
    LATE_ADDR();
    vmwait<KS>(); KV(12); st128_sc1(sv, oc2, rs_ws, 0u);
    vmwait<KS>(); KV(13); st128_sc1(sv, oc2, rs_ws, 4096u);
    vmwait<KS>(); KV(14); vmwait<KS>(); KV(15); vmwait<KS>(); KV(16); vmwait<KS>(); KV(17);
    vmwait<KS>(); KV(18); vmwait<KS>(); KV(19); vmwait<KS>(); KV(20); vmwait<KS>(); KV(21);
    vmwait<KS>(); KV(22); vmwait<KS>(); KV(23); vmwait<KS>(); KV(24);
    vmwait<11>();
    {
      u32 old = 0;
      if (l == 0) old = __hip_atomic_fetch_add((LAS u32*)(L + L_CNT), 1u, __ATOMIC_RELAXED, __HIP_MEMORY_SCOPE_WORKGROUP);
      old = (u32)__builtin_amdgcn_readfirstlane(old);
      if (old == 3u && l == 0) st64_sc1(u32x2{epoch, epoch}, CF_OFF + (u32)(((h >> 4) * 128 + (h & 15) * 8 + j) * 8), rs_ws);
    }
    vmwait<KS>(); KV(25); vmwait<KS>(); KV(26); vmwait<KS>(); KV(27); vmwait<KS>(); KV(28);
    vmwait<KS>(); KV(29); vmwait<KS>(); KV(30); vmwait<KS>(); KV(31);
    VMWAIT(0);
  }
  BAR();
  u32x4 bq[16];
#pragma unroll
  for (int kk = 0; kk < 16; ++kk) bq[kk] = lds128(L, a_qir[kk & 3] + (kk >> 2) * 4096);
  if (w < 2) {
    const u32x2 g0 = *(LAS u32x2*)(L + L_HS + tid * 8), u0 = *(LAS u32x2*)(L + L_HS + 1024 + tid * 8);
    const float ga = rbf(__uint_as_float(g0.x)), gb = rbf(__uint_as_float(g0.y));
    const float ua = rbf(__uint_as_float(u0.x)), ub = rbf(__uint_as_float(u0.y));
    const float sa = rbf(ga * __builtin_amdgcn_rcpf(1.0f + __expf(-ga))), sb = rbf(gb * __builtin_amdgcn_rcpf(1.0f + __expf(-gb)));
    const int f = 2 * (tid & 31), b = tid >> 5;
    *(LAS u32*)(L + L_HF + (u32)(((((f >> 5) * 4 + ((f >> 2) & 3)) * 4 + b) * 8 + 4 * ((f >> 4) & 1) + (f & 3)) * 2)) = pack2(sa * ua, sb * ub);
  }
  f32x4 sc;
  {
    u32x4 ak[16];
#pragma unroll
    for (int kk = 0; kk < 16; ++kk) ak[kk] = lds128(L, a_kvr[kk & 3] + (kk >> 2) * 256);
    SCHED();
    f32x4 s0 = {0.f, 0.f, 0.f, 0.f}, s1 = {0.f, 0.f, 0.f, 0.f};
#pragma unroll
    for (int kk = 0; kk < 16; kk += 2) { s0 = mfma32(ak[kk], bq[kk], s0); s1 = mfma32(ak[kk + 1], bq[kk + 1], s1); }
    sc = s0 + s1;
    float m = fmaxf(fmaxf(sc[0], sc[1]), fmaxf(sc[2], sc[3]));
    m = x16_max(m); m = x32_max(m);
    if (g == 0) *(LAS float*)(L + a_smw) = m;
  }
  STAMP(6);
  BAR();
  u32x4 cf4;
  ld128_sc1(cf4, CF_OFF + (u32)(ag * 1024 + l * 16), rs_ws, 0u);
  float mall;
  {
    float mv[8];
#pragma unroll
    for (int ww = 0; ww < 8; ++ww) mv[ww] = *(LAS float*)(L + a_smr + ww * 64);
    const float m = fmaxf(fmaxf(fmaxf(mv[0], mv[1]), fmaxf(mv[2], mv[3])), fmaxf(fmaxf(mv[4], mv[5]), fmaxf(mv[6], mv[7])));
    mall = m;
    const float p0 = __builtin_amdgcn_exp2f(sc[0] - m), p1 = __builtin_amdgcn_exp2f(sc[1] - m), p2 = __builtin_amdgcn_exp2f(sc[2] - m), p3 = __builtin_amdgcn_exp2f(sc[3] - m);
    u32x2 pv; pv.x = pack2(p0, p1); pv.y = pack2(p2, p3);
    *(LAS u32x2*)(L + a_pw) = pv;
    float s = (p0 + p1) + (p2 + p3);
    s = x16_sum(s); s = x32_sum(s);
    if (g == 0) *(LAS float*)(L + a_smw + 512) = s;
  }
  for (u32 spin = 0;; ++spin) {
    VMWAIT(0);
    PIN(cf4);
    if (__all(cf4.x == epoch && cf4.y == epoch && cf4.z == epoch && cf4.w == epoch)) break;
    if (spin > SPIN_LIMIT) { fail = true; break; }
    ld128_sc1(cf4, CF_OFF + (u32)(ag * 1024 + l * 16), rs_ws, 0u);
  }
  BAR();
  {
#pragma unroll
    for (int i = 0; i < 2; ++i) { const int row = 8 * w + 4 * i + g; dma16(lbase + L_U + (u32)((8 * w + 4 * i) * 256), (u32)(row * 8192 + ((idx ^ (2 * (row & 7))) * 16)), rs_wuv, (u32)(64 * j * 8192 + h * 256)); }
    const u32 vo_wd = (u32)((l >> 3) * 1024 + (((l & 7) ^ (2 * ((l >> 4) & 3))) * 16));
    dma16(lbase + L_WDN + (u32)(w * 1024), vo_wd, rs_wd, (u32)((uc * 64 + 8 * w) * 1024 + j * 128));
  }
  {
    u32x4 pf4[4], bk[4][4];
    float sl[8];
#pragma unroll
    for (int ks = 0; ks < 4; ++ks) pf4[ks] = lds128(L, a_pr + ks * 1024);
#pragma unroll
    for (int ct = 0; ct < 4; ++ct) {
      bk[ct][0] = trfrag(L, trb[ct], 0, 16384); bk[ct][1] = trfrag(L, trb[ct], 32768, 49152);
      bk[ct][2] = trfrag(L, trb2[ct], 0, 16384); bk[ct][3] = trfrag(L, trb2[ct], 32768, 49152);
    }
#pragma unroll
    for (int ww = 0; ww < 8; ++ww) sl[ww] = *(LAS float*)(L + a_smr + 512 + ww * 64);
    SCHED();
    f32x4 o[4];
#pragma unroll
    for (int ct = 0; ct < 4; ++ct) o[ct] = f32x4{0.f, 0.f, 0.f, 0.f};
#pragma unroll
    for (int ks = 0; ks < 4; ++ks)
#pragma unroll
      for (int ct = 0; ct < 4; ++ct) o[ct] = mfma32(bk[ct][ks], pf4[ks], o[ct]);
    const float ls = ((sl[0] + sl[1]) + (sl[2] + sl[3])) + ((sl[4] + sl[5]) + (sl[6] + sl[7]));
    const float inv = __builtin_amdgcn_rcpf(ls);
    u32 pk[8];
#pragma unroll
    for (int ct = 0; ct < 4; ++ct) { pk[2 * ct] = pack2(o[ct][0] * inv, o[ct][1] * inv); pk[2 * ct + 1] = pack2(o[ct][2] * inv, o[ct][3] * inv); }
#pragma unroll
    for (int i = 0; i < 8; ++i) pk[i] = min((u32)__builtin_amdgcn_ds_bpermute((int)bp_src, (int)pk[i]), SENT - 1u);
    st128_sc1(u32x4{pk[0], pk[1], pk[2], pk[3]}, o_e3w, rs_ws, 0u);
    st128_sc1(u32x4{pk[4], pk[5], pk[6], pk[7]}, o_e3w, rs_ws, 64u);
    if (w == 0) {
      const float lse = mall + __builtin_amdgcn_logf(ls);
      u32x2 v; v.x = __float_as_uint(lse); v.y = epoch;
      if (g == 0) st64_sc1(v, o_stw, rs_ws);
    }
  }
  STAMP(7);
  BAR();
  {
    const int wr = (w + j) & 7;
#pragma unroll
    for (int i = 0; i < 8; ++i)
      dma16_wo(lbase + L_R + (u32)((8 * wr + i) * 1024), dvo[i], rs_wo, (u32)((h * 128 + 8 * wr + i) * 1024));
  }
  const int lb = w >> 1, lp4 = 4 * (w & 1) + (l & 3), lss = l >> 2;
  u32 o_e3r = E3_OFF + (u32)(((h * 8 + j) * 4 + lb) * 32 * 128 + lss * 128 + lp4 * 16);
  u32 o_str = ST_OFF + (u32)(((h * 4 + lb) * 32 + (l & 31)) * 8);
  u32 a_wuvr = L_U + (u32)((4 * g + q_) * 256) + (u32)(((2 * w + (p_ >> 1)) ^ (8 * (g & 1) + 2 * q_)) * 16) + (u32)(8 * (p_ & 1));
  u32 a_latr = L_LAT + (u32)((g * 4 + (idx & 3)) * 16);
  u32 a_ctxw = L_CTX + (u32)((((w >> 1) * 4 + g) * 4 + (idx & 3)) * 16 + 8 * (w & 1));
  u32 a_ctxr = L_CTX + (u32)((g * 4 + (idx & 3)) * 16);
  u32 o_e5s = E5S_OFF + (u32)(((16 * w + 4 * (idx >> 2) + g) * 256 + bid) * 64 + (idx & 3) * 16);
  u32 a_latw;
  {
    const int e = ((l >> 5) & 1) * 4 + ((l >> 4) & 1) * 2 + ((l >> 3) & 1), k = e >> 1;
    const int c = 16 * (2 * (lp4 >> 2) + (k >> 1)) + 4 * (lp4 & 3) + 2 * (k & 1) + (e & 1);
    a_latw = L_LAT + (u32)(((((c >> 5) * 4 + ((c >> 2) & 3)) * 4 + lb) * 8 + 4 * ((c >> 4) & 1) + (c & 3)) * 2);
  }
  PIN(o_e3r); PIN(o_str); PIN(a_wuvr); PIN(a_latr); PIN(a_ctxw); PIN(a_ctxr); PIN(o_e5s); PIN(a_latw);
  {
    const int wr = (w + j) & 7;
#pragma unroll
    for (int i = 0; i < 8; ++i)
      dma16_wo(lbase + L_R + (u32)((64 + 8 * wr + i) * 1024), dvo[i], rs_wo, (u32)((h * 128 + 64 + 8 * wr + i) * 1024));
  }
  STAMP(8);
  {
    u32x4 e4[2]; u32x2 st;
    for (u32 spin = 0;; ++spin) {
      ld64_sc1(st, o_str, rs_ws, 0u);
      ld128_sc1(e4[0], o_e3r, rs_ws, 0u); ld128_sc1(e4[1], o_e3r, rs_ws, 2048u);
      VMWAIT(0);
      PIN(e4[0]); PIN(e4[1]); PIN(st);
      const u32 mx = max(max(max(e4[0].x, e4[0].y), max(e4[0].z, e4[0].w)), max(max(e4[1].x, e4[1].y), max(e4[1].z, e4[1].w)));
      if (__all(mx != SENT && st.y == epoch)) break;
      if (spin > SPIN_LIMIT) { fail = true; break; }
    }
    STAMP(9);
    const float lse = __uint_as_float(st.x);
    const float M = half_max32(lse);
    const float e = __builtin_amdgcn_exp2f(lse - M);
    const float S = half_sum32(e);
    const float wgt = e * __builtin_amdgcn_rcpf(S);
    const float w0 = __builtin_bit_cast(float, __builtin_amdgcn_ds_bpermute(lss * 4, __builtin_bit_cast(int, wgt)));
    const float w1 = __builtin_bit_cast(float, __builtin_amdgcn_ds_bpermute((16 + lss) * 4, __builtin_bit_cast(int, wgt)));
    float v[8];
    v[0] = fmaf(w1, bf2f_lo(e4[1].x), w0 * bf2f_lo(e4[0].x)); v[1] = fmaf(w1, bf2f_hi(e4[1].x), w0 * bf2f_hi(e4[0].x));
    v[2] = fmaf(w1, bf2f_lo(e4[1].y), w0 * bf2f_lo(e4[0].y)); v[3] = fmaf(w1, bf2f_hi(e4[1].y), w0 * bf2f_hi(e4[0].y));
    v[4] = fmaf(w1, bf2f_lo(e4[1].z), w0 * bf2f_lo(e4[0].z)); v[5] = fmaf(w1, bf2f_hi(e4[1].z), w0 * bf2f_hi(e4[0].z));
    v[6] = fmaf(w1, bf2f_lo(e4[1].w), w0 * bf2f_lo(e4[0].w)); v[7] = fmaf(w1, bf2f_hi(e4[1].w), w0 * bf2f_hi(e4[0].w));
#pragma unroll
    for (int i = 0; i < 4; ++i) { auto s = __builtin_amdgcn_permlane32_swap(__float_as_uint(v[i]), __float_as_uint(v[i + 4]), false, false); v[i] = __uint_as_float(s[0]) + __uint_as_float(s[1]); }
#pragma unroll
    for (int i = 0; i < 2; ++i) { auto s = __builtin_amdgcn_permlane16_swap(__float_as_uint(v[i]), __float_as_uint(v[i + 2]), false, false); v[i] = __uint_as_float(s[0]) + __uint_as_float(s[1]); }
    { const bool b3 = (l & 8) != 0; const float snd = b3 ? v[0] : v[1], keep = b3 ? v[1] : v[0]; v[0] = keep + dpp<XOR8>(snd); }
    {
      int t1 = __builtin_amdgcn_update_dpp(0, __builtin_bit_cast(int, v[0]), 0x114, 0xf, 0xa, false);
      t1 = __builtin_amdgcn_update_dpp(t1, __builtin_bit_cast(int, v[0]), 0x104, 0xf, 0x5, false);
      v[0] += __builtin_bit_cast(float, t1);
    }
    *(LAS u16*)(L + a_latw) = (u16)pack2(v[0], v[0]);
  }
  BAR();
  u32x4 wk3[4][4];
  {
    const u32x4 wa0 = trfrag(L, a_wuvr, 0, 4096), wa1 = trfrag(L, a_wuvr, 8192, 12288);
    const u32x4 lb0 = lds128(L, a_latr), lb1 = lds128(L, a_latr + 256);
#pragma unroll
    for (int mt = 0; mt < 4; ++mt) {
      wk3[mt][0] = trfrag(L, trb[mt], 0, 16384); wk3[mt][1] = trfrag(L, trb[mt], 32768, 49152);
      wk3[mt][2] = trfrag(L, trb2[mt], 0, 16384); wk3[mt][3] = trfrag(L, trb2[mt], 32768, 49152);
    }
    f32x4 c = mfma32(wa0, lb0, f32x4{0.f, 0.f, 0.f, 0.f});
    c = mfma32(wa1, lb1, c);
    if (idx < 4) { u32x2 v; v.x = pack2(c[0], c[1]); v.y = pack2(c[2], c[3]); *(LAS u32x2*)(L + a_ctxw) = v; }
  }
  BAR();
  {
    u32x4 cf[4];
#pragma unroll
    for (int ks = 0; ks < 4; ++ks) cf[ks] = lds128(L, a_ctxr + ks * 256);
    f32x4 o[4];
#pragma unroll
    for (int mt = 0; mt < 4; ++mt) o[mt] = f32x4{0.f, 0.f, 0.f, 0.f};
#pragma unroll
    for (int ks = 0; ks < 4; ++ks)
#pragma unroll
      for (int mt = 0; mt < 4; ++mt) o[mt] = mfma32(wk3[mt][ks], cf[ks], o[mt]);
    const int rep = idx >> 2;
    f32x4 v = rep == 0 ? o[0] : rep == 1 ? o[1] : rep == 2 ? o[2] : o[3];
    u32x4 s0;
    s0.x = min(__float_as_uint(v[0]), SENT - 1u); s0.y = min(__float_as_uint(v[1]), SENT - 1u); s0.z = min(__float_as_uint(v[2]), SENT - 1u); s0.w = min(__float_as_uint(v[3]), SENT - 1u);
    st128_sc1(s0, o_e5s, rs_ws, 0u);
  }
  if (w < 4) {
    const int row = 4 * g + q_;
    const u32 a0 = L_WDN + (u32)(row * 128) + (u32)(((2 * w + (p_ >> 1)) ^ (2 * ((row >> 1) & 3))) * 16) + (u32)(8 * (p_ & 1));
    const u32x4 wa0 = trfrag(L, a0, 0, 2048), wa1 = trfrag(L, a0, 4096, 6144);
    const u32x4 hb0 = lds128(L, L_HF + (u32)((g * 4 + (idx & 3)) * 16)), hb1 = lds128(L, L_HF + 256 + (u32)((g * 4 + (idx & 3)) * 16));
    f32x4 y = mfma32(wa0, hb0, f32x4{0.f, 0.f, 0.f, 0.f});
    y = mfma32(wa1, hb1, y);
    const u32 off = E5Y_OFF + (u32)(((16 * j + 4 * w + g) * 32 + uc) * 128 + (idx & 3) * 32);
    if (ffn_ok && idx < 4) {
      st128_sc1(u32x4{__float_as_uint(y[0]), epoch, __float_as_uint(y[1]), epoch}, off, rs_ws, 0u);
      st128_sc1(u32x4{__float_as_uint(y[2]), epoch, __float_as_uint(y[3]), epoch}, off, rs_ws, 16u);
    }
  }
  STAMP(10);
  if (bid < 128) {
    u32x4 a4[2], y4;
    const u32 xfin = ((const u32*)x)[(l & 3) * 256 + bid * 2 + ((l >> 3) & 1)];
    const u32 o_r = E5S_OFF + (u32)(bid * 16384 + tid * 16);
    const u32 o_y = E5Y_OFF + (u32)(bid * 4096 + (tid & 255) * 16);
    const bool yv = tid < 224;
    for (u32 spin = 0;; ++spin) {
      ld128_sc1(y4, o_y, rs_ws, 0u);
      ld128_sc1(a4[0], o_r, rs_ws, 0u); ld128_sc1(a4[1], o_r, rs_ws, 8192u);
      VMWAIT(0);
      PIN(a4[0]); PIN(a4[1]); PIN(y4);
      const u32 mx = max(max(max(a4[0].x, a4[0].y), max(a4[0].z, a4[0].w)), max(max(a4[1].x, a4[1].y), max(a4[1].z, a4[1].w)));
      if (__all(mx != SENT && (!yv || (y4.y == epoch && y4.w == epoch)))) break;
      if (spin > SPIN_LIMIT) { fail = true; break; }
    }
    STAMP(11);
    float v[4];
    v[0] = __uint_as_float(a4[0].x) + __uint_as_float(a4[1].x); v[1] = __uint_as_float(a4[0].y) + __uint_as_float(a4[1].y);
    v[2] = __uint_as_float(a4[0].z) + __uint_as_float(a4[1].z); v[3] = __uint_as_float(a4[0].w) + __uint_as_float(a4[1].w);
#pragma unroll
    for (int i = 0; i < 2; ++i) { auto s = __builtin_amdgcn_permlane32_swap(__float_as_uint(v[i]), __float_as_uint(v[i + 2]), false, false); v[i] = __uint_as_float(s[0]) + __uint_as_float(s[1]); }
    { auto s = __builtin_amdgcn_permlane16_swap(__float_as_uint(v[0]), __float_as_uint(v[1]), false, false); v[0] = __uint_as_float(s[0]) + __uint_as_float(s[1]); }
    v[0] += dpp<0x124>(v[0]); v[0] += dpp<0x128>(v[0]);
    float y0 = yv ? __uint_as_float(y4.x) : 0.f, y1 = yv ? __uint_as_float(y4.z) : 0.f;
    y0 += dpp<XOR8>(y0); y1 += dpp<XOR8>(y1);
    y0 = x16_sum(y0); y1 = x16_sum(y1); y0 = x32_sum(y0); y1 = x32_sum(y1);
    if ((l & 12) == 0) *(LAS float*)(L + L_FIN + (u32)((w * 16 + (l >> 4) * 4 + (l & 3)) * 4)) = v[0];
    if (l < 8 && w < 4) *(LAS u32x2*)(L + L_HS + (u32)((w * 8 + l) * 8)) = u32x2{__float_as_uint(y0), __float_as_uint(y1)};
    BAR();
    if (w == 0) {
      const int mi = (l >> 2) & 3, b = l & 3;
      float sv[8], yv4[4];
#pragma unroll
      for (int ww = 0; ww < 8; ++ww) sv[ww] = *(LAS float*)(L + L_FIN + (u32)((ww * 16 + mi * 4 + b) * 4));
#pragma unroll
      for (int ww = 0; ww < 4; ++ww) yv4[ww] = *(LAS float*)(L + L_HS + (u32)(((ww * 8 + b * 2 + (mi >> 1)) * 2 + (mi & 1)) * 4));
      const float s = ((sv[0] + sv[1]) + (sv[2] + sv[3])) + ((sv[4] + sv[5]) + (sv[6] + sv[7]));
      const float y = (yv4[0] + yv4[1]) + (yv4[2] + yv4[3]);
      const float sb = rbf(s), yb = rbf(y);
      const float gg = rbf(yb * __builtin_amdgcn_rcpf(1.0f + __expf(-yb)));
      const float xv_ = (mi & 1) ? bf2f_hi(xfin) : bf2f_lo(xfin);
      float o = xv_ + rbf(sb * gg);
      if (fail) o = __uint_as_float(0x7fc00000u);
      if (l < 16) out[b * 512 + 4 * bid + mi] = (u16)(pack2(o, 0.f));
      if (bid == 0 && l == 0) st32_sc1(epoch, EPOCH_OFF, rs_ws);
    }
  }
  STAMP(12);
  if (l == 0 && (w == 0 || w == 5)) {
    unsigned long long* dbg = (unsigned long long*)(ws + DBG_OFF) + bid * 32 + (w ? 16 : 0);
#pragma unroll
    for (int i = 0; i < 14; ++i) dbg[i] = stamps[i];
    dbg[14] = clk0; dbg[15] = __builtin_amdgcn_s_memrealtime();
    if ((bid == 3 || bid == 77 || bid == 200) ) printf("bid %d w %d rt0 %llu : %d %d %d %d %d %d %d %d %d %d %d %d | %d  (cycles) total_rt %d\n", bid, w, clk0,
      (int)(stamps[1] - stamps[0]), (int)(stamps[2] - stamps[0]), (int)(stamps[3] - stamps[0]), stamps[4] ? (int)(stamps[4] - stamps[0]) : 0, stamps[5] ? (int)(stamps[5] - stamps[0]) : 0, (int)(stamps[6] - stamps[0]), (int)(stamps[7] - stamps[0]), (int)(stamps[8] - stamps[0]),
      (int)(stamps[9] - stamps[0]), (int)(stamps[10] - stamps[0]), stamps[11] ? (int)(stamps[11] - stamps[0]) : 0, (int)(stamps[12] - stamps[0]), stamps[13] ? (int)(stamps[13] - stamps[0]) : 0, (int)(dbg[15] - clk0));
  }
}

extern "C" void kernel_launch(void* const* d_in, const int* in_sizes, int n_in, void* d_out, int out_size, void* d_ws, size_t ws_size, hipStream_t stream) {
  const u16* x = (const u16*)d_in[0]; const u16* ckv = (const u16*)d_in[1]; const u16* wq = (const u16*)d_in[2]; const u16* wuk = (const u16*)d_in[3];
  const u16* wuv = (const u16*)d_in[4]; const u16* wo = (const u16*)d_in[5]; const u16* wg = (const u16*)d_in[7]; const u16* wu = (const u16*)d_in[8]; const u16* wd = (const u16*)d_in[9];
  fused_decode<<<256, 512, 0, stream>>>(x, ckv, wq, wuk, wuv, wo, wg, wu, wd, (u16*)d_out, (unsigned char*)d_ws);
}
```

```cpp
#include <hip/hip_runtime.h>
#include <stdint.h>

typedef unsigned u32;
typedef unsigned short u16;
typedef u32 u32x4 __attribute__((ext_vector_type(4)));
typedef u32 u32x2 __attribute__((ext_vector_type(2)));
typedef float f32x4 __attribute__((ext_vector_type(4)));
typedef short s16x4 __attribute__((ext_vector_type(4)));
typedef __bf16 bf16x8 __attribute__((ext_vector_type(8)));
typedef __bf16 bf16x2 __attribute__((ext_vector_type(2)));
#define LAS __attribute__((address_space(3)))
typedef __amdgpu_buffer_rsrc_t rsrc_t;

#define DIAG 0
#define NT_W 0

constexpr u32 EPOCH_OFF = 0;
constexpr u32 QA_OFF = 4096;
constexpr u32 ST_OFF = QA_OFF + 4 * 32 * 2048;
constexpr u32 CF_OFF = ST_OFF + 32 * 4 * 32 * 8;
constexpr u32 EF_OFF = CF_OFF + 4096;
constexpr u32 E5Y_OFF = EF_OFF + 32 * 32768;
constexpr u32 E3_OFF = E5Y_OFF + 128 * 4096;
constexpr u32 E5S_OFF = E3_OFF + 256 * 16384;
constexpr u32 WS_END = E5S_OFF + 128 * 16384;
constexpr u32 DBG_OFF = WS_END;
constexpr u32 WS_TOTAL = DBG_OFF + 256 * 64 * 8;
constexpr u32 SENT = 0xFFFFFFFFu;

constexpr u32 L_R = 0;
constexpr u32 L_U = 131072;
constexpr u32 L_WDN = L_U + 16384;
constexpr u32 L_M = 155648;
constexpr u32 L_XN = L_M;
constexpr u32 L_P = L_M;
constexpr u32 L_QB = L_M + 4096;
constexpr u32 L_CTX = L_QB;
constexpr u32 L_SM = L_M + 5120;
constexpr u32 L_SL = L_M + 5632;
constexpr u32 L_LAT = L_M + 6144;
constexpr u32 L_HF = L_M + 6656;
constexpr u32 L_FIN = L_M + 7168;
constexpr u32 L_HS = L_M;
constexpr u32 L_FP = L_WDN;
constexpr u32 L_CNT = L_M + 7680;
constexpr u32 L_XF = L_M + 7696;
constexpr u32 LDS_BYTES = 163840;

constexpr u32 SPIN_LIMIT = 1u << 16;
#define KD 6
constexpr int PD = 8;
#define CPRE 10
#define KS 11
#define E2_SLEEP 8

__device__ __forceinline__ float bf2f_lo(u32 v) { return __uint_as_float(v << 16); }
__device__ __forceinline__ float bf2f_hi(u32 v) { return __uint_as_float(v & 0xffff0000u); }
__device__ __forceinline__ u32 pack2(float lo, float hi) { bf16x2 v = {(__bf16)lo, (__bf16)hi}; return __builtin_bit_cast(u32, v); }
__device__ __forceinline__ float rbf(float f) { return __uint_as_float(((u32)__builtin_bit_cast(u16, (__bf16)f)) << 16); }
__device__ __forceinline__ f32x4 mfma32(u32x4 a, u32x4 b, f32x4 c) {
  return __builtin_amdgcn_mfma_f32_16x16x32_bf16(__builtin_bit_cast(bf16x8, a), __builtin_bit_cast(bf16x8, b), c, 0, 0, 0);
}
__device__ __forceinline__ f32x4 mfma16(u32x2 a, u32x2 b, f32x4 c) {
  return __builtin_amdgcn_mfma_f32_16x16x16bf16_1k(__builtin_bit_cast(s16x4, a), __builtin_bit_cast(s16x4, b), c, 0, 0, 0);
}
template <int CTRL> __device__ __forceinline__ float dpp(float x) {
  return __builtin_bit_cast(float, __builtin_amdgcn_mov_dpp(__builtin_bit_cast(int, x), CTRL, 0xf, 0xf, true));
}
constexpr int XOR1 = 0xB1, XOR2 = 0x4E, XOR7 = 0x141, XOR15 = 0x140, XOR8 = 0x128;
__device__ __forceinline__ float x16_sum(float x) { auto s = __builtin_amdgcn_permlane16_swap(__float_as_uint(x), __float_as_uint(x), false, false); return __uint_as_float(s[0]) + __uint_as_float(s[1]); }
__device__ __forceinline__ float x32_sum(float x) { auto s = __builtin_amdgcn_permlane32_swap(__float_as_uint(x), __float_as_uint(x), false, false); return __uint_as_float(s[0]) + __uint_as_float(s[1]); }
__device__ __forceinline__ float x16_max(float x) { auto s = __builtin_amdgcn_permlane16_swap(__float_as_uint(x), __float_as_uint(x), false, false); return fmaxf(__uint_as_float(s[0]), __uint_as_float(s[1])); }
__device__ __forceinline__ float x32_max(float x) { auto s = __builtin_amdgcn_permlane32_swap(__float_as_uint(x), __float_as_uint(x), false, false); return fmaxf(__uint_as_float(s[0]), __uint_as_float(s[1])); }
__device__ __forceinline__ float wave_sum(float v) {
  v += dpp<XOR1>(v); v += dpp<XOR2>(v); v += dpp<XOR7>(v); v += dpp<XOR15>(v); v = x16_sum(v); v = x32_sum(v); return v;
}
__device__ __forceinline__ float half_sum32(float v) {
  v += dpp<XOR1>(v); v += dpp<XOR2>(v); v += dpp<XOR7>(v); v += dpp<XOR15>(v); v = x16_sum(v); return v;
}
__device__ __forceinline__ float half_max32(float v) {
  v = fmaxf(v, dpp<XOR1>(v)); v = fmaxf(v, dpp<XOR2>(v)); v = fmaxf(v, dpp<XOR7>(v)); v = fmaxf(v, dpp<XOR15>(v)); v = x16_max(v); return v;
}

#define BAR() asm volatile("s_waitcnt lgkmcnt(0)\n\ts_barrier" ::: "memory")
#define RAWBAR() asm volatile("s_barrier" ::: "memory")
#define VMWAIT(n) asm volatile("s_waitcnt vmcnt(" #n ")" ::: "memory")
#define SCHED() __builtin_amdgcn_sched_barrier(0)
template <int N> __device__ __forceinline__ void vmwait() { asm volatile("s_waitcnt vmcnt(%0)" :: "i"(N) : "memory"); }
#define PIN(a) asm volatile("" : "+v"(a))

#define NTS ""
__device__ __forceinline__ void ld128(u32x4& d, u32 voff, rsrc_t rs, u32 soff) {
  asm volatile("buffer_load_dwordx4 %0, %1, %2, %3 offen" : "=v"(d) : "v"(voff), "s"(rs), "s"(soff) : "memory");
}
__device__ __forceinline__ void ld128_w(u32x4& d, u32 voff, rsrc_t rs, u32 soff) {
  asm volatile("buffer_load_dwordx4 %0, %1, %2, %3 offen" NTS : "=v"(d) : "v"(voff), "s"(rs), "s"(soff) : "memory");
}
__device__ __forceinline__ void ld128_sc1(u32x4& d, u32 voff, rsrc_t rs, u32 soff) {
  asm volatile("buffer_load_dwordx4 %0, %1, %2, %3 offen sc1" : "=v"(d) : "v"(voff), "s"(rs), "s"(soff) : "memory");
}
__device__ __forceinline__ void ld64_sc1(u32x2& d, u32 voff, rsrc_t rs, u32 soff) {
  asm volatile("buffer_load_dwordx2 %0, %1, %2, %3 offen sc1" : "=v"(d) : "v"(voff), "s"(rs), "s"(soff) : "memory");
}
__device__ __forceinline__ void ld32_sc1(u32& d, u32 voff, rsrc_t rs, u32 soff) {
  asm volatile("buffer_load_dword %0, %1, %2, %3 offen sc1" : "=v"(d) : "v"(voff), "s"(rs), "s"(soff) : "memory");
}
__device__ __forceinline__ void ld32(u32& d, u32 voff, rsrc_t rs, u32 soff) {
  asm volatile("buffer_load_dword %0, %1, %2, %3 offen" : "=v"(d) : "v"(voff), "s"(rs), "s"(soff) : "memory");
}
__device__ __forceinline__ void st128_sc1(u32x4 d, u32 voff, rsrc_t rs, u32 soff) {
  asm volatile("buffer_store_dwordx4 %0, %1, %2, %3 offen sc1\n\ts_nop 1" :: "v"(d), "v"(voff), "s"(rs), "s"(soff) : "memory");
}
__device__ __forceinline__ void st64_sc1(u32x2 d, u32 voff, rsrc_t rs) {
  asm volatile("buffer_store_dwordx2 %0, %1, %2, 0 offen sc1\n\ts_nop 1" :: "v"(d), "v"(voff), "s"(rs) : "memory");
}
__device__ __forceinline__ void st32_sc1(u32 d, u32 voff, rsrc_t rs) {
  asm volatile("buffer_store_dword %0, %1, %2, 0 offen sc1\n\ts_nop 1" :: "v"(d), "v"(voff), "s"(rs) : "memory");
}
__device__ __forceinline__ void dma16(u32 lds_dst, u32 voff, rsrc_t rs, u32 soff) {
  asm volatile("s_mov_b32 m0, %0\n\ts_nop 0\n\tbuffer_load_dwordx4 %1, %2, %3 offen" NTS " lds" :: "s"(lds_dst), "v"(voff), "s"(rs), "s"(soff) : "memory");
}
__device__ __forceinline__ void dma16_def(u32 lds_dst, u32 voff, rsrc_t rs, u32 soff) {
  asm volatile("s_mov_b32 m0, %0\n\ts_nop 0\n\tbuffer_load_dwordx4 %1, %2, %3 offen lds" :: "s"(lds_dst), "v"(voff), "s"(rs), "s"(soff) : "memory");
}
#define KV_NT 1
#define KV_STAG 0
#define WO_NT 1
#define WQ_NT 1
#define dma16_kv dma16
#define dma16_wo dma16
#define ld128_wq ld128_w
__device__ __forceinline__ rsrc_t mk(const void* p, u32 bytes) { return __builtin_amdgcn_make_buffer_rsrc((void*)p, 0, bytes, 0x00020000); }
__device__ __forceinline__ u32x4 lds128(LAS unsigned char* L, u32 a) { return *(LAS u32x4*)(L + a); }
__device__ __forceinline__ u32x4 trfrag(LAS unsigned char* L, u32 a, u32 o0, u32 o1) {
  const s16x4 t0 = __builtin_amdgcn_ds_read_tr16_b64_v4i16((LAS s16x4*)(L + a + o0));
  const s16x4 t1 = __builtin_amdgcn_ds_read_tr16_b64_v4i16((LAS s16x4*)(L + a + o1));
  const u32x2 x = __builtin_bit_cast(u32x2, t0), y = __builtin_bit_cast(u32x2, t1);
  return u32x4{x.x, x.y, y.x, y.y};
}
__device__ __forceinline__ bool tags_ok(const u32x4* v, int n, u32 epoch) {
  u32 a = v[0].y & v[0].w, o = v[0].y | v[0].w;
#pragma unroll
  for (int k = 1; k < n; ++k) { a &= v[k].y & v[k].w; o |= v[k].y | v[k].w; }
  return a == epoch && o == epoch;
}

#define STAMP(i) do {} while (0)

__global__ void __launch_bounds__(512, 2)
fused_decode(const u16* __restrict__ x, const u16* __restrict__ ckv, const u16* __restrict__ wq, const u16* __restrict__ wuk,
             const u16* __restrict__ wuv, const u16* __restrict__ wo, const u16* __restrict__ wg, const u16* __restrict__ wu,
             const u16* __restrict__ wd, u16* __restrict__ out, unsigned char* __restrict__ ws) {
  __shared__ __attribute__((aligned(16))) unsigned char lds[LDS_BYTES];
  LAS unsigned char* L = (LAS unsigned char*)lds;
  const u32 lbase = (u32)(size_t)L;
  const int tid = threadIdx.x;
  const int w = __builtin_amdgcn_readfirstlane(tid >> 6);
  const int l = tid & 63, g = l >> 4, idx = l & 15;
  const int bid = blockIdx.x, xc = bid & 7, r = bid >> 3;
  if (bid < 8 && w == 7) asm volatile("buffer_wbl2 sc1" ::: "memory");
  const int h = xc + 8 * (r >> 3), j = r & 7;
  const int P = xc * 16 + (r & 15), ab = P >> 5, as = P & 31, ag = 1 - (r >> 4);
  const bool ffn_ok = h < 28;
  const int uc = ffn_ok ? h : 27;
  const int fmat = uc & 1, ftile = uc >> 1;
  STAMP(0);

  const rsrc_t rs_x = mk(x, 4096), rs_kv = mk(ckv, 4u * 4096 * 1024), rs_wq = mk(wq, 4u << 20), rs_wuk = mk(wuk, 4u << 20),
               rs_wuv = mk(wuv, 4u << 20), rs_wo = mk(wo, 4u << 20), rs_wg = mk(wg, 512 * 1792 * 2), rs_wu = mk(wu, 512 * 1792 * 2),
               rs_wd = mk(wd, 512 * 1792 * 2), rs_ws = mk(ws, WS_TOTAL);

  const bool chain = w < 4;
  const int sw = w & 3;
  const int q_ = idx >> 2, p_ = idx & 3, jp = idx & 7;
  const int blk2 = (j + 4 + sw) & 7;
  bool fail = false;
  u32 epoch;
  u32 dvo[8];
  u32 a_qir[4], a_kvr[4], trb[4], trb2[4];
  u32 a_smw, a_smr, a_pw, a_pr, o_e3w, bp_src, o_stw;
#define SEL_INIT() u32 sel_lo[4], sel_hi[4]; \
  _Pragma("unroll") for (int i = 0; i < 4; ++i) { const bool on = (i == (jp >> 1)); \
    sel_lo[i] = on ? ((jp & 1) ? 0x01000c0cu : 0x0c0c0100u) : 0x0c0c0c0cu; sel_hi[i] = on ? ((jp & 1) ? 0x03020c0cu : 0x0c0c0302u) : 0x0c0c0c0cu; \
    PIN(sel_lo[i]); PIN(sel_hi[i]); }
#define DIAG_A(p, sel) u32x4{__builtin_amdgcn_perm(p, p, sel[0]), __builtin_amdgcn_perm(p, p, sel[1]), __builtin_amdgcn_perm(p, p, sel[2]), __builtin_amdgcn_perm(p, p, sel[3])}
#define EPOCH_AND_XN() do { \
    asm volatile("s_load_dword %0, %1, 0x0\n\ts_waitcnt lgkmcnt(0)" : "=s"(epoch) : "s"(ws) : "memory"); epoch += 1u; \
    while (__hip_atomic_load((LAS u32*)(L + L_XF), __ATOMIC_RELAXED, __HIP_MEMORY_SCOPE_WORKGROUP) < 4u) __builtin_amdgcn_s_sleep(1); \
    asm volatile("" ::: "memory"); } while (0)
#define DSTEP(acc0, acc1, wf, p0, p1, par) do { const u32* sel_ = (par) ? sel_hi : sel_lo; \
    acc0 = mfma32(DIAG_A(p0, sel_), wf, acc0); acc1 = mfma32(DIAG_A(p1, sel_), wf, acc1); } while (0)
#define LATE_ADDR() do { \
    _Pragma("unroll") for (int c = 0; c < 4; ++c) { const u32 uu = (u32)(4 * c + g); \
      a_qir[c] = L_U + uu * 256 + (((u32)idx ^ uu) * 16); \
      a_kvr[c] = L_R + (u32)((16 * w + idx) * 1024) + ((uu ^ (u32)(2 * (idx & 7))) * 16); \
      const u32 pos = (u32)((8 * w + 2 * c + (p_ >> 1)) ^ (8 * (g & 1) + 2 * q_)); \
      trb[c] = L_R + (u32)((4 * g + q_) * 1024) + pos * 16 + (u32)(8 * (p_ & 1)); trb2[c] = trb[c] + 65536u; \
      PIN(a_qir[c]); PIN(a_kvr[c]); PIN(trb[c]); PIN(trb2[c]); } \
    _Pragma("unroll") for (int i = 0; i < 8; ++i) { dvo[i] = (u32)((l ^ (2 * i)) * 16); PIN(dvo[i]); } \
    a_smw = L_SM + (u32)((w * 16 + idx) * 4); a_smr = L_SM + (u32)(idx * 4); \
    a_pw = L_P + (u32)((((w >> 1) * 4 + g) * 16 + idx) * 16 + 8 * (w & 1)); a_pr = L_P + (u32)(l * 16); \
    o_e3w = E3_OFF + (u32)(((((16 * ag + (l >> 2)) * 8 + w) * 4 + ab) * 32 + as) * 128 + (l & 3) * 16); \
    bp_src = (u32)(((l >> 2) + 16 * (l & 3)) * 4); \
    o_stw = ST_OFF + (u32)((((16 * ag + idx) * 4 + ab) * 32 + as) * 8); \
    PIN(a_smw); PIN(a_smr); PIN(a_pw); PIN(a_pr); PIN(o_e3w); PIN(bp_src); PIN(o_stw); } while (0)
  const u32 a_qpw = L_U + (u32)(((w * 4 + (g >> 1)) * 128 + 8 * idx + 4 * (g & 1)) * 4);

  const int wb = (w + j) & 7;
  const u32 vo_wq = (u32)((64 * wb + 16 * g) * 8192 + h * 256 + idx * 16);
  const u32 a_xn = L_XN + (u32)((idx >> 3) * 1024 + (64 * wb + 16 * g) * 2);
  constexpr u32 L_WUK = L_R + 112 * 1024;
#define QLOOP(EXTRA) do { \
      const u32x4 xa0 = lds128(L, a_xn), xa1 = lds128(L, a_xn + 16), xb0 = lds128(L, a_xn + 2048), xb1 = lds128(L, a_xn + 2064); \
      f32x4 qa0 = {0.f, 0.f, 0.f, 0.f}, qa1 = {0.f, 0.f, 0.f, 0.f}; \
      QS(0, EXTRA, xa0[0], xb0[0]); QS(1, EXTRA, xa0[0], xb0[0]); QS(2, EXTRA, xa0[1], xb0[1]); QS(3, EXTRA, xa0[1], xb0[1]); \
      QS(4, EXTRA, xa0[2], xb0[2]); QS(5, EXTRA, xa0[2], xb0[2]); QS(6, EXTRA, xa0[3], xb0[3]); QS(7, EXTRA, xa0[3], xb0[3]); \
      QS(8, EXTRA, xa1[0], xb1[0]); QS(9, EXTRA, xa1[0], xb1[0]); QS(10, EXTRA, xa1[1], xb1[1]); QS(11, EXTRA, xa1[1], xb1[1]); \
      QS(12, EXTRA, xa1[2], xb1[2]); QS(13, EXTRA, xa1[2], xb1[2]); QS(14, EXTRA, xa1[3], xb1[3]); QS(15, EXTRA, xa1[3], xb1[3]); \
      *(LAS f32x4*)(L + a_qpw) = qa0; *(LAS f32x4*)(L + a_qpw + 1024) = qa1; } while (0)
#define QS(t, EXTRA, P0, P1) do { vmwait<15 - (t) + (EXTRA)>(); PIN(wqf[t]); DSTEP(qa0, qa1, wqf[t], P0, P1, (t) & 1); } while (0)
#define EPOCH() do { asm volatile("s_load_dword %0, %1, 0x0\n\ts_waitcnt lgkmcnt(0)" : "=s"(epoch) : "s"(ws) : "memory"); epoch += 1u; } while (0)

  if (chain) {
    u32x4 xv, wqf[16];
    __builtin_amdgcn_s_setprio(3);
    ld128(xv, (u32)(w * 1024 + l * 16), rs_x, 0u);
#pragma unroll
    for (int t = 0; t < CPRE; ++t) ld128_wq(wqf[t], vo_wq, rs_wq, (u32)(t * 8192));
    if (tid == 0) *(LAS u32*)(L + L_CNT) = 0u;
    SEL_INIT();
    const u32 a_qbr = L_QB + (u32)(((idx & 3) * 128 + 8 * g) * 2);
    const u32 o_qa = QA_OFF + (u32)((((idx & 3) * 32 + h) * 256 + (64 * j + 16 * sw + 4 * g) / 2) * 8);
    STAMP(1);
    {
      vmwait<CPRE>();
      PIN(xv);
      float v[8];
      v[0] = bf2f_lo(xv.x); v[1] = bf2f_hi(xv.x); v[2] = bf2f_lo(xv.y); v[3] = bf2f_hi(xv.y);
      v[4] = bf2f_lo(xv.z); v[5] = bf2f_hi(xv.z); v[6] = bf2f_lo(xv.w); v[7] = bf2f_hi(xv.w);
      float s = ((v[0] + v[1]) + (v[2] + v[3])) + ((v[4] + v[5]) + (v[6] + v[7]));
      s = wave_sum(s);
      const float mean = s * (1.f / 512.f);
      float ss = 0.f;
#pragma unroll
      for (int i = 0; i < 8; ++i) { const float d = v[i] - mean; ss = fmaf(d, d, ss); }
      ss = wave_sum(ss);
      const float var = rbf(ss * (1.f / 512.f));
      const float mb = rbf(mean);
      const float den = rbf(__builtin_amdgcn_sqrtf(rbf(var + 1e-5f)));
      const float rd = __builtin_amdgcn_rcpf(den);
      u32x4 o;
      o.x = pack2(rbf(v[0] - mb) * rd, rbf(v[1] - mb) * rd);
      o.y = pack2(rbf(v[2] - mb) * rd, rbf(v[3] - mb) * rd);
      o.z = pack2(rbf(v[4] - mb) * rd, rbf(v[5] - mb) * rd);
      o.w = pack2(rbf(v[6] - mb) * rd, rbf(v[7] - mb) * rd);
      *(LAS u32x4*)(L + L_XN + w * 1024 + l * 16) = o;
    }
#pragma unroll
    for (int t = CPRE; t < 16; ++t) ld128_wq(wqf[t], vo_wq, rs_wq, (u32)(t * 8192));
    EPOCH();
    BAR();
    STAMP(2);
    QLOOP(0);
    STAMP(3);
    BAR();
    u32x4 wuk[4];
    {
      u32x2 pv[8];
#pragma unroll
      for (int ww = 0; ww < 8; ++ww) pv[ww] = *(LAS u32x2*)(L + L_U + tid * 8 + ww * 2048);
      float s0, s1;
      s0 = ((__uint_as_float(pv[0].x) + __uint_as_float(pv[1].x)) + (__uint_as_float(pv[2].x) + __uint_as_float(pv[3].x))) + ((__uint_as_float(pv[4].x) + __uint_as_float(pv[5].x)) + (__uint_as_float(pv[6].x) + __uint_as_float(pv[7].x)));
      s1 = ((__uint_as_float(pv[0].y) + __uint_as_float(pv[1].y)) + (__uint_as_float(pv[2].y) + __uint_as_float(pv[3].y))) + ((__uint_as_float(pv[4].y) + __uint_as_float(pv[5].y)) + (__uint_as_float(pv[6].y) + __uint_as_float(pv[7].y)));
      *(LAS u32*)(L + L_QB + tid * 4) = pack2(s0, s1);
    }
    BAR();
    {
      u32x4 bq[4];
#pragma unroll
      for (int kk = 0; kk < 4; ++kk) bq[kk] = lds128(L, a_qbr + kk * 64);
#pragma unroll
      for (int kk = 0; kk < 4; ++kk) wuk[kk] = lds128(L, L_WUK + (u32)((16 * sw + idx) * 256) + (u32)(((4 * kk + g) ^ (2 * (idx & 7))) * 16));
      f32x4 acc = {0.f, 0.f, 0.f, 0.f}, acc2 = {0.f, 0.f, 0.f, 0.f};
      acc = mfma32(wuk[0], bq[0], acc); acc2 = mfma32(wuk[1], bq[1], acc2);
      acc = mfma32(wuk[2], bq[2], acc); acc2 = mfma32(wuk[3], bq[3], acc2);
      constexpr float SC = 0.08838834764831845f * 1.4426950408889634f;
      u32x4 o; o.x = pack2((acc[0] + acc2[0]) * SC, (acc[1] + acc2[1]) * SC); o.y = epoch; o.z = pack2((acc[2] + acc2[2]) * SC, (acc[3] + acc2[3]) * SC); o.w = epoch;
      if (idx < 4) st128_sc1(o, o_qa, rs_ws, 0u);
    }
    STAMP(4);
    BAR();
    {
      const int t = tid;
      const u32 o_efw = EF_OFF + (u32)((2 * ftile + ((t & 63) >> 5)) * 32768 + ((fmat * 8 + j) * 4 + (t >> 6)) * 512 + (t & 31) * 16);
      u32x2 pv[4];
#pragma unroll
      for (int ww = 0; ww < 4; ++ww) pv[ww] = *(LAS u32x2*)(L + L_FP + t * 8 + ww * 2048);
      const float s0 = (__uint_as_float(pv[0].x) + __uint_as_float(pv[1].x)) + (__uint_as_float(pv[2].x) + __uint_as_float(pv[3].x));
      const float s1 = (__uint_as_float(pv[0].y) + __uint_as_float(pv[1].y)) + (__uint_as_float(pv[2].y) + __uint_as_float(pv[3].y));
      if (ffn_ok) st128_sc1(u32x4{__float_as_uint(s0), epoch, __float_as_uint(s1), epoch}, o_efw, rs_ws, 0u);
    }
    LATE_ADDR();
    const u32 o_e2 = QA_OFF + (u32)((ab * 32 + 16 * ag) * 2048 + w * 8192 + l * 16);
    u32 a_qiw[4];
#pragma unroll
    for (int k2 = 0; k2 < 4; ++k2) { const u32 uu = (u32)(l >> 1); a_qiw[k2] = L_U + uu * 256 + (((u32)(4 * w + k2) ^ (uu & 15)) * 16) + (u32)(8 * (l & 1)); PIN(a_qiw[k2]); }
    __builtin_amdgcn_s_sleep(E2_SLEEP);
    STAMP(13);
    u32x4 q4[8];
    for (u32 spin = 0;; ++spin) {
#pragma unroll
      for (int k = 0; k < 8; ++k) ld128_sc1(q4[k], o_e2, rs_ws, (u32)(k * 1024));
      VMWAIT(0);
#pragma unroll
      for (int k = 0; k < 8; ++k) PIN(q4[k]);
      if (__all(tags_ok(q4, 8, epoch))) break;
      if (spin > SPIN_LIMIT) { fail = true; break; }
    }
    STAMP(5);
#pragma unroll
    for (int k = 0; k < 8; ++k) {
      *(LAS u32*)(L + a_qiw[k >> 1] + (k & 1) * 8192) = q4[k].x;
      *(LAS u32*)(L + a_qiw[k >> 1] + (k & 1) * 8192 + 4) = q4[k].z;
    }
  } else {
    u32x4 wqf[16], f1w[4];
#pragma unroll
    for (int t = 0; t < 16; ++t) ld128_wq(wqf[t], vo_wq, rs_wq, (u32)(t * 8192));
    const u32 kvrow0 = (u32)((ab * 4096 + as * 128 + 32 * sw) * 1024);
#pragma unroll
    for (int i = 0; i < 8; ++i) { dvo[i] = (u32)((l ^ (2 * i)) * 16); PIN(dvo[i]); }
#define KV(i) dma16_kv(lbase + L_R + (u32)((32 * sw + (i)) * 1024), dvo[(i) & 7], rs_kv, kvrow0 + (u32)((i) * 1024))
    SEL_INIT();
    const u32 a_fx = L_XN + (u32)((idx >> 3) * 1024 + (64 * j + 16 * sw + 4 * g) * 2);
    const u32 vo3 = (u32)((64 * j + 16 * sw + 4 * g) * 3584 + ftile * 256 + idx * 16);
    const rsrc_t rs_f1 = fmat ? rs_wu : rs_wg;
    EPOCH();
    BAR();
#pragma unroll
    for (int i = 0; i < 4; ++i) { const int row = 16 * sw + 4 * i + g;
      dma16(lbase + L_WUK + (u32)((16 * sw + 4 * i) * 256), (u32)(row * 8192 + ((idx ^ (2 * (row & 7))) * 16)), rs_wuk, (u32)(64 * j * 8192 + h * 256)); }
#pragma unroll
    for (int t = 0; t < 4; ++t) ld128_w(f1w[t], vo3, rs_f1, (u32)(t * 3584));
    KV(0); KV(1); KV(2); KV(3);
    STAMP(2);
    const u32x2 fx0 = *(LAS u32x2*)(L + a_fx), fx1 = *(LAS u32x2*)(L + a_fx + 2048);
    QLOOP(12);
    STAMP(3);
    BAR();
    vmwait<8>();
    BAR();
    {
      vmwait<4>();
      PIN(f1w[0]); PIN(f1w[1]); PIN(f1w[2]); PIN(f1w[3]);
      f32x4 f0a = {0.f, 0.f, 0.f, 0.f}, f1a = {0.f, 0.f, 0.f, 0.f};
      DSTEP(f0a, f1a, f1w[0], fx0.x, fx1.x, 0); DSTEP(f0a, f1a, f1w[1], fx0.x, fx1.x, 1);
      DSTEP(f0a, f1a, f1w[2], fx0.y, fx1.y, 0); DSTEP(f0a, f1a, f1w[3], fx0.y, fx1.y, 1);
      const u32 a_fpw = L_FP + (u32)(((sw * 4 + (g >> 1)) * 128 + 8 * idx + 4 * (g & 1)) * 4);
      *(LAS f32x4*)(L + a_fpw) = f0a;
      *(LAS f32x4*)(L + a_fpw + 1024) = f1a;
    }
    BAR();
    const u32x4 sv = {SENT, SENT, SENT, SENT};
    const u32 oc = E3_OFF + (u32)((h * 8 + j) * 16384 + (tid - 256) * 16);
    const u32 oc2 = E5S_OFF + (u32)((bid >> 1) * 16384 + (bid & 1) * 8192 + (tid - 256) * 16);
    KV(4); st128_sc1(sv, oc, rs_ws, 0u);
    KV(5); st128_sc1(sv, oc, rs_ws, 4096u);
    KV(6); st128_sc1(sv, oc, rs_ws, 8192u);
    KV(7); st128_sc1(sv, oc, rs_ws, 12288u);
    KV(8); st128_sc1(sv, oc2, rs_ws, 0u);
    KV(9); st128_sc1(sv, oc2, rs_ws, 4096u);
    KV(10); KV(11);
    LATE_ADDR();
    vmwait<KS>(); KV(12); vmwait<KS>(); KV(13);
    vmwait<KS>(); KV(14); vmwait<KS>(); KV(15);
    vmwait<6>();
    {
      u32 old = 0;
      if (l == 0) old = __hip_atomic_fetch_add((LAS u32*)(L + L_CNT), 1u, __ATOMIC_RELAXED, __HIP_MEMORY_SCOPE_WORKGROUP);
      old = (u32)__builtin_amdgcn_readfirstlane(old);
      if (old == 3u && l == 0) st64_sc1(u32x2{epoch, epoch}, CF_OFF + (u32)(((h >> 4) * 128 + (h & 15) * 8 + j) * 8), rs_ws);
    }
    KV(16); KV(17); KV(18); KV(19); KV(20);
    vmwait<KS>(); KV(21); vmwait<KS>(); KV(22); vmwait<KS>(); KV(23); vmwait<KS>(); KV(24);
    vmwait<KS>(); KV(25); vmwait<KS>(); KV(26); vmwait<KS>(); KV(27); vmwait<KS>(); KV(28);
    vmwait<KS>(); KV(29); vmwait<KS>(); KV(30); vmwait<KS>(); KV(31);
    VMWAIT(0);
  }
  BAR();
  u32x4 bq[16];
#pragma unroll
  for (int kk = 0; kk < 16; ++kk) bq[kk] = lds128(L, a_qir[kk & 3] + (kk >> 2) * 4096);
  u32x4 cf4;
  ld128_sc1(cf4, CF_OFF + (u32)(ag * 1024 + l * 16), rs_ws, 0u);
  f32x4 sc;
  {
    u32x4 ak[16];
#pragma unroll
    for (int kk = 0; kk < 16; ++kk) ak[kk] = lds128(L, a_kvr[kk & 3] + (kk >> 2) * 256);
    SCHED();
    f32x4 s0 = {0.f, 0.f, 0.f, 0.f}, s1 = {0.f, 0.f, 0.f, 0.f};
#pragma unroll
    for (int kk = 0; kk < 16; kk += 2) { s0 = mfma32(ak[kk], bq[kk], s0); s1 = mfma32(ak[kk + 1], bq[kk + 1], s1); }
    sc = s0 + s1;
    float m = fmaxf(fmaxf(sc[0], sc[1]), fmaxf(sc[2], sc[3]));
    m = x16_max(m); m = x32_max(m);
    if (g == 0) *(LAS float*)(L + a_smw) = m;
  }
  STAMP(6);
  BAR();
  float mall;
  {
    float mv[8];
#pragma unroll
    for (int ww = 0; ww < 8; ++ww) mv[ww] = *(LAS float*)(L + a_smr + ww * 64);
    const float m = fmaxf(fmaxf(fmaxf(mv[0], mv[1]), fmaxf(mv[2], mv[3])), fmaxf(fmaxf(mv[4], mv[5]), fmaxf(mv[6], mv[7])));
    mall = m;
    const float p0 = __builtin_amdgcn_exp2f(sc[0] - m), p1 = __builtin_amdgcn_exp2f(sc[1] - m), p2 = __builtin_amdgcn_exp2f(sc[2] - m), p3 = __builtin_amdgcn_exp2f(sc[3] - m);
    u32x2 pv; pv.x = pack2(p0, p1); pv.y = pack2(p2, p3);
    *(LAS u32x2*)(L + a_pw) = pv;
    float s = (p0 + p1) + (p2 + p3);
    s = x16_sum(s); s = x32_sum(s);
    if (g == 0) *(LAS float*)(L + a_smw + 512) = s;
  }
  for (u32 spin = 0;; ++spin) {
    VMWAIT(0);
    PIN(cf4);
    if (__all(cf4.x == epoch && cf4.y == epoch && cf4.z == epoch && cf4.w == epoch)) break;
    if (spin > SPIN_LIMIT) { fail = true; break; }
    ld128_sc1(cf4, CF_OFF + (u32)(ag * 1024 + l * 16), rs_ws, 0u);
  }
  BAR();
  {
#pragma unroll
    for (int i = 0; i < 2; ++i) { const int row = 8 * w + 4 * i + g; dma16(lbase + L_U + (u32)((8 * w + 4 * i) * 256), (u32)(row * 8192 + ((idx ^ (2 * (row & 7))) * 16)), rs_wuv, (u32)(64 * j * 8192 + h * 256)); }
    const u32 vo_wd = (u32)((l >> 3) * 1024 + (((l & 7) ^ (2 * ((l >> 4) & 3))) * 16));
    dma16(lbase + L_WDN + (u32)(w * 1024), vo_wd, rs_wd, (u32)((uc * 64 + 8 * w) * 1024 + j * 128));
  }
  {
    u32x4 pf4[4], bk[4][4];
    float sl[8];
#pragma unroll
    for (int ks = 0; ks < 4; ++ks) pf4[ks] = lds128(L, a_pr + ks * 1024);
#pragma unroll
    for (int ct = 0; ct < 4; ++ct) {
      bk[ct][0] = trfrag(L, trb[ct], 0, 16384); bk[ct][1] = trfrag(L, trb[ct], 32768, 49152);
      bk[ct][2] = trfrag(L, trb2[ct], 0, 16384); bk[ct][3] = trfrag(L, trb2[ct], 32768, 49152);
    }
#pragma unroll
    for (int ww = 0; ww < 8; ++ww) sl[ww] = *(LAS float*)(L + a_smr + 512 + ww * 64);
    SCHED();
    f32x4 o[4];
#pragma unroll
    for (int ct = 0; ct < 4; ++ct) o[ct] = f32x4{0.f, 0.f, 0.f, 0.f};
#pragma unroll
    for (int ks = 0; ks < 4; ++ks)
#pragma unroll
      for (int ct = 0; ct < 4; ++ct) o[ct] = mfma32(bk[ct][ks], pf4[ks], o[ct]);
    const float ls = ((sl[0] + sl[1]) + (sl[2] + sl[3])) + ((sl[4] + sl[5]) + (sl[6] + sl[7]));
    const float inv = __builtin_amdgcn_rcpf(ls);
    u32 pk[8];
#pragma unroll
    for (int ct = 0; ct < 4; ++ct) { pk[2 * ct] = pack2(o[ct][0] * inv, o[ct][1] * inv); pk[2 * ct + 1] = pack2(o[ct][2] * inv, o[ct][3] * inv); }
#pragma unroll
    for (int i = 0; i < 8; ++i) pk[i] = min((u32)__builtin_amdgcn_ds_bpermute((int)bp_src, (int)pk[i]), SENT - 1u);
    st128_sc1(u32x4{pk[0], pk[1], pk[2], pk[3]}, o_e3w, rs_ws, 0u);
    st128_sc1(u32x4{pk[4], pk[5], pk[6], pk[7]}, o_e3w, rs_ws, 64u);
    if (w == 0) {
      const float lse = mall + __builtin_amdgcn_logf(ls);
      u32x2 v; v.x = __float_as_uint(lse); v.y = epoch;
      if (g == 0) st64_sc1(v, o_stw, rs_ws);
    }
  }
  STAMP(7);
  BAR();
  if (!chain) {
    const int wr = (2 * sw + 2 * j) & 7;
#pragma unroll
    for (int i = 0; i < 32; ++i) {
      const int row = ((32 * sw + 16 * j) & 127) + i;
      const int rr = row & 127;
      dma16_wo(lbase + L_R + (u32)(rr * 1024), dvo[(i) & 7], rs_wo, (u32)((h * 128 + rr) * 1024));
    }
    (void)wr;
  }
  if (chain) {
    const u32 o_efr = EF_OFF + (u32)(uc * 32768 + (tid >> 7) * 16384 + (tid & 127) * 16);
    u32x4 e4[8];
#pragma unroll
    for (int k = 0; k < 8; ++k) e4[k] = u32x4{0u, 0u, 0u, 0u};
    for (u32 spin = 0; ffn_ok; ++spin) {
#pragma unroll
      for (int k = 0; k < 8; ++k) ld128_sc1(e4[k], o_efr, rs_ws, (u32)(k * 2048));
      VMWAIT(0);
#pragma unroll
      for (int k = 0; k < 8; ++k) PIN(e4[k]);
      if (__all(tags_ok(e4, 8, epoch))) break;
      if (spin > SPIN_LIMIT) { fail = true; break; }
    }
    float s0 = 0.f, s1 = 0.f;
#pragma unroll
    for (int k = 0; k < 8; ++k) { s0 += __uint_as_float(e4[k].x); s1 += __uint_as_float(e4[k].z); }
    *(LAS u32x2*)(L + L_HS + tid * 8) = u32x2{__float_as_uint(s0), __float_as_uint(s1)};
  }
  const int lb = w >> 1, lp4 = 4 * (w & 1) + (l & 3), lss = l >> 2;
  u32 o_e3r = E3_OFF + (u32)(((h * 8 + j) * 4 + lb) * 32 * 128 + lss * 128 + lp4 * 16);
  u32 o_str = ST_OFF + (u32)(((h * 4 + lb) * 32 + (l & 31)) * 8);
  u32 a_wuvr = L_U + (u32)((4 * g + q_) * 256) + (u32)(((2 * w + (p_ >> 1)) ^ (8 * (g & 1) + 2 * q_)) * 16) + (u32)(8 * (p_ & 1));
  u32 a_latr = L_LAT + (u32)((g * 4 + (idx & 3)) * 16);
  u32 a_ctxw = L_CTX + (u32)((((w >> 1) * 4 + g) * 4 + (idx & 3)) * 16 + 8 * (w & 1));
  u32 a_ctxr = L_CTX + (u32)((g * 4 + (idx & 3)) * 16);
  u32 o_e5s = E5S_OFF + (u32)(((16 * w + 4 * (idx >> 2) + g) * 256 + bid) * 64 + (idx & 3) * 16);
  u32 a_latw;
  {
    const int e = ((l >> 5) & 1) * 4 + ((l >> 4) & 1) * 2 + ((l >> 3) & 1), k = e >> 1;
    const int c = 16 * (2 * (lp4 >> 2) + (k >> 1)) + 4 * (lp4 & 3) + 2 * (k & 1) + (e & 1);
    a_latw = L_LAT + (u32)(((((c >> 5) * 4 + ((c >> 2) & 3)) * 4 + lb) * 8 + 4 * ((c >> 4) & 1) + (c & 3)) * 2);
  }
  PIN(o_e3r); PIN(o_str); PIN(a_wuvr); PIN(a_latr); PIN(a_ctxw); PIN(a_ctxr); PIN(o_e5s); PIN(a_latw);
  STAMP(8);
  {
    u32x4 e4[2]; u32x2 st;
    for (u32 spin = 0;; ++spin) {
      ld64_sc1(st, o_str, rs_ws, 0u);
      ld128_sc1(e4[0], o_e3r, rs_ws, 0u); ld128_sc1(e4[1], o_e3r, rs_ws, 2048u);
      VMWAIT(0);
      PIN(e4[0]); PIN(e4[1]); PIN(st);
      const u32 mx = max(max(max(e4[0].x, e4[0].y), max(e4[0].z, e4[0].w)), max(max(e4[1].x, e4[1].y), max(e4[1].z, e4[1].w)));
      if (__all(mx != SENT && st.y == epoch)) break;
      if (spin > SPIN_LIMIT) { fail = true; break; }
    }
    STAMP(9);
    const float lse = __uint_as_float(st.x);
    const float M = half_max32(lse);
    const float e = __builtin_amdgcn_exp2f(lse - M);
    const float S = half_sum32(e);
    const float wgt = e * __builtin_amdgcn_rcpf(S);
    const float w0 = __builtin_bit_cast(float, __builtin_amdgcn_ds_bpermute(lss * 4, __builtin_bit_cast(int, wgt)));
    const float w1 = __builtin_bit_cast(float, __builtin_amdgcn_ds_bpermute((16 + lss) * 4, __builtin_bit_cast(int, wgt)));
    float v[8];
    v[0] = fmaf(w1, bf2f_lo(e4[1].x), w0 * bf2f_lo(e4[0].x)); v[1] = fmaf(w1, bf2f_hi(e4[1].x), w0 * bf2f_hi(e4[0].x));
    v[2] = fmaf(w1, bf2f_lo(e4[1].y), w0 * bf2f_lo(e4[0].y)); v[3] = fmaf(w1, bf2f_hi(e4[1].y), w0 * bf2f_hi(e4[0].y));
    v[4] = fmaf(w1, bf2f_lo(e4[1].z), w0 * bf2f_lo(e4[0].z)); v[5] = fmaf(w1, bf2f_hi(e4[1].z), w0 * bf2f_hi(e4[0].z));
    v[6] = fmaf(w1, bf2f_lo(e4[1].w), w0 * bf2f_lo(e4[0].w)); v[7] = fmaf(w1, bf2f_hi(e4[1].w), w0 * bf2f_hi(e4[0].w));
#pragma unroll
    for (int i = 0; i < 4; ++i) { auto s = __builtin_amdgcn_permlane32_swap(__float_as_uint(v[i]), __float_as_uint(v[i + 4]), false, false); v[i] = __uint_as_float(s[0]) + __uint_as_float(s[1]); }
#pragma unroll
    for (int i = 0; i < 2; ++i) { auto s = __builtin_amdgcn_permlane16_swap(__float_as_uint(v[i]), __float_as_uint(v[i + 2]), false, false); v[i] = __uint_as_float(s[0]) + __uint_as_float(s[1]); }
    { const bool b3 = (l & 8) != 0; const float snd = b3 ? v[0] : v[1], keep = b3 ? v[1] : v[0]; v[0] = keep + dpp<XOR8>(snd); }
    {
      int t1 = __builtin_amdgcn_update_dpp(0, __builtin_bit_cast(int, v[0]), 0x114, 0xf, 0xa, false);
      t1 = __builtin_amdgcn_update_dpp(t1, __builtin_bit_cast(int, v[0]), 0x104, 0xf, 0x5, false);
      v[0] += __builtin_bit_cast(float, t1);
    }
    *(LAS u16*)(L + a_latw) = (u16)pack2(v[0], v[0]);
  }
  BAR();
  if (w < 2) {
    const u32x2 g0 = *(LAS u32x2*)(L + L_HS + tid * 8), u0 = *(LAS u32x2*)(L + L_HS + 1024 + tid * 8);
    const float ga = rbf(__uint_as_float(g0.x)), gb = rbf(__uint_as_float(g0.y));
    const float ua = rbf(__uint_as_float(u0.x)), ub = rbf(__uint_as_float(u0.y));
    const float sa = rbf(ga * __builtin_amdgcn_rcpf(1.0f + __expf(-ga))), sb = rbf(gb * __builtin_amdgcn_rcpf(1.0f + __expf(-gb)));
    const int f = 2 * (tid & 31), b = tid >> 5;
    *(LAS u32*)(L + L_HF + (u32)(((((f >> 5) * 4 + ((f >> 2) & 3)) * 4 + b) * 8 + 4 * ((f >> 4) & 1) + (f & 3)) * 2)) = pack2(sa * ua, sb * ub);
  }
  u32x4 wk3[4][4];
  {
    const u32x4 wa0 = trfrag(L, a_wuvr, 0, 4096), wa1 = trfrag(L, a_wuvr, 8192, 12288);
    const u32x4 lb0 = lds128(L, a_latr), lb1 = lds128(L, a_latr + 256);
#pragma unroll
    for (int mt = 0; mt < 4; ++mt) {
      wk3[mt][0] = trfrag(L, trb[mt], 0, 16384); wk3[mt][1] = trfrag(L, trb[mt], 32768, 49152);
      wk3[mt][2] = trfrag(L, trb2[mt], 0, 16384); wk3[mt][3] = trfrag(L, trb2[mt], 32768, 49152);
    }
    f32x4 c = mfma32(wa0, lb0, f32x4{0.f, 0.f, 0.f, 0.f});
    c = mfma32(wa1, lb1, c);
    if (idx < 4) { u32x2 v; v.x = pack2(c[0], c[1]); v.y = pack2(c[2], c[3]); *(LAS u32x2*)(L + a_ctxw) = v; }
  }
  BAR();
  {
    u32x4 cf[4];
#pragma unroll
    for (int ks = 0; ks < 4; ++ks) cf[ks] = lds128(L, a_ctxr + ks * 256);
    f32x4 o[4];
#pragma unroll
    for (int mt = 0; mt < 4; ++mt) o[mt] = f32x4{0.f, 0.f, 0.f, 0.f};
#pragma unroll
    for (int ks = 0; ks < 4; ++ks)
#pragma unroll
      for (int mt = 0; mt < 4; ++mt) o[mt] = mfma32(wk3[mt][ks], cf[ks], o[mt]);
    const int rep = idx >> 2;
    f32x4 v = rep == 0 ? o[0] : rep == 1 ? o[1] : rep == 2 ? o[2] : o[3];
    u32x4 s0;
    s0.x = min(__float_as_uint(v[0]), SENT - 1u); s0.y = min(__float_as_uint(v[1]), SENT - 1u); s0.z = min(__float_as_uint(v[2]), SENT - 1u); s0.w = min(__float_as_uint(v[3]), SENT - 1u);
    st128_sc1(s0, o_e5s, rs_ws, 0u);
  }
  if (w < 4) {
    const int row = 4 * g + q_;
    const u32 a0 = L_WDN + (u32)(row * 128) + (u32)(((2 * w + (p_ >> 1)) ^ (2 * ((row >> 1) & 3))) * 16) + (u32)(8 * (p_ & 1));
    const u32x4 wa0 = trfrag(L, a0, 0, 2048), wa1 = trfrag(L, a0, 4096, 6144);
    const u32x4 hb0 = lds128(L, L_HF + (u32)((g * 4 + (idx & 3)) * 16)), hb1 = lds128(L, L_HF + 256 + (u32)((g * 4 + (idx & 3)) * 16));
    f32x4 y = mfma32(wa0, hb0, f32x4{0.f, 0.f, 0.f, 0.f});
    y = mfma32(wa1, hb1, y);
    const u32 off = E5Y_OFF + (u32)(((16 * j + 4 * w + g) * 32 + uc) * 128 + (idx & 3) * 32);
    if (ffn_ok && idx < 4) {
      st128_sc1(u32x4{__float_as_uint(y[0]), epoch, __float_as_uint(y[1]), epoch}, off, rs_ws, 0u);
      st128_sc1(u32x4{__float_as_uint(y[2]), epoch, __float_as_uint(y[3]), epoch}, off, rs_ws, 16u);
    }
  }
  STAMP(10);
  if (bid < 128) {
    u32x4 a4[2], y4;
    const u32 xfin = ((const u32*)x)[(l & 3) * 256 + bid * 2 + ((l >> 3) & 1)];
    const u32 o_r = E5S_OFF + (u32)(bid * 16384 + tid * 16);
    const u32 o_y = E5Y_OFF + (u32)(bid * 4096 + (tid & 255) * 16);
    const bool yv = tid < 224;
    for (u32 spin = 0;; ++spin) {
      ld128_sc1(y4, o_y, rs_ws, 0u);
      ld128_sc1(a4[0], o_r, rs_ws, 0u); ld128_sc1(a4[1], o_r, rs_ws, 8192u);
      VMWAIT(0);
      PIN(a4[0]); PIN(a4[1]); PIN(y4);
      const u32 mx = max(max(max(a4[0].x, a4[0].y), max(a4[0].z, a4[0].w)), max(max(a4[1].x, a4[1].y), max(a4[1].z, a4[1].w)));
      if (__all(mx != SENT && (!yv || (y4.y == epoch && y4.w == epoch)))) break;
      if (spin > SPIN_LIMIT) { fail = true; break; }
    }
    STAMP(11);
    float v[4];
    v[0] = __uint_as_float(a4[0].x) + __uint_as_float(a4[1].x); v[1] = __uint_as_float(a4[0].y) + __uint_as_float(a4[1].y);
    v[2] = __uint_as_float(a4[0].z) + __uint_as_float(a4[1].z); v[3] = __uint_as_float(a4[0].w) + __uint_as_float(a4[1].w);
#pragma unroll
    for (int i = 0; i < 2; ++i) { auto s = __builtin_amdgcn_permlane32_swap(__float_as_uint(v[i]), __float_as_uint(v[i + 2]), false, false); v[i] = __uint_as_float(s[0]) + __uint_as_float(s[1]); }
    { auto s = __builtin_amdgcn_permlane16_swap(__float_as_uint(v[0]), __float_as_uint(v[1]), false, false); v[0] = __uint_as_float(s[0]) + __uint_as_float(s[1]); }
    v[0] += dpp<0x124>(v[0]); v[0] += dpp<0x128>(v[0]);
    float y0 = yv ? __uint_as_float(y4.x) : 0.f, y1 = yv ? __uint_as_float(y4.z) : 0.f;
    y0 += dpp<XOR8>(y0); y1 += dpp<XOR8>(y1);
    y0 = x16_sum(y0); y1 = x16_sum(y1); y0 = x32_sum(y0); y1 = x32_sum(y1);
    if ((l & 12) == 0) *(LAS float*)(L + L_FIN + (u32)((w * 16 + (l >> 4) * 4 + (l & 3)) * 4)) = v[0];
    if (l < 8 && w < 4) *(LAS u32x2*)(L + L_HS + (u32)((w * 8 + l) * 8)) = u32x2{__float_as_uint(y0), __float_as_uint(y1)};
    BAR();
    if (w == 0) {
      const int mi = (l >> 2) & 3, b = l & 3;
      float sv[8], yv4[4];
#pragma unroll
      for (int ww = 0; ww < 8; ++ww) sv[ww] = *(LAS float*)(L + L_FIN + (u32)((ww * 16 + mi * 4 + b) * 4));
#pragma unroll
      for (int ww = 0; ww < 4; ++ww) yv4[ww] = *(LAS float*)(L + L_HS + (u32)(((ww * 8 + b * 2 + (mi >> 1)) * 2 + (mi & 1)) * 4));
      const float s = ((sv[0] + sv[1]) + (sv[2] + sv[3])) + ((sv[4] + sv[5]) + (sv[6] + sv[7]));
      const float y = (yv4[0] + yv4[1]) + (yv4[2] + yv4[3]);
      const float sb = rbf(s), yb = rbf(y);
      const float gg = rbf(yb * __builtin_amdgcn_rcpf(1.0f + __expf(-yb)));
      const float xv_ = (mi & 1) ? bf2f_hi(xfin) : bf2f_lo(xfin);
      float o = xv_ + rbf(sb * gg);
      if (fail) o = __uint_as_float(0x7fc00000u);
      if (l < 16) out[b * 512 + 4 * bid + mi] = (u16)(pack2(o, 0.f));
      if (bid == 0 && l == 0) st32_sc1(epoch, EPOCH_OFF, rs_ws);
    }
  }
}

extern "C" void kernel_launch(void* const* d_in, const int* in_sizes, int n_in, void* d_out, int out_size, void* d_ws, size_t ws_size, hipStream_t stream) {
  const u16* x = (const u16*)d_in[0]; const u16* ckv = (const u16*)d_in[1]; const u16* wq = (const u16*)d_in[2]; const u16* wuk = (const u16*)d_in[3];
  const u16* wuv = (const u16*)d_in[4]; const u16* wo = (const u16*)d_in[5]; const u16* wg = (const u16*)d_in[7]; const u16* wu = (const u16*)d_in[8]; const u16* wd = (const u16*)d_in[9];
  fused_decode<<<256, 512, 0, stream>>>(x, ckv, wq, wuk, wuv, wo, wg, wu, wd, (u16*)d_out, (unsigned char*)d_ws);
}
```

```cpp
#include <hip/hip_runtime.h>
#include <stdint.h>

typedef unsigned u32;
typedef unsigned short u16;
typedef u32 u32x4 __attribute__((ext_vector_type(4)));
typedef u32 u32x2 __attribute__((ext_vector_type(2)));
typedef float f32x4 __attribute__((ext_vector_type(4)));
typedef short s16x4 __attribute__((ext_vector_type(4)));
typedef __bf16 bf16x8 __attribute__((ext_vector_type(8)));
typedef __bf16 bf16x2 __attribute__((ext_vector_type(2)));
#define LAS __attribute__((address_space(3)))
typedef __amdgpu_buffer_rsrc_t rsrc_t;

#define DIAG 0
#define NT_W 0

constexpr u32 EPOCH_OFF = 0;
constexpr u32 QA_OFF = 4096;
constexpr u32 ST_OFF = QA_OFF + 4 * 32 * 2048;
constexpr u32 CF_OFF = ST_OFF + 32 * 4 * 32 * 8;
constexpr u32 EF_OFF = CF_OFF + 4096;
constexpr u32 E5Y_OFF = EF_OFF + 32 * 32768;
constexpr u32 E3_OFF = E5Y_OFF + 128 * 4096;
constexpr u32 E5S_OFF = E3_OFF + 256 * 16384;
constexpr u32 WS_END = E5S_OFF + 128 * 16384;
constexpr u32 DBG_OFF = WS_END;
constexpr u32 WS_TOTAL = DBG_OFF + 256 * 64 * 8;
constexpr u32 SENT = 0xFFFFFFFFu;

constexpr u32 L_R = 0;
constexpr u32 L_U = 131072;
constexpr u32 L_WDN = L_U + 16384;
constexpr u32 L_M = 155648;
constexpr u32 L_XN = L_M;
constexpr u32 L_P = L_M;
constexpr u32 L_QB = L_M + 4096;
constexpr u32 L_CTX = L_QB;
constexpr u32 L_SM = L_M + 5120;
constexpr u32 L_SL = L_M + 5632;
constexpr u32 L_LAT = L_M + 6144;
constexpr u32 L_HF = L_M + 6656;
constexpr u32 L_FIN = L_M + 7168;
constexpr u32 L_HS = L_M;
constexpr u32 L_FP = L_WDN;
constexpr u32 L_CNT = L_M + 7680;
constexpr u32 L_XF = L_M + 7696;
constexpr u32 LDS_BYTES = 163840;

constexpr u32 SPIN_LIMIT = 1u << 16;
#define KD 6
constexpr int PD = 8;
#define CLR_SLEEP 4
#define CLR_FIRST 4
#define CPRE 10
#define KS 11
#define E2_SLEEP 8

__device__ __forceinline__ float bf2f_lo(u32 v) { return __uint_as_float(v << 16); }
__device__ __forceinline__ float bf2f_hi(u32 v) { return __uint_as_float(v & 0xffff0000u); }
__device__ __forceinline__ u32 pack2(float lo, float hi) { bf16x2 v = {(__bf16)lo, (__bf16)hi}; return __builtin_bit_cast(u32, v); }
__device__ __forceinline__ float rbf(float f) { return __uint_as_float(((u32)__builtin_bit_cast(u16, (__bf16)f)) << 16); }
__device__ __forceinline__ f32x4 mfma32(u32x4 a, u32x4 b, f32x4 c) {
  return __builtin_amdgcn_mfma_f32_16x16x32_bf16(__builtin_bit_cast(bf16x8, a), __builtin_bit_cast(bf16x8, b), c, 0, 0, 0);
}
__device__ __forceinline__ f32x4 mfma16(u32x2 a, u32x2 b, f32x4 c) {
  return __builtin_amdgcn_mfma_f32_16x16x16bf16_1k(__builtin_bit_cast(s16x4, a), __builtin_bit_cast(s16x4, b), c, 0, 0, 0);
}
template <int CTRL> __device__ __forceinline__ float dpp(float x) {
  return __builtin_bit_cast(float, __builtin_amdgcn_mov_dpp(__builtin_bit_cast(int, x), CTRL, 0xf, 0xf, true));
}
constexpr int XOR1 = 0xB1, XOR2 = 0x4E, XOR7 = 0x141, XOR15 = 0x140, XOR8 = 0x128;
__device__ __forceinline__ float x16_sum(float x) { auto s = __builtin_amdgcn_permlane16_swap(__float_as_uint(x), __float_as_uint(x), false, false); return __uint_as_float(s[0]) + __uint_as_float(s[1]); }
__device__ __forceinline__ float x32_sum(float x) { auto s = __builtin_amdgcn_permlane32_swap(__float_as_uint(x), __float_as_uint(x), false, false); return __uint_as_float(s[0]) + __uint_as_float(s[1]); }
__device__ __forceinline__ float x16_max(float x) { auto s = __builtin_amdgcn_permlane16_swap(__float_as_uint(x), __float_as_uint(x), false, false); return fmaxf(__uint_as_float(s[0]), __uint_as_float(s[1])); }
__device__ __forceinline__ float x32_max(float x) { auto s = __builtin_amdgcn_permlane32_swap(__float_as_uint(x), __float_as_uint(x), false, false); return fmaxf(__uint_as_float(s[0]), __uint_as_float(s[1])); }
__device__ __forceinline__ float wave_sum(float v) {
  v += dpp<XOR1>(v); v += dpp<XOR2>(v); v += dpp<XOR7>(v); v += dpp<XOR15>(v); v = x16_sum(v); v = x32_sum(v); return v;
}
__device__ __forceinline__ float half_sum32(float v) {
  v += dpp<XOR1>(v); v += dpp<XOR2>(v); v += dpp<XOR7>(v); v += dpp<XOR15>(v); v = x16_sum(v); return v;
}
__device__ __forceinline__ float half_max32(float v) {
  v = fmaxf(v, dpp<XOR1>(v)); v = fmaxf(v, dpp<XOR2>(v)); v = fmaxf(v, dpp<XOR7>(v)); v = fmaxf(v, dpp<XOR15>(v)); v = x16_max(v); return v;
}

#define BAR() asm volatile("s_waitcnt lgkmcnt(0)\n\ts_barrier" ::: "memory")
#define RAWBAR() asm volatile("s_barrier" ::: "memory")
#define VMWAIT(n) asm volatile("s_waitcnt vmcnt(" #n ")" ::: "memory")
#define SCHED() __builtin_amdgcn_sched_barrier(0)
template <int N> __device__ __forceinline__ void vmwait() { asm volatile("s_waitcnt vmcnt(%0)" :: "i"(N) : "memory"); }
#define PIN(a) asm volatile("" : "+v"(a))

#define NTS ""
__device__ __forceinline__ void ld128(u32x4& d, u32 voff, rsrc_t rs, u32 soff) {
  asm volatile("buffer_load_dwordx4 %0, %1, %2, %3 offen" : "=v"(d) : "v"(voff), "s"(rs), "s"(soff) : "memory");
}
__device__ __forceinline__ void ld128_w(u32x4& d, u32 voff, rsrc_t rs, u32 soff) {
  asm volatile("buffer_load_dwordx4 %0, %1, %2, %3 offen" NTS : "=v"(d) : "v"(voff), "s"(rs), "s"(soff) : "memory");
}
__device__ __forceinline__ void ld128_sc1(u32x4& d, u32 voff, rsrc_t rs, u32 soff) {
  asm volatile("buffer_load_dwordx4 %0, %1, %2, %3 offen sc1" : "=v"(d) : "v"(voff), "s"(rs), "s"(soff) : "memory");
}
__device__ __forceinline__ void ld64_sc1(u32x2& d, u32 voff, rsrc_t rs, u32 soff) {
  asm volatile("buffer_load_dwordx2 %0, %1, %2, %3 offen sc1" : "=v"(d) : "v"(voff), "s"(rs), "s"(soff) : "memory");
}
__device__ __forceinline__ void ld32_sc1(u32& d, u32 voff, rsrc_t rs, u32 soff) {
  asm volatile("buffer_load_dword %0, %1, %2, %3 offen sc1" : "=v"(d) : "v"(voff), "s"(rs), "s"(soff) : "memory");
}
__device__ __forceinline__ void ld32(u32& d, u32 voff, rsrc_t rs, u32 soff) {
  asm volatile("buffer_load_dword %0, %1, %2, %3 offen" : "=v"(d) : "v"(voff), "s"(rs), "s"(soff) : "memory");
}
__device__ __forceinline__ void st128_sc1(u32x4 d, u32 voff, rsrc_t rs, u32 soff) {
  asm volatile("buffer_store_dwordx4 %0, %1, %2, %3 offen sc1\n\ts_nop 1" :: "v"(d), "v"(voff), "s"(rs), "s"(soff) : "memory");
}
__device__ __forceinline__ void st64_sc1(u32x2 d, u32 voff, rsrc_t rs) {
  asm volatile("buffer_store_dwordx2 %0, %1, %2, 0 offen sc1\n\ts_nop 1" :: "v"(d), "v"(voff), "s"(rs) : "memory");
}
__device__ __forceinline__ void st32_sc1(u32 d, u32 voff, rsrc_t rs) {
  asm volatile("buffer_store_dword %0, %1, %2, 0 offen sc1\n\ts_nop 1" :: "v"(d), "v"(voff), "s"(rs) : "memory");
}
__device__ __forceinline__ void dma16(u32 lds_dst, u32 voff, rsrc_t rs, u32 soff) {
  asm volatile("s_mov_b32 m0, %0\n\ts_nop 0\n\tbuffer_load_dwordx4 %1, %2, %3 offen" NTS " lds" :: "s"(lds_dst), "v"(voff), "s"(rs), "s"(soff) : "memory");
}
__device__ __forceinline__ void dma16_def(u32 lds_dst, u32 voff, rsrc_t rs, u32 soff) {
  asm volatile("s_mov_b32 m0, %0\n\ts_nop 0\n\tbuffer_load_dwordx4 %1, %2, %3 offen lds" :: "s"(lds_dst), "v"(voff), "s"(rs), "s"(soff) : "memory");
}
#define KV_NT 1
#define KV_STAG 0
#define WO_NT 1
#define WQ_NT 1
#define dma16_kv dma16
#define dma16_wo dma16
#define ld128_wq ld128_w
__device__ __forceinline__ rsrc_t mk(const void* p, u32 bytes) { return __builtin_amdgcn_make_buffer_rsrc((void*)p, 0, bytes, 0x00020000); }
__device__ __forceinline__ u32x4 lds128(LAS unsigned char* L, u32 a) { return *(LAS u32x4*)(L + a); }
__device__ __forceinline__ u32x4 trfrag(LAS unsigned char* L, u32 a, u32 o0, u32 o1) {
  const s16x4 t0 = __builtin_amdgcn_ds_read_tr16_b64_v4i16((LAS s16x4*)(L + a + o0));
  const s16x4 t1 = __builtin_amdgcn_ds_read_tr16_b64_v4i16((LAS s16x4*)(L + a + o1));
  const u32x2 x = __builtin_bit_cast(u32x2, t0), y = __builtin_bit_cast(u32x2, t1);
  return u32x4{x.x, x.y, y.x, y.y};
}
__device__ __forceinline__ bool tags_ok(const u32x4* v, int n, u32 epoch) {
  u32 a = v[0].y & v[0].w, o = v[0].y | v[0].w;
#pragma unroll
  for (int k = 1; k < n; ++k) { a &= v[k].y & v[k].w; o |= v[k].y | v[k].w; }
  return a == epoch && o == epoch;
}

#define STAMP(i) do {} while (0)

__global__ void __launch_bounds__(576, 3)
fused_decode(const u16* __restrict__ x, const u16* __restrict__ ckv, const u16* __restrict__ wq, const u16* __restrict__ wuk,
             const u16* __restrict__ wuv, const u16* __restrict__ wo, const u16* __restrict__ wg, const u16* __restrict__ wu,
             const u16* __restrict__ wd, u16* __restrict__ out, unsigned char* __restrict__ ws) {
  __shared__ __attribute__((aligned(16))) unsigned char lds[LDS_BYTES];
  LAS unsigned char* L = (LAS unsigned char*)lds;
  const u32 lbase = (u32)(size_t)L;
  const int tid = threadIdx.x;
  const int w = __builtin_amdgcn_readfirstlane(tid >> 6);
  const int l = tid & 63, g = l >> 4, idx = l & 15;
  const int bid = blockIdx.x, xc = bid & 7, r = bid >> 3;
  const int h = xc + 8 * (r >> 3), j = r & 7;
  const int P = xc * 16 + (r & 15), ab = P >> 5, as = P & 31, ag = 1 - (r >> 4);
  const bool ffn_ok = h < 28;
  const int uc = ffn_ok ? h : 27;
  const int fmat = uc & 1, ftile = uc >> 1;
  STAMP(0);

  const rsrc_t rs_x = mk(x, 4096), rs_kv = mk(ckv, 4u * 4096 * 1024), rs_wq = mk(wq, 4u << 20), rs_wuk = mk(wuk, 4u << 20),
               rs_wuv = mk(wuv, 4u << 20), rs_wo = mk(wo, 4u << 20), rs_wg = mk(wg, 512 * 1792 * 2), rs_wu = mk(wu, 512 * 1792 * 2),
               rs_wd = mk(wd, 512 * 1792 * 2), rs_ws = mk(ws, WS_TOTAL);

  const bool chain = w < 4;
  const int sw = w & 3;
  const int q_ = idx >> 2, p_ = idx & 3, jp = idx & 7;
  const int blk2 = (j + 4 + sw) & 7;
  bool fail = false;
  u32 epoch;
  u32 dvo[8];
  u32 a_qir[4], a_kvr[4], trb[4], trb2[4];
  u32 a_smw, a_smr, a_pw, a_pr, o_e3w, bp_src, o_stw;
#define SEL_INIT() u32 sel_lo[4], sel_hi[4]; \
  _Pragma("unroll") for (int i = 0; i < 4; ++i) { const bool on = (i == (jp >> 1)); \
    sel_lo[i] = on ? ((jp & 1) ? 0x01000c0cu : 0x0c0c0100u) : 0x0c0c0c0cu; sel_hi[i] = on ? ((jp & 1) ? 0x03020c0cu : 0x0c0c0302u) : 0x0c0c0c0cu; \
    PIN(sel_lo[i]); PIN(sel_hi[i]); }
#define DIAG_A(p, sel) u32x4{__builtin_amdgcn_perm(p, p, sel[0]), __builtin_amdgcn_perm(p, p, sel[1]), __builtin_amdgcn_perm(p, p, sel[2]), __builtin_amdgcn_perm(p, p, sel[3])}
#define EPOCH_AND_XN() do { \
    asm volatile("s_load_dword %0, %1, 0x0\n\ts_waitcnt lgkmcnt(0)" : "=s"(epoch) : "s"(ws) : "memory"); epoch += 1u; \
    while (__hip_atomic_load((LAS u32*)(L + L_XF), __ATOMIC_RELAXED, __HIP_MEMORY_SCOPE_WORKGROUP) < 4u) __builtin_amdgcn_s_sleep(1); \
    asm volatile("" ::: "memory"); } while (0)
#define DSTEP(acc0, acc1, wf, p0, p1, par) do { const u32* sel_ = (par) ? sel_hi : sel_lo; \
    acc0 = mfma32(DIAG_A(p0, sel_), wf, acc0); acc1 = mfma32(DIAG_A(p1, sel_), wf, acc1); } while (0)
#define LATE_ADDR() do { \
    _Pragma("unroll") for (int c = 0; c < 4; ++c) { const u32 uu = (u32)(4 * c + g); \
      a_qir[c] = L_U + uu * 256 + (((u32)idx ^ uu) * 16); \
      a_kvr[c] = L_R + (u32)((16 * w + idx) * 1024) + ((uu ^ (u32)(2 * (idx & 7))) * 16); \
      const u32 pos = (u32)((8 * w + 2 * c + (p_ >> 1)) ^ (8 * (g & 1) + 2 * q_)); \
      trb[c] = L_R + (u32)((4 * g + q_) * 1024) + pos * 16 + (u32)(8 * (p_ & 1)); trb2[c] = trb[c] + 65536u; \
      PIN(a_qir[c]); PIN(a_kvr[c]); PIN(trb[c]); PIN(trb2[c]); } \
    _Pragma("unroll") for (int i = 0; i < 8; ++i) { dvo[i] = (u32)((l ^ (2 * i)) * 16); PIN(dvo[i]); } \
    a_smw = L_SM + (u32)((w * 16 + idx) * 4); a_smr = L_SM + (u32)(idx * 4); \
    a_pw = L_P + (u32)((((w >> 1) * 4 + g) * 16 + idx) * 16 + 8 * (w & 1)); a_pr = L_P + (u32)(l * 16); \
    o_e3w = E3_OFF + (u32)(((((16 * ag + (l >> 2)) * 8 + w) * 4 + ab) * 32 + as) * 128 + (l & 3) * 16); \
    bp_src = (u32)(((l >> 2) + 16 * (l & 3)) * 4); \
    o_stw = ST_OFF + (u32)((((16 * ag + idx) * 4 + ab) * 32 + as) * 8); \
    PIN(a_smw); PIN(a_smr); PIN(a_pw); PIN(a_pr); PIN(o_e3w); PIN(bp_src); PIN(o_stw); } while (0)
  const u32 a_qpw = L_U + (u32)(((w * 4 + (g >> 1)) * 128 + 8 * idx + 4 * (g & 1)) * 4);

  const int wb = (w + j) & 7;
  const u32 vo_wq = (u32)((64 * wb + 16 * g) * 8192 + h * 256 + idx * 16);
  const u32 a_xn = L_XN + (u32)((idx >> 3) * 1024 + (64 * wb + 16 * g) * 2);
  const u32 kvx = (u32)(KV_STAG * ag * 16384);
  const u32 L_WUK = L_R + 112 * 1024 - kvx;
#define QLOOP(EXTRA) do { \
      const u32x4 xa0 = lds128(L, a_xn), xa1 = lds128(L, a_xn + 16), xb0 = lds128(L, a_xn + 2048), xb1 = lds128(L, a_xn + 2064); \
      f32x4 qa0 = {0.f, 0.f, 0.f, 0.f}, qa1 = {0.f, 0.f, 0.f, 0.f}; \
      QS(0, EXTRA, xa0[0], xb0[0]); QS(1, EXTRA, xa0[0], xb0[0]); QS(2, EXTRA, xa0[1], xb0[1]); QS(3, EXTRA, xa0[1], xb0[1]); \
      QS(4, EXTRA, xa0[2], xb0[2]); QS(5, EXTRA, xa0[2], xb0[2]); QS(6, EXTRA, xa0[3], xb0[3]); QS(7, EXTRA, xa0[3], xb0[3]); \
      QS(8, EXTRA, xa1[0], xb1[0]); QS(9, EXTRA, xa1[0], xb1[0]); QS(10, EXTRA, xa1[1], xb1[1]); QS(11, EXTRA, xa1[1], xb1[1]); \
      QS(12, EXTRA, xa1[2], xb1[2]); QS(13, EXTRA, xa1[2], xb1[2]); QS(14, EXTRA, xa1[3], xb1[3]); QS(15, EXTRA, xa1[3], xb1[3]); \
      *(LAS f32x4*)(L + a_qpw) = qa0; *(LAS f32x4*)(L + a_qpw + 1024) = qa1; } while (0)
#define QS(t, EXTRA, P0, P1) do { vmwait<15 - (t) + (EXTRA)>(); PIN(wqf[t]); DSTEP(qa0, qa1, wqf[t], P0, P1, (t) & 1); } while (0)
#define EPOCH() do { asm volatile("s_load_dword %0, %1, 0x0\n\ts_waitcnt lgkmcnt(0)" : "=s"(epoch) : "s"(ws) : "memory"); epoch += 1u; } while (0)

  if (w == 8) {
    const u32x4 sv = {SENT, SENT, SENT, SENT};
    const u32 oc = E3_OFF + (u32)((h * 8 + j) * 16384 + l * 16);
    const u32 oc2 = E5S_OFF + (u32)((bid >> 1) * 16384 + (bid & 1) * 8192 + l * 16);
#define CLR(i) do { if ((i) < 16) st128_sc1(sv, oc, rs_ws, (u32)((i) * 1024)); else st128_sc1(sv, oc2, rs_ws, (u32)(((i) - 16) * 1024)); } while (0)
    __builtin_amdgcn_s_sleep(CLR_FIRST);
    CLR(0); CLR(1); CLR(2); CLR(3); CLR(4); CLR(5); CLR(6); CLR(7); CLR(8); CLR(9); CLR(10); CLR(11);
    CLR(12); CLR(13); CLR(14); CLR(15); CLR(16); CLR(17); CLR(18); CLR(19); CLR(20); CLR(21); CLR(22); CLR(23);
    EPOCH();
    BAR();
    BAR();
    BAR();
    VMWAIT(0);
    if (l == 0) st64_sc1(u32x2{epoch, epoch}, CF_OFF + (u32)(((h >> 4) * 128 + (h & 15) * 8 + j) * 8), rs_ws);
    return;
  }
  if (chain) {
    u32x4 xv, wqf[16];
    __builtin_amdgcn_s_setprio(3);
    ld128(xv, (u32)(w * 1024 + l * 16), rs_x, 0u);
#pragma unroll
    for (int t = 0; t < CPRE; ++t) ld128_wq(wqf[t], vo_wq, rs_wq, (u32)(t * 8192));
    SEL_INIT();
    const u32 a_qbr = L_QB + (u32)(((idx & 3) * 128 + 8 * g) * 2);
    const u32 o_qa = QA_OFF + (u32)((((idx & 3) * 32 + h) * 256 + (64 * j + 16 * sw + 4 * g) / 2) * 8);
    STAMP(1);
    {
      vmwait<CPRE>();
      PIN(xv);
      float v[8];
      v[0] = bf2f_lo(xv.x); v[1] = bf2f_hi(xv.x); v[2] = bf2f_lo(xv.y); v[3] = bf2f_hi(xv.y);
      v[4] = bf2f_lo(xv.z); v[5] = bf2f_hi(xv.z); v[6] = bf2f_lo(xv.w); v[7] = bf2f_hi(xv.w);
      float s = ((v[0] + v[1]) + (v[2] + v[3])) + ((v[4] + v[5]) + (v[6] + v[7]));
      s = wave_sum(s);
      const float mean = s * (1.f / 512.f);
      float ss = 0.f;
#pragma unroll
      for (int i = 0; i < 8; ++i) { const float d = v[i] - mean; ss = fmaf(d, d, ss); }
      ss = wave_sum(ss);
      const float var = rbf(ss * (1.f / 512.f));
      const float mb = rbf(mean);
      const float den = rbf(__builtin_amdgcn_sqrtf(rbf(var + 1e-5f)));
      const float rd = __builtin_amdgcn_rcpf(den);
      u32x4 o;
      o.x = pack2(rbf(v[0] - mb) * rd, rbf(v[1] - mb) * rd);
      o.y = pack2(rbf(v[2] - mb) * rd, rbf(v[3] - mb) * rd);
      o.z = pack2(rbf(v[4] - mb) * rd, rbf(v[5] - mb) * rd);
      o.w = pack2(rbf(v[6] - mb) * rd, rbf(v[7] - mb) * rd);
      *(LAS u32x4*)(L + L_XN + w * 1024 + l * 16) = o;
    }
#pragma unroll
    for (int t = CPRE; t < 16; ++t) ld128_wq(wqf[t], vo_wq, rs_wq, (u32)(t * 8192));
    EPOCH();
    BAR();
    STAMP(2);
    QLOOP(0);
    STAMP(3);
    BAR();
    u32x4 wuk[4];
    {
      u32x2 pv[8];
#pragma unroll
      for (int ww = 0; ww < 8; ++ww) pv[ww] = *(LAS u32x2*)(L + L_U + tid * 8 + ww * 2048);
      float s0, s1;
      s0 = ((__uint_as_float(pv[0].x) + __uint_as_float(pv[1].x)) + (__uint_as_float(pv[2].x) + __uint_as_float(pv[3].x))) + ((__uint_as_float(pv[4].x) + __uint_as_float(pv[5].x)) + (__uint_as_float(pv[6].x) + __uint_as_float(pv[7].x)));
      s1 = ((__uint_as_float(pv[0].y) + __uint_as_float(pv[1].y)) + (__uint_as_float(pv[2].y) + __uint_as_float(pv[3].y))) + ((__uint_as_float(pv[4].y) + __uint_as_float(pv[5].y)) + (__uint_as_float(pv[6].y) + __uint_as_float(pv[7].y)));
      *(LAS u32*)(L + L_QB + tid * 4) = pack2(s0, s1);
    }
    BAR();
    {
      u32x4 bq[4];
#pragma unroll
      for (int kk = 0; kk < 4; ++kk) bq[kk] = lds128(L, a_qbr + kk * 64);
#pragma unroll
      for (int kk = 0; kk < 4; ++kk) wuk[kk] = lds128(L, L_WUK + (u32)((16 * sw + idx) * 256) + (u32)(((4 * kk + g) ^ (2 * (idx & 7))) * 16));
      f32x4 acc = {0.f, 0.f, 0.f, 0.f}, acc2 = {0.f, 0.f, 0.f, 0.f};
      acc = mfma32(wuk[0], bq[0], acc); acc2 = mfma32(wuk[1], bq[1], acc2);
      acc = mfma32(wuk[2], bq[2], acc); acc2 = mfma32(wuk[3], bq[3], acc2);
      constexpr float SC = 0.08838834764831845f * 1.4426950408889634f;
      u32x4 o; o.x = pack2((acc[0] + acc2[0]) * SC, (acc[1] + acc2[1]) * SC); o.y = epoch; o.z = pack2((acc[2] + acc2[2]) * SC, (acc[3] + acc2[3]) * SC); o.w = epoch;
      if (idx < 4) st128_sc1(o, o_qa, rs_ws, 0u);
    }
    STAMP(4);
    BAR();
    {
      const int t = tid;
      const u32 o_efw = EF_OFF + (u32)((2 * ftile + ((t & 63) >> 5)) * 32768 + ((fmat * 8 + j) * 4 + (t >> 6)) * 512 + (t & 31) * 16);
      u32x2 pv[4];
#pragma unroll
      for (int ww = 0; ww < 4; ++ww) pv[ww] = *(LAS u32x2*)(L + L_FP + t * 8 + ww * 2048);
      const float s0 = (__uint_as_float(pv[0].x) + __uint_as_float(pv[1].x)) + (__uint_as_float(pv[2].x) + __uint_as_float(pv[3].x));
      const float s1 = (__uint_as_float(pv[0].y) + __uint_as_float(pv[1].y)) + (__uint_as_float(pv[2].y) + __uint_as_float(pv[3].y));
      if (ffn_ok) st128_sc1(u32x4{__float_as_uint(s0), epoch, __float_as_uint(s1), epoch}, o_efw, rs_ws, 0u);
    }
    LATE_ADDR();
    const u32 o_e2 = QA_OFF + (u32)((ab * 32 + 16 * ag) * 2048 + w * 8192 + l * 16);
    u32 a_qiw[4];
#pragma unroll
    for (int k2 = 0; k2 < 4; ++k2) { const u32 uu = (u32)(l >> 1); a_qiw[k2] = L_U + uu * 256 + (((u32)(4 * w + k2) ^ (uu & 15)) * 16) + (u32)(8 * (l & 1)); PIN(a_qiw[k2]); }
    __builtin_amdgcn_s_sleep(E2_SLEEP);
    STAMP(13);
    u32x4 q4[8];
    for (u32 spin = 0;; ++spin) {
#pragma unroll
      for (int k = 0; k < 8; ++k) ld128_sc1(q4[k], o_e2, rs_ws, (u32)(k * 1024));
      VMWAIT(0);
#pragma unroll
      for (int k = 0; k < 8; ++k) PIN(q4[k]);
      if (__all(tags_ok(q4, 8, epoch))) break;
      if (spin > SPIN_LIMIT) { fail = true; break; }
    }
    STAMP(5);
#pragma unroll
    for (int k = 0; k < 8; ++k) {
      *(LAS u32*)(L + a_qiw[k >> 1] + (k & 1) * 8192) = q4[k].x;
      *(LAS u32*)(L + a_qiw[k >> 1] + (k & 1) * 8192 + 4) = q4[k].z;
    }
  } else {
    u32x4 wqf[16], f1w[4];
#pragma unroll
    for (int t = 0; t < 16; ++t) ld128_wq(wqf[t], vo_wq, rs_wq, (u32)(t * 8192));
    const u32 kvrow0 = (u32)((ab * 4096 + as * 128 + 32 * sw) * 1024);
#pragma unroll
    for (int i = 0; i < 8; ++i) { dvo[i] = (u32)((l ^ (2 * i)) * 16); PIN(dvo[i]); }
#define KV(i) dma16_kv(lbase + L_R + (u32)(32 * sw * 1024) + ((u32)((i) * 1024) ^ kvx), dvo[(i) & 7], rs_kv, kvrow0 + ((u32)((i) * 1024) ^ kvx))
    SEL_INIT();
    const u32 a_fx = L_XN + (u32)((idx >> 3) * 1024 + (64 * j + 16 * sw + 4 * g) * 2);
    const u32 vo3 = (u32)((64 * j + 16 * sw + 4 * g) * 3584 + ftile * 256 + idx * 16);
    const rsrc_t rs_f1 = fmat ? rs_wu : rs_wg;
    EPOCH();
    BAR();
#pragma unroll
    for (int i = 0; i < 4; ++i) { const int row = 16 * sw + 4 * i + g;
      dma16(lbase + L_WUK + (u32)((16 * sw + 4 * i) * 256), (u32)(row * 8192 + ((idx ^ (2 * (row & 7))) * 16)), rs_wuk, (u32)(64 * j * 8192 + h * 256)); }
#pragma unroll
    for (int t = 0; t < 4; ++t) ld128_w(f1w[t], vo3, rs_f1, (u32)(t * 3584));
    KV(0); KV(1); KV(2); KV(3);
    STAMP(2);
    const u32x2 fx0 = *(LAS u32x2*)(L + a_fx), fx1 = *(LAS u32x2*)(L + a_fx + 2048);
    QLOOP(12);
    STAMP(3);
    BAR();
    vmwait<8>();
    BAR();
    {
      vmwait<4>();
      PIN(f1w[0]); PIN(f1w[1]); PIN(f1w[2]); PIN(f1w[3]);
      f32x4 f0a = {0.f, 0.f, 0.f, 0.f}, f1a = {0.f, 0.f, 0.f, 0.f};
      DSTEP(f0a, f1a, f1w[0], fx0.x, fx1.x, 0); DSTEP(f0a, f1a, f1w[1], fx0.x, fx1.x, 1);
      DSTEP(f0a, f1a, f1w[2], fx0.y, fx1.y, 0); DSTEP(f0a, f1a, f1w[3], fx0.y, fx1.y, 1);
      const u32 a_fpw = L_FP + (u32)(((sw * 4 + (g >> 1)) * 128 + 8 * idx + 4 * (g & 1)) * 4);
      *(LAS f32x4*)(L + a_fpw) = f0a;
      *(LAS f32x4*)(L + a_fpw + 1024) = f1a;
    }
    BAR();
    KV(4); KV(5); KV(6); KV(7); KV(8); KV(9); KV(10); KV(11);
    LATE_ADDR();
    vmwait<KS>(); KV(12); vmwait<KS>(); KV(13); vmwait<KS>(); KV(14); vmwait<KS>(); KV(15);
    vmwait<KS>(); KV(16); vmwait<KS>(); KV(17); vmwait<KS>(); KV(18); vmwait<KS>(); KV(19); vmwait<KS>(); KV(20);
    vmwait<KS>(); KV(21); vmwait<KS>(); KV(22); vmwait<KS>(); KV(23); vmwait<KS>(); KV(24);
    vmwait<KS>(); KV(25); vmwait<KS>(); KV(26); vmwait<KS>(); KV(27); vmwait<KS>(); KV(28);
    vmwait<KS>(); KV(29); vmwait<KS>(); KV(30); vmwait<KS>(); KV(31);
    VMWAIT(0);
  }
  BAR();
  u32x4 bq[16];
#pragma unroll
  for (int kk = 0; kk < 16; ++kk) bq[kk] = lds128(L, a_qir[kk & 3] + (kk >> 2) * 4096);
  u32x4 cf4;
  ld128_sc1(cf4, CF_OFF + (u32)(ag * 1024 + l * 16), rs_ws, 0u);
  f32x4 sc;
  {
    u32x4 ak[12];
#pragma unroll
    for (int kk = 0; kk < 12; ++kk) ak[kk] = lds128(L, a_kvr[kk & 3] + (kk >> 2) * 256);
    SCHED();
    f32x4 s0 = {0.f, 0.f, 0.f, 0.f}, s1 = {0.f, 0.f, 0.f, 0.f};
#pragma unroll
    for (int kk = 0; kk < 4; kk += 2) { s0 = mfma32(ak[kk], bq[kk], s0); s1 = mfma32(ak[kk + 1], bq[kk + 1], s1); }
    SCHED();
    u32x4 ak2[4];
#pragma unroll
    for (int kk = 0; kk < 4; ++kk) ak2[kk] = lds128(L, a_kvr[kk & 3] + 3 * 256);
    SCHED();
#pragma unroll
    for (int kk = 4; kk < 12; kk += 2) { s0 = mfma32(ak[kk], bq[kk], s0); s1 = mfma32(ak[kk + 1], bq[kk + 1], s1); }
#pragma unroll
    for (int kk = 0; kk < 4; kk += 2) { s0 = mfma32(ak2[kk], bq[12 + kk], s0); s1 = mfma32(ak2[kk + 1], bq[13 + kk], s1); }
    sc = s0 + s1;
    float m = fmaxf(fmaxf(sc[0], sc[1]), fmaxf(sc[2], sc[3]));
    m = x16_max(m); m = x32_max(m);
    if (g == 0) *(LAS float*)(L + a_smw) = m;
  }
  STAMP(6);
  BAR();
  float mall;
  {
    float mv[8];
#pragma unroll
    for (int ww = 0; ww < 8; ++ww) mv[ww] = *(LAS float*)(L + a_smr + ww * 64);
    const float m = fmaxf(fmaxf(fmaxf(mv[0], mv[1]), fmaxf(mv[2], mv[3])), fmaxf(fmaxf(mv[4], mv[5]), fmaxf(mv[6], mv[7])));
    mall = m;
    const float p0 = __builtin_amdgcn_exp2f(sc[0] - m), p1 = __builtin_amdgcn_exp2f(sc[1] - m), p2 = __builtin_amdgcn_exp2f(sc[2] - m), p3 = __builtin_amdgcn_exp2f(sc[3] - m);
    u32x2 pv; pv.x = pack2(p0, p1); pv.y = pack2(p2, p3);
    *(LAS u32x2*)(L + a_pw) = pv;
    float s = (p0 + p1) + (p2 + p3);
    s = x16_sum(s); s = x32_sum(s);
    if (g == 0) *(LAS float*)(L + a_smw + 512) = s;
  }
  for (u32 spin = 0;; ++spin) {
    VMWAIT(0);
    PIN(cf4);
    if (__all(cf4.x == epoch && cf4.y == epoch && cf4.z == epoch && cf4.w == epoch)) break;
    if (spin > SPIN_LIMIT) { fail = true; break; }
    ld128_sc1(cf4, CF_OFF + (u32)(ag * 1024 + l * 16), rs_ws, 0u);
  }
  BAR();
  {
#pragma unroll
    for (int i = 0; i < 2; ++i) { const int row = 8 * w + 4 * i + g; dma16(lbase + L_U + (u32)((8 * w + 4 * i) * 256), (u32)(row * 8192 + ((idx ^ (2 * (row & 7))) * 16)), rs_wuv, (u32)(64 * j * 8192 + h * 256)); }
    const u32 vo_wd = (u32)((l >> 3) * 1024 + (((l & 7) ^ (2 * ((l >> 4) & 3))) * 16));
    dma16(lbase + L_WDN + (u32)(w * 1024), vo_wd, rs_wd, (u32)((uc * 64 + 8 * w) * 1024 + j * 128));
  }
  {
    u32x4 pf4[4], bk[4][4];
    float sl[8];
#pragma unroll
    for (int ks = 0; ks < 4; ++ks) pf4[ks] = lds128(L, a_pr + ks * 1024);
#pragma unroll
    for (int ct = 0; ct < 4; ++ct) {
      bk[ct][0] = trfrag(L, trb[ct], 0, 16384); bk[ct][1] = trfrag(L, trb[ct], 32768, 49152);
      bk[ct][2] = trfrag(L, trb2[ct], 0, 16384); bk[ct][3] = trfrag(L, trb2[ct], 32768, 49152);
    }
#pragma unroll
    for (int ww = 0; ww < 8; ++ww) sl[ww] = *(LAS float*)(L + a_smr + 512 + ww * 64);
    SCHED();
    f32x4 o[4];
#pragma unroll
    for (int ct = 0; ct < 4; ++ct) o[ct] = f32x4{0.f, 0.f, 0.f, 0.f};
#pragma unroll
    for (int ks = 0; ks < 4; ++ks)
#pragma unroll
      for (int ct = 0; ct < 4; ++ct) o[ct] = mfma32(bk[ct][ks], pf4[ks], o[ct]);
    const float ls = ((sl[0] + sl[1]) + (sl[2] + sl[3])) + ((sl[4] + sl[5]) + (sl[6] + sl[7]));
    const float inv = __builtin_amdgcn_rcpf(ls);
    u32 pk[8];
#pragma unroll
    for (int ct = 0; ct < 4; ++ct) { pk[2 * ct] = pack2(o[ct][0] * inv, o[ct][1] * inv); pk[2 * ct + 1] = pack2(o[ct][2] * inv, o[ct][3] * inv); }
#pragma unroll
    for (int i = 0; i < 8; ++i) pk[i] = min((u32)__builtin_amdgcn_ds_bpermute((int)bp_src, (int)pk[i]), SENT - 1u);
    st128_sc1(u32x4{pk[0], pk[1], pk[2], pk[3]}, o_e3w, rs_ws, 0u);
    st128_sc1(u32x4{pk[4], pk[5], pk[6], pk[7]}, o_e3w, rs_ws, 64u);
    if (w == 0) {
      const float lse = mall + __builtin_amdgcn_logf(ls);
      u32x2 v; v.x = __float_as_uint(lse); v.y = epoch;
      if (g == 0) st64_sc1(v, o_stw, rs_ws);
    }
  }
  STAMP(7);
  BAR();
  if (!chain) {
    const int wr = (2 * sw + 2 * j) & 7;
#pragma unroll
    for (int i = 0; i < 32; ++i) {
      const int row = ((32 * sw + 16 * j) & 127) + i;
      const int rr = row & 127;
      dma16_wo(lbase + L_R + (u32)(rr * 1024), dvo[(i) & 7], rs_wo, (u32)((h * 128 + rr) * 1024));
    }
    (void)wr;
  }
  if (chain) {
    const u32 o_efr = EF_OFF + (u32)(uc * 32768 + (tid >> 7) * 16384 + (tid & 127) * 16);
    u32x4 e4[8];
#pragma unroll
    for (int k = 0; k < 8; ++k) e4[k] = u32x4{0u, 0u, 0u, 0u};
    for (u32 spin = 0; ffn_ok; ++spin) {
#pragma unroll
      for (int k = 0; k < 8; ++k) ld128_sc1(e4[k], o_efr, rs_ws, (u32)(k * 2048));
      VMWAIT(0);
#pragma unroll
      for (int k = 0; k < 8; ++k) PIN(e4[k]);
      if (__all(tags_ok(e4, 8, epoch))) break;
      if (spin > SPIN_LIMIT) { fail = true; break; }
    }
    float s0 = 0.f, s1 = 0.f;
#pragma unroll
    for (int k = 0; k < 8; ++k) { s0 += __uint_as_float(e4[k].x); s1 += __uint_as_float(e4[k].z); }
    *(LAS u32x2*)(L + L_HS + tid * 8) = u32x2{__float_as_uint(s0), __float_as_uint(s1)};
  }
  const int lb = w >> 1, lp4 = 4 * (w & 1) + (l & 3), lss = l >> 2;
  u32 o_e3r = E3_OFF + (u32)(((h * 8 + j) * 4 + lb) * 32 * 128 + lss * 128 + lp4 * 16);
  u32 o_str = ST_OFF + (u32)(((h * 4 + lb) * 32 + (l & 31)) * 8);
  u32 a_wuvr = L_U + (u32)((4 * g + q_) * 256) + (u32)(((2 * w + (p_ >> 1)) ^ (8 * (g & 1) + 2 * q_)) * 16) + (u32)(8 * (p_ & 1));
  u32 a_latr = L_LAT + (u32)((g * 4 + (idx & 3)) * 16);
  u32 a_ctxw = L_CTX + (u32)((((w >> 1) * 4 + g) * 4 + (idx & 3)) * 16 + 8 * (w & 1));
  u32 a_ctxr = L_CTX + (u32)((g * 4 + (idx & 3)) * 16);
  u32 o_e5s = E5S_OFF + (u32)(((16 * w + 4 * (idx >> 2) + g) * 256 + bid) * 64 + (idx & 3) * 16);
  u32 a_latw;
  {
    const int e = ((l >> 5) & 1) * 4 + ((l >> 4) & 1) * 2 + ((l >> 3) & 1), k = e >> 1;
    const int c = 16 * (2 * (lp4 >> 2) + (k >> 1)) + 4 * (lp4 & 3) + 2 * (k & 1) + (e & 1);
    a_latw = L_LAT + (u32)(((((c >> 5) * 4 + ((c >> 2) & 3)) * 4 + lb) * 8 + 4 * ((c >> 4) & 1) + (c & 3)) * 2);
  }
  PIN(o_e3r); PIN(o_str); PIN(a_wuvr); PIN(a_latr); PIN(a_ctxw); PIN(a_ctxr); PIN(o_e5s); PIN(a_latw);
  STAMP(8);
  {
    u32x4 e4[2]; u32x2 st;
    for (u32 spin = 0;; ++spin) {
      ld64_sc1(st, o_str, rs_ws, 0u);
      ld128_sc1(e4[0], o_e3r, rs_ws, 0u); ld128_sc1(e4[1], o_e3r, rs_ws, 2048u);
      VMWAIT(0);
      PIN(e4[0]); PIN(e4[1]); PIN(st);
      const u32 mx = max(max(max(e4[0].x, e4[0].y), max(e4[0].z, e4[0].w)), max(max(e4[1].x, e4[1].y), max(e4[1].z, e4[1].w)));
      if (__all(mx != SENT && st.y == epoch)) break;
      if (spin > SPIN_LIMIT) { fail = true; break; }
    }
    STAMP(9);
    const float lse = __uint_as_float(st.x);
    const float M = half_max32(lse);
    const float e = __builtin_amdgcn_exp2f(lse - M);
    const float S = half_sum32(e);
    const float wgt = e * __builtin_amdgcn_rcpf(S);
    const float w0 = __builtin_bit_cast(float, __builtin_amdgcn_ds_bpermute(lss * 4, __builtin_bit_cast(int, wgt)));
    const float w1 = __builtin_bit_cast(float, __builtin_amdgcn_ds_bpermute((16 + lss) * 4, __builtin_bit_cast(int, wgt)));
    float v[8];
    v[0] = fmaf(w1, bf2f_lo(e4[1].x), w0 * bf2f_lo(e4[0].x)); v[1] = fmaf(w1, bf2f_hi(e4[1].x), w0 * bf2f_hi(e4[0].x));
    v[2] = fmaf(w1, bf2f_lo(e4[1].y), w0 * bf2f_lo(e4[0].y)); v[3] = fmaf(w1, bf2f_hi(e4[1].y), w0 * bf2f_hi(e4[0].y));
    v[4] = fmaf(w1, bf2f_lo(e4[1].z), w0 * bf2f_lo(e4[0].z)); v[5] = fmaf(w1, bf2f_hi(e4[1].z), w0 * bf2f_hi(e4[0].z));
    v[6] = fmaf(w1, bf2f_lo(e4[1].w), w0 * bf2f_lo(e4[0].w)); v[7] = fmaf(w1, bf2f_hi(e4[1].w), w0 * bf2f_hi(e4[0].w));
#pragma unroll
    for (int i = 0; i < 4; ++i) { auto s = __builtin_amdgcn_permlane32_swap(__float_as_uint(v[i]), __float_as_uint(v[i + 4]), false, false); v[i] = __uint_as_float(s[0]) + __uint_as_float(s[1]); }
#pragma unroll
    for (int i = 0; i < 2; ++i) { auto s = __builtin_amdgcn_permlane16_swap(__float_as_uint(v[i]), __float_as_uint(v[i + 2]), false, false); v[i] = __uint_as_float(s[0]) + __uint_as_float(s[1]); }
    { const bool b3 = (l & 8) != 0; const float snd = b3 ? v[0] : v[1], keep = b3 ? v[1] : v[0]; v[0] = keep + dpp<XOR8>(snd); }
    {
      int t1 = __builtin_amdgcn_update_dpp(0, __builtin_bit_cast(int, v[0]), 0x114, 0xf, 0xa, false);
      t1 = __builtin_amdgcn_update_dpp(t1, __builtin_bit_cast(int, v[0]), 0x104, 0xf, 0x5, false);
      v[0] += __builtin_bit_cast(float, t1);
    }
    *(LAS u16*)(L + a_latw) = (u16)pack2(v[0], v[0]);
  }
  BAR();
  if (w < 2) {
    const u32x2 g0 = *(LAS u32x2*)(L + L_HS + tid * 8), u0 = *(LAS u32x2*)(L + L_HS + 1024 + tid * 8);
    const float ga = rbf(__uint_as_float(g0.x)), gb = rbf(__uint_as_float(g0.y));
    const float ua = rbf(__uint_as_float(u0.x)), ub = rbf(__uint_as_float(u0.y));
    const float sa = rbf(ga * __builtin_amdgcn_rcpf(1.0f + __expf(-ga))), sb = rbf(gb * __builtin_amdgcn_rcpf(1.0f + __expf(-gb)));
    const int f = 2 * (tid & 31), b = tid >> 5;
    *(LAS u32*)(L + L_HF + (u32)(((((f >> 5) * 4 + ((f >> 2) & 3)) * 4 + b) * 8 + 4 * ((f >> 4) & 1) + (f & 3)) * 2)) = pack2(sa * ua, sb * ub);
  }
  u32x4 wk3[4][4];
  {
    const u32x4 wa0 = trfrag(L, a_wuvr, 0, 4096), wa1 = trfrag(L, a_wuvr, 8192, 12288);
    const u32x4 lb0 = lds128(L, a_latr), lb1 = lds128(L, a_latr + 256);
#pragma unroll
    for (int mt = 0; mt < 4; ++mt) {
      wk3[mt][0] = trfrag(L, trb[mt], 0, 16384); wk3[mt][1] = trfrag(L, trb[mt], 32768, 49152);
      wk3[mt][2] = trfrag(L, trb2[mt], 0, 16384); wk3[mt][3] = trfrag(L, trb2[mt], 32768, 49152);
    }
    f32x4 c = mfma32(wa0, lb0, f32x4{0.f, 0.f, 0.f, 0.f});
    c = mfma32(wa1, lb1, c);
    if (idx < 4) { u32x2 v; v.x = pack2(c[0], c[1]); v.y = pack2(c[2], c[3]); *(LAS u32x2*)(L + a_ctxw) = v; }
  }
  BAR();
  {
    u32x4 cf[4];
#pragma unroll
    for (int ks = 0; ks < 4; ++ks) cf[ks] = lds128(L, a_ctxr + ks * 256);
    f32x4 o[4];
#pragma unroll
    for (int mt = 0; mt < 4; ++mt) o[mt] = f32x4{0.f, 0.f, 0.f, 0.f};
#pragma unroll
    for (int ks = 0; ks < 4; ++ks)
#pragma unroll
      for (int mt = 0; mt < 4; ++mt) o[mt] = mfma32(wk3[mt][ks], cf[ks], o[mt]);
    const int rep = idx >> 2;
    f32x4 v = rep == 0 ? o[0] : rep == 1 ? o[1] : rep == 2 ? o[2] : o[3];
    u32x4 s0;
    s0.x = min(__float_as_uint(v[0]), SENT - 1u); s0.y = min(__float_as_uint(v[1]), SENT - 1u); s0.z = min(__float_as_uint(v[2]), SENT - 1u); s0.w = min(__float_as_uint(v[3]), SENT - 1u);
    st128_sc1(s0, o_e5s, rs_ws, 0u);
  }
  if (w < 4) {
    const int row = 4 * g + q_;
    const u32 a0 = L_WDN + (u32)(row * 128) + (u32)(((2 * w + (p_ >> 1)) ^ (2 * ((row >> 1) & 3))) * 16) + (u32)(8 * (p_ & 1));
    const u32x4 wa0 = trfrag(L, a0, 0, 2048), wa1 = trfrag(L, a0, 4096, 6144);
    const u32x4 hb0 = lds128(L, L_HF + (u32)((g * 4 + (idx & 3)) * 16)), hb1 = lds128(L, L_HF + 256 + (u32)((g * 4 + (idx & 3)) * 16));
    f32x4 y = mfma32(wa0, hb0, f32x4{0.f, 0.f, 0.f, 0.f});
    y = mfma32(wa1, hb1, y);
    const u32 off = E5Y_OFF + (u32)(((16 * j + 4 * w + g) * 32 + uc) * 128 + (idx & 3) * 32);
    if (ffn_ok && idx < 4) {
      st128_sc1(u32x4{__float_as_uint(y[0]), epoch, __float_as_uint(y[1]), epoch}, off, rs_ws, 0u);
      st128_sc1(u32x4{__float_as_uint(y[2]), epoch, __float_as_uint(y[3]), epoch}, off, rs_ws, 16u);
    }
  }
  STAMP(10);
  if (bid < 128) {
    u32x4 a4[2], y4;
    const u32 xfin = ((const u32*)x)[(l & 3) * 256 + bid * 2 + ((l >> 3) & 1)];
    const u32 o_r = E5S_OFF + (u32)(bid * 16384 + tid * 16);
    const u32 o_y = E5Y_OFF + (u32)(bid * 4096 + (tid & 255) * 16);
    const bool yv = tid < 224;
    for (u32 spin = 0;; ++spin) {
      ld128_sc1(y4, o_y, rs_ws, 0u);
      ld128_sc1(a4[0], o_r, rs_ws, 0u); ld128_sc1(a4[1], o_r, rs_ws, 8192u);
      VMWAIT(0);
      PIN(a4[0]); PIN(a4[1]); PIN(y4);
      const u32 mx = max(max(max(a4[0].x, a4[0].y), max(a4[0].z, a4[0].w)), max(max(a4[1].x, a4[1].y), max(a4[1].z, a4[1].w)));
      if (__all(mx != SENT && (!yv || (y4.y == epoch && y4.w == epoch)))) break;
      if (spin > SPIN_LIMIT) { fail = true; break; }
    }
    STAMP(11);
    float v[4];
    v[0] = __uint_as_float(a4[0].x) + __uint_as_float(a4[1].x); v[1] = __uint_as_float(a4[0].y) + __uint_as_float(a4[1].y);
    v[2] = __uint_as_float(a4[0].z) + __uint_as_float(a4[1].z); v[3] = __uint_as_float(a4[0].w) + __uint_as_float(a4[1].w);
#pragma unroll
    for (int i = 0; i < 2; ++i) { auto s = __builtin_amdgcn_permlane32_swap(__float_as_uint(v[i]), __float_as_uint(v[i + 2]), false, false); v[i] = __uint_as_float(s[0]) + __uint_as_float(s[1]); }
    { auto s = __builtin_amdgcn_permlane16_swap(__float_as_uint(v[0]), __float_as_uint(v[1]), false, false); v[0] = __uint_as_float(s[0]) + __uint_as_float(s[1]); }
    v[0] += dpp<0x124>(v[0]); v[0] += dpp<0x128>(v[0]);
    float y0 = yv ? __uint_as_float(y4.x) : 0.f, y1 = yv ? __uint_as_float(y4.z) : 0.f;
    y0 += dpp<XOR8>(y0); y1 += dpp<XOR8>(y1);
    y0 = x16_sum(y0); y1 = x16_sum(y1); y0 = x32_sum(y0); y1 = x32_sum(y1);
    if ((l & 12) == 0) *(LAS float*)(L + L_FIN + (u32)((w * 16 + (l >> 4) * 4 + (l & 3)) * 4)) = v[0];
    if (l < 8 && w < 4) *(LAS u32x2*)(L + L_HS + (u32)((w * 8 + l) * 8)) = u32x2{__float_as_uint(y0), __float_as_uint(y1)};
    BAR();
    if (w == 0) {
      const int mi = (l >> 2) & 3, b = l & 3;
      float sv[8], yv4[4];
#pragma unroll
      for (int ww = 0; ww < 8; ++ww) sv[ww] = *(LAS float*)(L + L_FIN + (u32)((ww * 16 + mi * 4 + b) * 4));
#pragma unroll
      for (int ww = 0; ww < 4; ++ww) yv4[ww] = *(LAS float*)(L + L_HS + (u32)(((ww * 8 + b * 2 + (mi >> 1)) * 2 + (mi & 1)) * 4));
      const float s = ((sv[0] + sv[1]) + (sv[2] + sv[3])) + ((sv[4] + sv[5]) + (sv[6] + sv[7]));
      const float y = (yv4[0] + yv4[1]) + (yv4[2] + yv4[3]);
      const float sb = rbf(s), yb = rbf(y);
      const float gg = rbf(yb * __builtin_amdgcn_rcpf(1.0f + __expf(-yb)));
      const float xv_ = (mi & 1) ? bf2f_hi(xfin) : bf2f_lo(xfin);
      float o = xv_ + rbf(sb * gg);
      if (fail) o = __uint_as_float(0x7fc00000u);
      if (l < 16) out[b * 512 + 4 * bid + mi] = (u16)(pack2(o, 0.f));
      if (bid == 0 && l == 0) st32_sc1(epoch, EPOCH_OFF, rs_ws);
    }
  }
}

extern "C" void kernel_launch(void* const* d_in, const int* in_sizes, int n_in, void* d_out, int out_size, void* d_ws, size_t ws_size, hipStream_t stream) {
  const u16* x = (const u16*)d_in[0]; const u16* ckv = (const u16*)d_in[1]; const u16* wq = (const u16*)d_in[2]; const u16* wuk = (const u16*)d_in[3];
  const u16* wuv = (const u16*)d_in[4]; const u16* wo = (const u16*)d_in[5]; const u16* wg = (const u16*)d_in[7]; const u16* wu = (const u16*)d_in[8]; const u16* wd = (const u16*)d_in[9];
  fused_decode<<<256, 576, 0, stream>>>(x, ckv, wq, wuk, wuv, wo, wg, wu, wd, (u16*)d_out, (unsigned char*)d_ws);
}
```

```cpp
#include <hip/hip_runtime.h>
#include <stdint.h>

typedef unsigned u32;
typedef unsigned short u16;
typedef u32 u32x4 __attribute__((ext_vector_type(4)));
typedef u32 u32x2 __attribute__((ext_vector_type(2)));
typedef float f32x4 __attribute__((ext_vector_type(4)));
typedef short s16x4 __attribute__((ext_vector_type(4)));
typedef __bf16 bf16x8 __attribute__((ext_vector_type(8)));
typedef __bf16 bf16x2 __attribute__((ext_vector_type(2)));
#define LAS __attribute__((address_space(3)))
typedef __amdgpu_buffer_rsrc_t rsrc_t;

#define DIAG 0
#define NT_W 0

constexpr u32 EPOCH_OFF = 0;
constexpr u32 QA_OFF = 4096;
constexpr u32 ST_OFF = QA_OFF + 4 * 32 * 2048;
constexpr u32 CF_OFF = ST_OFF + 32 * 4 * 32 * 8;
constexpr u32 EF_OFF = CF_OFF + 4096;
constexpr u32 E5Y_OFF = EF_OFF + 32 * 32768;
constexpr u32 E3_OFF = E5Y_OFF + 128 * 4096;
constexpr u32 E5S_OFF = E3_OFF + 256 * 32768;
constexpr u32 WS_END = E5S_OFF + 128 * 32768;
constexpr u32 DBG_OFF = WS_END;
constexpr u32 WS_TOTAL = DBG_OFF + 256 * 64 * 8;
constexpr u32 SENT = 0xFFFFFFFFu;

constexpr u32 L_R = 0;
constexpr u32 L_U = 131072;
constexpr u32 L_WDN = L_U + 16384;
constexpr u32 L_M = 155648;
constexpr u32 L_XN = L_M;
constexpr u32 L_P = L_M;
constexpr u32 L_QB = L_M + 4096;
constexpr u32 L_CTX = L_QB;
constexpr u32 L_SM = L_M + 5120;
constexpr u32 L_SL = L_M + 5632;
constexpr u32 L_LAT = L_M + 6144;
constexpr u32 L_HF = L_M + 6656;
constexpr u32 L_FIN = L_M + 7168;
constexpr u32 L_HS = L_M;
constexpr u32 L_FP = L_WDN;
constexpr u32 L_CNT = L_M + 7680;
constexpr u32 L_XF = L_M + 7696;
constexpr u32 LDS_BYTES = 163840;

constexpr u32 SPIN_LIMIT = 1u << 16;
#define KD 6
constexpr int PD = 8;
#define CLR_SLEEP 4
#define CLR_FIRST 8
#define CPRE 10
#define KS 11
#define E2_SLEEP 8

__device__ __forceinline__ float bf2f_lo(u32 v) { return __uint_as_float(v << 16); }
__device__ __forceinline__ float bf2f_hi(u32 v) { return __uint_as_float(v & 0xffff0000u); }
__device__ __forceinline__ u32 pack2(float lo, float hi) { bf16x2 v = {(__bf16)lo, (__bf16)hi}; return __builtin_bit_cast(u32, v); }
__device__ __forceinline__ float rbf(float f) { return __uint_as_float(((u32)__builtin_bit_cast(u16, (__bf16)f)) << 16); }
__device__ __forceinline__ f32x4 mfma32(u32x4 a, u32x4 b, f32x4 c) {
  return __builtin_amdgcn_mfma_f32_16x16x32_bf16(__builtin_bit_cast(bf16x8, a), __builtin_bit_cast(bf16x8, b), c, 0, 0, 0);
}
__device__ __forceinline__ f32x4 mfma16(u32x2 a, u32x2 b, f32x4 c) {
  return __builtin_amdgcn_mfma_f32_16x16x16bf16_1k(__builtin_bit_cast(s16x4, a), __builtin_bit_cast(s16x4, b), c, 0, 0, 0);
}
template <int CTRL> __device__ __forceinline__ float dpp(float x) {
  return __builtin_bit_cast(float, __builtin_amdgcn_mov_dpp(__builtin_bit_cast(int, x), CTRL, 0xf, 0xf, true));
}
constexpr int XOR1 = 0xB1, XOR2 = 0x4E, XOR7 = 0x141, XOR15 = 0x140, XOR8 = 0x128;
__device__ __forceinline__ float x16_sum(float x) { auto s = __builtin_amdgcn_permlane16_swap(__float_as_uint(x), __float_as_uint(x), false, false); return __uint_as_float(s[0]) + __uint_as_float(s[1]); }
__device__ __forceinline__ float x32_sum(float x) { auto s = __builtin_amdgcn_permlane32_swap(__float_as_uint(x), __float_as_uint(x), false, false); return __uint_as_float(s[0]) + __uint_as_float(s[1]); }
__device__ __forceinline__ float x16_max(float x) { auto s = __builtin_amdgcn_permlane16_swap(__float_as_uint(x), __float_as_uint(x), false, false); return fmaxf(__uint_as_float(s[0]), __uint_as_float(s[1])); }
__device__ __forceinline__ float x32_max(float x) { auto s = __builtin_amdgcn_permlane32_swap(__float_as_uint(x), __float_as_uint(x), false, false); return fmaxf(__uint_as_float(s[0]), __uint_as_float(s[1])); }
__device__ __forceinline__ float wave_sum(float v) {
  v += dpp<XOR1>(v); v += dpp<XOR2>(v); v += dpp<XOR7>(v); v += dpp<XOR15>(v); v = x16_sum(v); v = x32_sum(v); return v;
}
__device__ __forceinline__ float half_sum32(float v) {
  v += dpp<XOR1>(v); v += dpp<XOR2>(v); v += dpp<XOR7>(v); v += dpp<XOR15>(v); v = x16_sum(v); return v;
}
__device__ __forceinline__ float half_max32(float v) {
  v = fmaxf(v, dpp<XOR1>(v)); v = fmaxf(v, dpp<XOR2>(v)); v = fmaxf(v, dpp<XOR7>(v)); v = fmaxf(v, dpp<XOR15>(v)); v = x16_max(v); return v;
}

#define BAR() asm volatile("s_waitcnt lgkmcnt(0)\n\ts_barrier" ::: "memory")
#define RAWBAR() asm volatile("s_barrier" ::: "memory")
#define VMWAIT(n) asm volatile("s_waitcnt vmcnt(" #n ")" ::: "memory")
#define SCHED() __builtin_amdgcn_sched_barrier(0)
template <int N> __device__ __forceinline__ void vmwait() { asm volatile("s_waitcnt vmcnt(%0)" :: "i"(N) : "memory"); }
#define PIN(a) asm volatile("" : "+v"(a))

#define NTS ""
__device__ __forceinline__ void ld128(u32x4& d, u32 voff, rsrc_t rs, u32 soff) {
  asm volatile("buffer_load_dwordx4 %0, %1, %2, %3 offen" : "=v"(d) : "v"(voff), "s"(rs), "s"(soff) : "memory");
}
__device__ __forceinline__ void ld128_w(u32x4& d, u32 voff, rsrc_t rs, u32 soff) {
  asm volatile("buffer_load_dwordx4 %0, %1, %2, %3 offen" NTS : "=v"(d) : "v"(voff), "s"(rs), "s"(soff) : "memory");
}
__device__ __forceinline__ void ld128_sc1(u32x4& d, u32 voff, rsrc_t rs, u32 soff) {
  asm volatile("buffer_load_dwordx4 %0, %1, %2, %3 offen sc1" : "=v"(d) : "v"(voff), "s"(rs), "s"(soff) : "memory");
}
__device__ __forceinline__ void ld64_sc1(u32x2& d, u32 voff, rsrc_t rs, u32 soff) {
  asm volatile("buffer_load_dwordx2 %0, %1, %2, %3 offen sc1" : "=v"(d) : "v"(voff), "s"(rs), "s"(soff) : "memory");
}
__device__ __forceinline__ void ld32_sc1(u32& d, u32 voff, rsrc_t rs, u32 soff) {
  asm volatile("buffer_load_dword %0, %1, %2, %3 offen sc1" : "=v"(d) : "v"(voff), "s"(rs), "s"(soff) : "memory");
}
__device__ __forceinline__ void ld32(u32& d, u32 voff, rsrc_t rs, u32 soff) {
  asm volatile("buffer_load_dword %0, %1, %2, %3 offen" : "=v"(d) : "v"(voff), "s"(rs), "s"(soff) : "memory");
}
__device__ __forceinline__ void st128_sc1(u32x4 d, u32 voff, rsrc_t rs, u32 soff) {
  asm volatile("buffer_store_dwordx4 %0, %1, %2, %3 offen sc1\n\ts_nop 1" :: "v"(d), "v"(voff), "s"(rs), "s"(soff) : "memory");
}
__device__ __forceinline__ void st64_sc1(u32x2 d, u32 voff, rsrc_t rs) {
  asm volatile("buffer_store_dwordx2 %0, %1, %2, 0 offen sc1\n\ts_nop 1" :: "v"(d), "v"(voff), "s"(rs) : "memory");
}
__device__ __forceinline__ void st32_sc1(u32 d, u32 voff, rsrc_t rs) {
  asm volatile("buffer_store_dword %0, %1, %2, 0 offen sc1\n\ts_nop 1" :: "v"(d), "v"(voff), "s"(rs) : "memory");
}
__device__ __forceinline__ void dma16(u32 lds_dst, u32 voff, rsrc_t rs, u32 soff) {
  asm volatile("s_mov_b32 m0, %0\n\ts_nop 0\n\tbuffer_load_dwordx4 %1, %2, %3 offen" NTS " lds" :: "s"(lds_dst), "v"(voff), "s"(rs), "s"(soff) : "memory");
}
__device__ __forceinline__ void dma16_def(u32 lds_dst, u32 voff, rsrc_t rs, u32 soff) {
  asm volatile("s_mov_b32 m0, %0\n\ts_nop 0\n\tbuffer_load_dwordx4 %1, %2, %3 offen lds" :: "s"(lds_dst), "v"(voff), "s"(rs), "s"(soff) : "memory");
}
#define KV_NT 1
#define KV_STAG 0
#define WO_NT 1
#define WQ_NT 1
#define dma16_kv dma16
#define dma16_wo dma16
#define ld128_wq ld128_w
__device__ __forceinline__ rsrc_t mk(const void* p, u32 bytes) { return __builtin_amdgcn_make_buffer_rsrc((void*)p, 0, bytes, 0x00020000); }
__device__ __forceinline__ u32x4 lds128(LAS unsigned char* L, u32 a) { return *(LAS u32x4*)(L + a); }
__device__ __forceinline__ u32x4 trfrag(LAS unsigned char* L, u32 a, u32 o0, u32 o1) {
  const s16x4 t0 = __builtin_amdgcn_ds_read_tr16_b64_v4i16((LAS s16x4*)(L + a + o0));
  const s16x4 t1 = __builtin_amdgcn_ds_read_tr16_b64_v4i16((LAS s16x4*)(L + a + o1));
  const u32x2 x = __builtin_bit_cast(u32x2, t0), y = __builtin_bit_cast(u32x2, t1);
  return u32x4{x.x, x.y, y.x, y.y};
}
__device__ __forceinline__ bool tags_ok(const u32x4* v, int n, u32 epoch) {
  u32 a = v[0].y & v[0].w, o = v[0].y | v[0].w;
#pragma unroll
  for (int k = 1; k < n; ++k) { a &= v[k].y & v[k].w; o |= v[k].y | v[k].w; }
  return a == epoch && o == epoch;
}

#define STAMP(i) do {} while (0)

__global__ void __launch_bounds__(512, 2)
fused_decode(const u16* __restrict__ x, const u16* __restrict__ ckv, const u16* __restrict__ wq, const u16* __restrict__ wuk,
             const u16* __restrict__ wuv, const u16* __restrict__ wo, const u16* __restrict__ wg, const u16* __restrict__ wu,
             const u16* __restrict__ wd, u16* __restrict__ out, unsigned char* __restrict__ ws, const u32* __restrict__ ws_epoch) {
  __shared__ __attribute__((aligned(16))) unsigned char lds[LDS_BYTES];
  LAS unsigned char* L = (LAS unsigned char*)lds;
  const u32 lbase = (u32)(size_t)L;
  const int tid = threadIdx.x;
  const int w = __builtin_amdgcn_readfirstlane(tid >> 6);
  const int l = tid & 63, g = l >> 4, idx = l & 15;
  const int bid = blockIdx.x, xc = bid & 7, r = bid >> 3;
  const int h = xc + 8 * (r >> 3), j = r & 7;
  const int P = xc * 16 + (r & 15), ab = P >> 5, as = P & 31, ag = 1 - (r >> 4);
  const bool ffn_ok = h < 28;
  const int uc = ffn_ok ? h : 27;
  const int fmat = uc & 1, ftile = uc >> 1;
  STAMP(0);

  const rsrc_t rs_x = mk(x, 4096), rs_kv = mk(ckv, 4u * 4096 * 1024), rs_wq = mk(wq, 4u << 20), rs_wuk = mk(wuk, 4u << 20),
               rs_wuv = mk(wuv, 4u << 20), rs_wo = mk(wo, 4u << 20), rs_wg = mk(wg, 512 * 1792 * 2), rs_wu = mk(wu, 512 * 1792 * 2),
               rs_wd = mk(wd, 512 * 1792 * 2), rs_ws = mk(ws, WS_TOTAL);

  const bool chain = w < 4;
  const int sw = w & 3;
  const int q_ = idx >> 2, p_ = idx & 3, jp = idx & 7;
  const int blk2 = (j + 4 + sw) & 7;
  bool fail = false;
  const u32 epoch = ws_epoch[0] + 1u;
  u32 dvo[8];
  u32 a_qir[4], a_kvr[4], trb[4], trb2[4];
  u32 a_smw, a_smr, a_pw, a_pr, o_e3w, bp_src, o_stw;
#define SEL_INIT() u32 sel_lo[4], sel_hi[4]; \
  _Pragma("unroll") for (int i = 0; i < 4; ++i) { const bool on = (i == (jp >> 1)); \
    sel_lo[i] = on ? ((jp & 1) ? 0x01000c0cu : 0x0c0c0100u) : 0x0c0c0c0cu; sel_hi[i] = on ? ((jp & 1) ? 0x03020c0cu : 0x0c0c0302u) : 0x0c0c0c0cu; \
    PIN(sel_lo[i]); PIN(sel_hi[i]); }
#define DIAG_A(p, sel) u32x4{__builtin_amdgcn_perm(p, p, sel[0]), __builtin_amdgcn_perm(p, p, sel[1]), __builtin_amdgcn_perm(p, p, sel[2]), __builtin_amdgcn_perm(p, p, sel[3])}
#define EPOCH_AND_XN() do { \
    asm volatile("s_load_dword %0, %1, 0x0\n\ts_waitcnt lgkmcnt(0)" : "=s"(epoch) : "s"(ws) : "memory"); epoch += 1u; \
    while (__hip_atomic_load((LAS u32*)(L + L_XF), __ATOMIC_RELAXED, __HIP_MEMORY_SCOPE_WORKGROUP) < 4u) __builtin_amdgcn_s_sleep(1); \
    asm volatile("" ::: "memory"); } while (0)
#define DSTEP(acc0, acc1, wf, p0, p1, par) do { const u32* sel_ = (par) ? sel_hi : sel_lo; \
    acc0 = mfma32(DIAG_A(p0, sel_), wf, acc0); acc1 = mfma32(DIAG_A(p1, sel_), wf, acc1); } while (0)
#define LATE_ADDR() do { \
    _Pragma("unroll") for (int c = 0; c < 4; ++c) { const u32 uu = (u32)(4 * c + g); \
      a_qir[c] = L_U + uu * 256 + (((u32)idx ^ uu) * 16); \
      a_kvr[c] = L_R + (u32)((16 * w + idx) * 1024) + ((uu ^ (u32)(2 * (idx & 7))) * 16); \
      const u32 pos = (u32)((8 * w + 2 * c + (p_ >> 1)) ^ (8 * (g & 1) + 2 * q_)); \
      trb[c] = L_R + (u32)((4 * g + q_) * 1024) + pos * 16 + (u32)(8 * (p_ & 1)); trb2[c] = trb[c] + 65536u; \
      PIN(a_qir[c]); PIN(a_kvr[c]); PIN(trb[c]); PIN(trb2[c]); } \
    _Pragma("unroll") for (int i = 0; i < 8; ++i) { dvo[i] = (u32)((l ^ (2 * i)) * 16); PIN(dvo[i]); } \
    a_smw = L_SM + (u32)((w * 16 + idx) * 4); a_smr = L_SM + (u32)(idx * 4); \
    a_pw = L_P + (u32)((((w >> 1) * 4 + g) * 16 + idx) * 16 + 8 * (w & 1)); a_pr = L_P + (u32)(l * 16); \
    o_e3w = E3_OFF + (u32)(((((16 * ag + (l >> 2)) * 8 + w) * 4 + ab) * 32 + as) * 256 + (l & 3) * 16); \
    bp_src = (u32)(((l >> 2) + 16 * (l & 3)) * 4); \
    o_stw = ST_OFF + (u32)((((16 * ag + idx) * 4 + ab) * 32 + as) * 8); \
    PIN(a_smw); PIN(a_smr); PIN(a_pw); PIN(a_pr); PIN(o_e3w); PIN(bp_src); PIN(o_stw); } while (0)
  const u32 a_qpw = L_U + (u32)(((w * 4 + (g >> 1)) * 128 + 8 * idx + 4 * (g & 1)) * 4);

  const int wb = (w + j) & 7;
  const u32 vo_wq = (u32)((64 * wb + 16 * g) * 8192 + h * 256 + idx * 16);
  const u32 a_xn = L_XN + (u32)((idx >> 3) * 1024 + (64 * wb + 16 * g) * 2);
  const u32 kvx = (u32)(KV_STAG * ag * 16384);
  const u32 L_WUK = L_R + 112 * 1024 - kvx;
#define QLOOP(EXTRA) do { \
      const u32x4 xa0 = lds128(L, a_xn), xa1 = lds128(L, a_xn + 16), xb0 = lds128(L, a_xn + 2048), xb1 = lds128(L, a_xn + 2064); \
      f32x4 qa0 = {0.f, 0.f, 0.f, 0.f}, qa1 = {0.f, 0.f, 0.f, 0.f}; \
      QS(0, EXTRA, xa0[0], xb0[0]); QS(1, EXTRA, xa0[0], xb0[0]); QS(2, EXTRA, xa0[1], xb0[1]); QS(3, EXTRA, xa0[1], xb0[1]); \
      QS(4, EXTRA, xa0[2], xb0[2]); QS(5, EXTRA, xa0[2], xb0[2]); QS(6, EXTRA, xa0[3], xb0[3]); QS(7, EXTRA, xa0[3], xb0[3]); \
      QS(8, EXTRA, xa1[0], xb1[0]); QS(9, EXTRA, xa1[0], xb1[0]); QS(10, EXTRA, xa1[1], xb1[1]); QS(11, EXTRA, xa1[1], xb1[1]); \
      QS(12, EXTRA, xa1[2], xb1[2]); QS(13, EXTRA, xa1[2], xb1[2]); QS(14, EXTRA, xa1[3], xb1[3]); QS(15, EXTRA, xa1[3], xb1[3]); \
      *(LAS f32x4*)(L + a_qpw) = qa0; *(LAS f32x4*)(L + a_qpw + 1024) = qa1; } while (0)
#define QS(t, EXTRA, P0, P1) do { vmwait<15 - (t) + (EXTRA)>(); PIN(wqf[t]); DSTEP(qa0, qa1, wqf[t], P0, P1, (t) & 1); } while (0)
#define EPOCH() do {} while (0)

  if (chain) {
    u32x4 xv, wqf[16];
    __builtin_amdgcn_s_setprio(3);
    ld128(xv, (u32)(w * 1024 + l * 16), rs_x, 0u);
#pragma unroll
    for (int t = 0; t < CPRE; ++t) ld128_wq(wqf[t], vo_wq, rs_wq, (u32)(t * 8192));
    SEL_INIT();
    const u32 a_qbr = L_QB + (u32)(((idx & 3) * 128 + 8 * g) * 2);
    const u32 o_qa = QA_OFF + (u32)((((idx & 3) * 32 + h) * 256 + (64 * j + 16 * sw + 4 * g) / 2) * 8);
    STAMP(1);
    {
      vmwait<CPRE>();
      PIN(xv);
      float v[8];
      v[0] = bf2f_lo(xv.x); v[1] = bf2f_hi(xv.x); v[2] = bf2f_lo(xv.y); v[3] = bf2f_hi(xv.y);
      v[4] = bf2f_lo(xv.z); v[5] = bf2f_hi(xv.z); v[6] = bf2f_lo(xv.w); v[7] = bf2f_hi(xv.w);
      float s = ((v[0] + v[1]) + (v[2] + v[3])) + ((v[4] + v[5]) + (v[6] + v[7]));
      s = wave_sum(s);
      const float mean = s * (1.f / 512.f);
      float ss = 0.f;
#pragma unroll
      for (int i = 0; i < 8; ++i) { const float d = v[i] - mean; ss = fmaf(d, d, ss); }
      ss = wave_sum(ss);
      const float var = rbf(ss * (1.f / 512.f));
      const float mb = rbf(mean);
      const float den = rbf(__builtin_amdgcn_sqrtf(rbf(var + 1e-5f)));
      const float rd = __builtin_amdgcn_rcpf(den);
      u32x4 o;
      o.x = pack2(rbf(v[0] - mb) * rd, rbf(v[1] - mb) * rd);
      o.y = pack2(rbf(v[2] - mb) * rd, rbf(v[3] - mb) * rd);
      o.z = pack2(rbf(v[4] - mb) * rd, rbf(v[5] - mb) * rd);
      o.w = pack2(rbf(v[6] - mb) * rd, rbf(v[7] - mb) * rd);
      *(LAS u32x4*)(L + L_XN + w * 1024 + l * 16) = o;
    }
#pragma unroll
    for (int t = CPRE; t < 16; ++t) ld128_wq(wqf[t], vo_wq, rs_wq, (u32)(t * 8192));
    EPOCH();
    BAR();
    STAMP(2);
    QLOOP(0);
    STAMP(3);
    BAR();
    u32x4 wuk[4];
    {
      u32x2 pv[8];
#pragma unroll
      for (int ww = 0; ww < 8; ++ww) pv[ww] = *(LAS u32x2*)(L + L_U + tid * 8 + ww * 2048);
      float s0, s1;
      s0 = ((__uint_as_float(pv[0].x) + __uint_as_float(pv[1].x)) + (__uint_as_float(pv[2].x) + __uint_as_float(pv[3].x))) + ((__uint_as_float(pv[4].x) + __uint_as_float(pv[5].x)) + (__uint_as_float(pv[6].x) + __uint_as_float(pv[7].x)));
      s1 = ((__uint_as_float(pv[0].y) + __uint_as_float(pv[1].y)) + (__uint_as_float(pv[2].y) + __uint_as_float(pv[3].y))) + ((__uint_as_float(pv[4].y) + __uint_as_float(pv[5].y)) + (__uint_as_float(pv[6].y) + __uint_as_float(pv[7].y)));
      *(LAS u32*)(L + L_QB + tid * 4) = pack2(s0, s1);
    }
    BAR();
    {
      u32x4 bq[4];
#pragma unroll
      for (int kk = 0; kk < 4; ++kk) bq[kk] = lds128(L, a_qbr + kk * 64);
#pragma unroll
      for (int kk = 0; kk < 4; ++kk) wuk[kk] = lds128(L, L_WUK + (u32)((16 * sw + idx) * 256) + (u32)(((4 * kk + g) ^ (2 * (idx & 7))) * 16));
      f32x4 acc = {0.f, 0.f, 0.f, 0.f}, acc2 = {0.f, 0.f, 0.f, 0.f};
      acc = mfma32(wuk[0], bq[0], acc); acc2 = mfma32(wuk[1], bq[1], acc2);
      acc = mfma32(wuk[2], bq[2], acc); acc2 = mfma32(wuk[3], bq[3], acc2);
      constexpr float SC = 0.08838834764831845f * 1.4426950408889634f;
      u32x4 o; o.x = pack2((acc[0] + acc2[0]) * SC, (acc[1] + acc2[1]) * SC); o.y = epoch; o.z = pack2((acc[2] + acc2[2]) * SC, (acc[3] + acc2[3]) * SC); o.w = epoch;
      if (idx < 4) st128_sc1(o, o_qa, rs_ws, 0u);
    }
    STAMP(4);
    BAR();
    {
      const int t = tid;
      const u32 o_efw = EF_OFF + (u32)((2 * ftile + ((t & 63) >> 5)) * 32768 + ((fmat * 8 + j) * 4 + (t >> 6)) * 512 + (t & 31) * 16);
      u32x2 pv[4];
#pragma unroll
      for (int ww = 0; ww < 4; ++ww) pv[ww] = *(LAS u32x2*)(L + L_FP + t * 8 + ww * 2048);
      const float s0 = (__uint_as_float(pv[0].x) + __uint_as_float(pv[1].x)) + (__uint_as_float(pv[2].x) + __uint_as_float(pv[3].x));
      const float s1 = (__uint_as_float(pv[0].y) + __uint_as_float(pv[1].y)) + (__uint_as_float(pv[2].y) + __uint_as_float(pv[3].y));
      if (ffn_ok) st128_sc1(u32x4{__float_as_uint(s0), epoch, __float_as_uint(s1), epoch}, o_efw, rs_ws, 0u);
    }
    LATE_ADDR();
    const u32 o_e2 = QA_OFF + (u32)((ab * 32 + 16 * ag) * 2048 + w * 8192 + l * 16);
    u32 a_qiw[4];
#pragma unroll
    for (int k2 = 0; k2 < 4; ++k2) { const u32 uu = (u32)(l >> 1); a_qiw[k2] = L_U + uu * 256 + (((u32)(4 * w + k2) ^ (uu & 15)) * 16) + (u32)(8 * (l & 1)); PIN(a_qiw[k2]); }
    __builtin_amdgcn_s_sleep(E2_SLEEP);
    STAMP(13);
    u32x4 q4[8];
    for (u32 spin = 0;; ++spin) {
#pragma unroll
      for (int k = 0; k < 8; ++k) ld128_sc1(q4[k], o_e2, rs_ws, (u32)(k * 1024));
      VMWAIT(0);
#pragma unroll
      for (int k = 0; k < 8; ++k) PIN(q4[k]);
      if (__all(tags_ok(q4, 8, epoch))) break;
      if (spin > SPIN_LIMIT) { fail = true; break; }
    }
    STAMP(5);
#pragma unroll
    for (int k = 0; k < 8; ++k) {
      *(LAS u32*)(L + a_qiw[k >> 1] + (k & 1) * 8192) = q4[k].x;
      *(LAS u32*)(L + a_qiw[k >> 1] + (k & 1) * 8192 + 4) = q4[k].z;
    }
  } else {
    u32x4 wqf[16], f1w[4];
#pragma unroll
    for (int t = 0; t < 16; ++t) ld128_wq(wqf[t], vo_wq, rs_wq, (u32)(t * 8192));
    const u32 kvrow0 = (u32)((ab * 4096 + as * 128 + 32 * sw) * 1024);
#pragma unroll
    for (int i = 0; i < 8; ++i) { dvo[i] = (u32)((l ^ (2 * i)) * 16); PIN(dvo[i]); }
#define KV(i) dma16_kv(lbase + L_R + (u32)(32 * sw * 1024) + ((u32)((i) * 1024) ^ kvx), dvo[(i) & 7], rs_kv, kvrow0 + ((u32)((i) * 1024) ^ kvx))
    SEL_INIT();
    const u32 a_fx = L_XN + (u32)((idx >> 3) * 1024 + (64 * j + 16 * sw + 4 * g) * 2);
    const u32 vo3 = (u32)((64 * j + 16 * sw + 4 * g) * 3584 + ftile * 256 + idx * 16);
    const rsrc_t rs_f1 = fmat ? rs_wu : rs_wg;
    EPOCH();
    BAR();
#pragma unroll
    for (int i = 0; i < 4; ++i) { const int row = 16 * sw + 4 * i + g;
      dma16(lbase + L_WUK + (u32)((16 * sw + 4 * i) * 256), (u32)(row * 8192 + ((idx ^ (2 * (row & 7))) * 16)), rs_wuk, (u32)(64 * j * 8192 + h * 256)); }
#pragma unroll
    for (int t = 0; t < 4; ++t) ld128_w(f1w[t], vo3, rs_f1, (u32)(t * 3584));
    KV(0); KV(1); KV(2); KV(3);
    STAMP(2);
    const u32x2 fx0 = *(LAS u32x2*)(L + a_fx), fx1 = *(LAS u32x2*)(L + a_fx + 2048);
    QLOOP(12);
    STAMP(3);
    BAR();
    vmwait<8>();
    BAR();
    {
      vmwait<4>();
      PIN(f1w[0]); PIN(f1w[1]); PIN(f1w[2]); PIN(f1w[3]);
      f32x4 f0a = {0.f, 0.f, 0.f, 0.f}, f1a = {0.f, 0.f, 0.f, 0.f};
      DSTEP(f0a, f1a, f1w[0], fx0.x, fx1.x, 0); DSTEP(f0a, f1a, f1w[1], fx0.x, fx1.x, 1);
      DSTEP(f0a, f1a, f1w[2], fx0.y, fx1.y, 0); DSTEP(f0a, f1a, f1w[3], fx0.y, fx1.y, 1);
      const u32 a_fpw = L_FP + (u32)(((sw * 4 + (g >> 1)) * 128 + 8 * idx + 4 * (g & 1)) * 4);
      *(LAS f32x4*)(L + a_fpw) = f0a;
      *(LAS f32x4*)(L + a_fpw + 1024) = f1a;
    }
    BAR();
    KV(4); KV(5); KV(6); KV(7); KV(8); KV(9); KV(10); KV(11);
    LATE_ADDR();
    vmwait<KS>(); KV(12); vmwait<KS>(); KV(13); vmwait<KS>(); KV(14); vmwait<KS>(); KV(15);
    vmwait<KS>(); KV(16); vmwait<KS>(); KV(17); vmwait<KS>(); KV(18); vmwait<KS>(); KV(19); vmwait<KS>(); KV(20);
    vmwait<KS>(); KV(21); vmwait<KS>(); KV(22); vmwait<KS>(); KV(23); vmwait<KS>(); KV(24);
    vmwait<KS>(); KV(25); vmwait<KS>(); KV(26); vmwait<KS>(); KV(27); vmwait<KS>(); KV(28);
    vmwait<KS>(); KV(29); vmwait<KS>(); KV(30); vmwait<KS>(); KV(31);
    VMWAIT(0);
  }
  BAR();
  u32x4 bq[16];
#pragma unroll
  for (int kk = 0; kk < 16; ++kk) bq[kk] = lds128(L, a_qir[kk & 3] + (kk >> 2) * 4096);
  f32x4 sc;
  {
    u32x4 ak[12];
#pragma unroll
    for (int kk = 0; kk < 12; ++kk) ak[kk] = lds128(L, a_kvr[kk & 3] + (kk >> 2) * 256);
    SCHED();
    f32x4 s0 = {0.f, 0.f, 0.f, 0.f}, s1 = {0.f, 0.f, 0.f, 0.f};
#pragma unroll
    for (int kk = 0; kk < 4; kk += 2) { s0 = mfma32(ak[kk], bq[kk], s0); s1 = mfma32(ak[kk + 1], bq[kk + 1], s1); }
    SCHED();
    u32x4 ak2[4];
#pragma unroll
    for (int kk = 0; kk < 4; ++kk) ak2[kk] = lds128(L, a_kvr[kk & 3] + 3 * 256);
    SCHED();
#pragma unroll
    for (int kk = 4; kk < 12; kk += 2) { s0 = mfma32(ak[kk], bq[kk], s0); s1 = mfma32(ak[kk + 1], bq[kk + 1], s1); }
#pragma unroll
    for (int kk = 0; kk < 4; kk += 2) { s0 = mfma32(ak2[kk], bq[12 + kk], s0); s1 = mfma32(ak2[kk + 1], bq[13 + kk], s1); }
    sc = s0 + s1;
    float m = fmaxf(fmaxf(sc[0], sc[1]), fmaxf(sc[2], sc[3]));
    m = x16_max(m); m = x32_max(m);
    if (g == 0) *(LAS float*)(L + a_smw) = m;
  }
  STAMP(6);
  BAR();
  float mall;
  {
    float mv[8];
#pragma unroll
    for (int ww = 0; ww < 8; ++ww) mv[ww] = *(LAS float*)(L + a_smr + ww * 64);
    const float m = fmaxf(fmaxf(fmaxf(mv[0], mv[1]), fmaxf(mv[2], mv[3])), fmaxf(fmaxf(mv[4], mv[5]), fmaxf(mv[6], mv[7])));
    mall = m;
    const float p0 = __builtin_amdgcn_exp2f(sc[0] - m), p1 = __builtin_amdgcn_exp2f(sc[1] - m), p2 = __builtin_amdgcn_exp2f(sc[2] - m), p3 = __builtin_amdgcn_exp2f(sc[3] - m);
    u32x2 pv; pv.x = pack2(p0, p1); pv.y = pack2(p2, p3);
    *(LAS u32x2*)(L + a_pw) = pv;
    float s = (p0 + p1) + (p2 + p3);
    s = x16_sum(s); s = x32_sum(s);
    if (g == 0) *(LAS float*)(L + a_smw + 512) = s;
  }
  BAR();
  {
#pragma unroll
    for (int i = 0; i < 2; ++i) { const int row = 8 * w + 4 * i + g; dma16(lbase + L_U + (u32)((8 * w + 4 * i) * 256), (u32)(row * 8192 + ((idx ^ (2 * (row & 7))) * 16)), rs_wuv, (u32)(64 * j * 8192 + h * 256)); }
    const u32 vo_wd = (u32)((l >> 3) * 1024 + (((l & 7) ^ (2 * ((l >> 4) & 3))) * 16));
    dma16(lbase + L_WDN + (u32)(w * 1024), vo_wd, rs_wd, (u32)((uc * 64 + 8 * w) * 1024 + j * 128));
  }
  {
    u32x4 pf4[4], bk[4][4];
    float sl[8];
#pragma unroll
    for (int ks = 0; ks < 4; ++ks) pf4[ks] = lds128(L, a_pr + ks * 1024);
#pragma unroll
    for (int ct = 0; ct < 4; ++ct) {
      bk[ct][0] = trfrag(L, trb[ct], 0, 16384); bk[ct][1] = trfrag(L, trb[ct], 32768, 49152);
      bk[ct][2] = trfrag(L, trb2[ct], 0, 16384); bk[ct][3] = trfrag(L, trb2[ct], 32768, 49152);
    }
#pragma unroll
    for (int ww = 0; ww < 8; ++ww) sl[ww] = *(LAS float*)(L + a_smr + 512 + ww * 64);
    SCHED();
    f32x4 o[4];
#pragma unroll
    for (int ct = 0; ct < 4; ++ct) o[ct] = f32x4{0.f, 0.f, 0.f, 0.f};
#pragma unroll
    for (int ks = 0; ks < 4; ++ks)
#pragma unroll
      for (int ct = 0; ct < 4; ++ct) o[ct] = mfma32(bk[ct][ks], pf4[ks], o[ct]);
    const float ls = ((sl[0] + sl[1]) + (sl[2] + sl[3])) + ((sl[4] + sl[5]) + (sl[6] + sl[7]));
    const float inv = __builtin_amdgcn_rcpf(ls);
    u32 pk[8];
#pragma unroll
    for (int ct = 0; ct < 4; ++ct) { pk[2 * ct] = pack2(o[ct][0] * inv, o[ct][1] * inv); pk[2 * ct + 1] = pack2(o[ct][2] * inv, o[ct][3] * inv); }
#pragma unroll
    for (int i = 0; i < 8; ++i) pk[i] = (u32)__builtin_amdgcn_ds_bpermute((int)bp_src, (int)pk[i]);
#pragma unroll
    for (int ct = 0; ct < 4; ++ct) st128_sc1(u32x4{pk[2 * ct], epoch, pk[2 * ct + 1], epoch}, o_e3w, rs_ws, (u32)(ct * 64));
    if (w == 0) {
      const float lse = mall + __builtin_amdgcn_logf(ls);
      u32x2 v; v.x = __float_as_uint(lse); v.y = epoch;
      if (g == 0) st64_sc1(v, o_stw, rs_ws);
    }
  }
  STAMP(7);
  BAR();
  if (!chain) {
    const int wr = (2 * sw + 2 * j) & 7;
#pragma unroll
    for (int i = 0; i < 32; ++i) {
      const int row = ((32 * sw + 16 * j) & 127) + i;
      const int rr = row & 127;
      dma16_wo(lbase + L_R + (u32)(rr * 1024), dvo[(i) & 7], rs_wo, (u32)((h * 128 + rr) * 1024));
    }
    (void)wr;
  }
  if (chain) {
    const u32 o_efr = EF_OFF + (u32)(uc * 32768 + (tid >> 7) * 16384 + (tid & 127) * 16);
    u32x4 e4[8];
#pragma unroll
    for (int k = 0; k < 8; ++k) e4[k] = u32x4{0u, 0u, 0u, 0u};
    for (u32 spin = 0; ffn_ok; ++spin) {
#pragma unroll
      for (int k = 0; k < 8; ++k) ld128_sc1(e4[k], o_efr, rs_ws, (u32)(k * 2048));
      VMWAIT(0);
#pragma unroll
      for (int k = 0; k < 8; ++k) PIN(e4[k]);
      if (__all(tags_ok(e4, 8, epoch))) break;
      if (spin > SPIN_LIMIT) { fail = true; break; }
    }
    float s0 = 0.f, s1 = 0.f;
#pragma unroll
    for (int k = 0; k < 8; ++k) { s0 += __uint_as_float(e4[k].x); s1 += __uint_as_float(e4[k].z); }
    *(LAS u32x2*)(L + L_HS + tid * 8) = u32x2{__float_as_uint(s0), __float_as_uint(s1)};
  }
  const int lb = w >> 1, lp8 = 8 * (w & 1) + (l & 7), lss = l >> 3;
  u32 o_e3r = E3_OFF + (u32)(((h * 8 + j) * 4 + lb) * 32 * 256 + lss * 256 + lp8 * 16);
  u32 o_str = ST_OFF + (u32)(((h * 4 + lb) * 32 + (l & 31)) * 8);
  u32 a_wuvr = L_U + (u32)((4 * g + q_) * 256) + (u32)(((2 * w + (p_ >> 1)) ^ (8 * (g & 1) + 2 * q_)) * 16) + (u32)(8 * (p_ & 1));
  u32 a_latr = L_LAT + (u32)((g * 4 + (idx & 3)) * 16);
  u32 a_ctxw = L_CTX + (u32)((((w >> 1) * 4 + g) * 4 + (idx & 3)) * 16 + 8 * (w & 1));
  u32 a_ctxr = L_CTX + (u32)((g * 4 + (idx & 3)) * 16);
  u32 o_e5s = E5S_OFF + (u32)(((16 * w + 4 * (idx >> 2) + g) * 256 + bid) * 128 + (idx & 3) * 16);
  u32 a_latw;
  {
    const int e = ((l >> 5) & 1) * 2 + ((l >> 4) & 1);
    const int c = 16 * (lp8 >> 2) + 4 * (lp8 & 3) + e;
    a_latw = L_LAT + (u32)(((((c >> 5) * 4 + ((c >> 2) & 3)) * 4 + lb) * 8 + 4 * ((c >> 4) & 1) + (c & 3)) * 2);
  }
  PIN(o_e3r); PIN(o_str); PIN(a_wuvr); PIN(a_latr); PIN(a_ctxw); PIN(a_ctxr); PIN(o_e5s); PIN(a_latw);
  STAMP(8);
  {
    u32x4 e4[4]; u32x2 st;
    for (u32 spin = 0;; ++spin) {
      ld64_sc1(st, o_str, rs_ws, 0u);
#pragma unroll
      for (int k = 0; k < 4; ++k) ld128_sc1(e4[k], o_e3r, rs_ws, (u32)(k * 2048));
      VMWAIT(0);
#pragma unroll
      for (int k = 0; k < 4; ++k) PIN(e4[k]);
      PIN(st);
      if (__all(tags_ok(e4, 4, epoch) && st.y == epoch)) break;
      if (spin > SPIN_LIMIT) { fail = true; break; }
    }
    STAMP(9);
    const float lse = __uint_as_float(st.x);
    const float M = half_max32(lse);
    const float e = __builtin_amdgcn_exp2f(lse - M);
    const float S = half_sum32(e);
    const float wgt = e * __builtin_amdgcn_rcpf(S);
    float wk[4];
#pragma unroll
    for (int k = 0; k < 4; ++k) wk[k] = __builtin_bit_cast(float, __builtin_amdgcn_ds_bpermute((8 * k + lss) * 4, __builtin_bit_cast(int, wgt)));
    float v[4] = {0.f, 0.f, 0.f, 0.f};
#pragma unroll
    for (int k = 0; k < 4; ++k) {
      v[0] = fmaf(wk[k], bf2f_lo(e4[k].x), v[0]); v[1] = fmaf(wk[k], bf2f_hi(e4[k].x), v[1]);
      v[2] = fmaf(wk[k], bf2f_lo(e4[k].z), v[2]); v[3] = fmaf(wk[k], bf2f_hi(e4[k].z), v[3]);
    }
#pragma unroll
    for (int i = 0; i < 2; ++i) { auto s = __builtin_amdgcn_permlane32_swap(__float_as_uint(v[i]), __float_as_uint(v[i + 2]), false, false); v[i] = __uint_as_float(s[0]) + __uint_as_float(s[1]); }
    { auto s = __builtin_amdgcn_permlane16_swap(__float_as_uint(v[0]), __float_as_uint(v[1]), false, false); v[0] = __uint_as_float(s[0]) + __uint_as_float(s[1]); }
    v[0] += dpp<XOR8>(v[0]);
    *(LAS u16*)(L + a_latw) = (u16)pack2(v[0], v[0]);
  }
  BAR();
  if (w < 2) {
    const u32x2 g0 = *(LAS u32x2*)(L + L_HS + tid * 8), u0 = *(LAS u32x2*)(L + L_HS + 1024 + tid * 8);
    const float ga = rbf(__uint_as_float(g0.x)), gb = rbf(__uint_as_float(g0.y));
    const float ua = rbf(__uint_as_float(u0.x)), ub = rbf(__uint_as_float(u0.y));
    const float sa = rbf(ga * __builtin_amdgcn_rcpf(1.0f + __expf(-ga))), sb = rbf(gb * __builtin_amdgcn_rcpf(1.0f + __expf(-gb)));
    const int f = 2 * (tid & 31), b = tid >> 5;
    *(LAS u32*)(L + L_HF + (u32)(((((f >> 5) * 4 + ((f >> 2) & 3)) * 4 + b) * 8 + 4 * ((f >> 4) & 1) + (f & 3)) * 2)) = pack2(sa * ua, sb * ub);
  }
  u32x4 wk3[4][4];
  {
    const u32x4 wa0 = trfrag(L, a_wuvr, 0, 4096), wa1 = trfrag(L, a_wuvr, 8192, 12288);
    const u32x4 lb0 = lds128(L, a_latr), lb1 = lds128(L, a_latr + 256);
#pragma unroll
    for (int mt = 0; mt < 4; ++mt) {
      wk3[mt][0] = trfrag(L, trb[mt], 0, 16384); wk3[mt][1] = trfrag(L, trb[mt], 32768, 49152);
      wk3[mt][2] = trfrag(L, trb2[mt], 0, 16384); wk3[mt][3] = trfrag(L, trb2[mt], 32768, 49152);
    }
    f32x4 c = mfma32(wa0, lb0, f32x4{0.f, 0.f, 0.f, 0.f});
    c = mfma32(wa1, lb1, c);
    if (idx < 4) { u32x2 v; v.x = pack2(c[0], c[1]); v.y = pack2(c[2], c[3]); *(LAS u32x2*)(L + a_ctxw) = v; }
  }
  BAR();
  {
    u32x4 cf[4];
#pragma unroll
    for (int ks = 0; ks < 4; ++ks) cf[ks] = lds128(L, a_ctxr + ks * 256);
    f32x4 o[4];
#pragma unroll
    for (int mt = 0; mt < 4; ++mt) o[mt] = f32x4{0.f, 0.f, 0.f, 0.f};
#pragma unroll
    for (int ks = 0; ks < 4; ++ks)
#pragma unroll
      for (int mt = 0; mt < 4; ++mt) o[mt] = mfma32(wk3[mt][ks], cf[ks], o[mt]);
    const int rep = idx >> 2;
    f32x4 v = rep == 0 ? o[0] : rep == 1 ? o[1] : rep == 2 ? o[2] : o[3];
    st128_sc1(u32x4{__float_as_uint(v[0]), epoch, __float_as_uint(v[1]), epoch}, o_e5s, rs_ws, 0u);
    st128_sc1(u32x4{__float_as_uint(v[2]), epoch, __float_as_uint(v[3]), epoch}, o_e5s, rs_ws, 64u);
  }
  if (w < 4) {
    const int row = 4 * g + q_;
    const u32 a0 = L_WDN + (u32)(row * 128) + (u32)(((2 * w + (p_ >> 1)) ^ (2 * ((row >> 1) & 3))) * 16) + (u32)(8 * (p_ & 1));
    const u32x4 wa0 = trfrag(L, a0, 0, 2048), wa1 = trfrag(L, a0, 4096, 6144);
    const u32x4 hb0 = lds128(L, L_HF + (u32)((g * 4 + (idx & 3)) * 16)), hb1 = lds128(L, L_HF + 256 + (u32)((g * 4 + (idx & 3)) * 16));
    f32x4 y = mfma32(wa0, hb0, f32x4{0.f, 0.f, 0.f, 0.f});
    y = mfma32(wa1, hb1, y);
    const u32 off = E5Y_OFF + (u32)(((16 * j + 4 * w + g) * 32 + uc) * 128 + (idx & 3) * 32);
    if (ffn_ok && idx < 4) {
      st128_sc1(u32x4{__float_as_uint(y[0]), epoch, __float_as_uint(y[1]), epoch}, off, rs_ws, 0u);
      st128_sc1(u32x4{__float_as_uint(y[2]), epoch, __float_as_uint(y[3]), epoch}, off, rs_ws, 16u);
    }
  }
  STAMP(10);
  if (bid < 128) {
    u32x4 a4[4], y4;
    const u32 xfin = ((const u32*)x)[(l & 3) * 256 + bid * 2 + ((l >> 3) & 1)];
    const u32 o_r = E5S_OFF + (u32)(bid * 32768 + tid * 16);
    const u32 o_y = E5Y_OFF + (u32)(bid * 4096 + (tid & 255) * 16);
    const bool yv = tid < 224;
    for (u32 spin = 0;; ++spin) {
      ld128_sc1(y4, o_y, rs_ws, 0u);
#pragma unroll
      for (int k = 0; k < 4; ++k) ld128_sc1(a4[k], o_r, rs_ws, (u32)(k * 8192));
      VMWAIT(0);
#pragma unroll
      for (int k = 0; k < 4; ++k) PIN(a4[k]);
      PIN(y4);
      if (__all(tags_ok(a4, 4, epoch) && (!yv || (y4.y == epoch && y4.w == epoch)))) break;
      if (spin > SPIN_LIMIT) { fail = true; break; }
    }
    STAMP(11);
    float v[2];
    v[0] = (__uint_as_float(a4[0].x) + __uint_as_float(a4[1].x)) + (__uint_as_float(a4[2].x) + __uint_as_float(a4[3].x));
    v[1] = (__uint_as_float(a4[0].z) + __uint_as_float(a4[1].z)) + (__uint_as_float(a4[2].z) + __uint_as_float(a4[3].z));
    { auto s = __builtin_amdgcn_permlane32_swap(__float_as_uint(v[0]), __float_as_uint(v[1]), false, false); v[0] = __uint_as_float(s[0]) + __uint_as_float(s[1]); }
    v[0] = x16_sum(v[0]); v[0] += dpp<XOR8>(v[0]);
    float y0 = yv ? __uint_as_float(y4.x) : 0.f, y1 = yv ? __uint_as_float(y4.z) : 0.f;
    y0 += dpp<XOR8>(y0); y1 += dpp<XOR8>(y1);
    y0 = x16_sum(y0); y1 = x16_sum(y1); y0 = x32_sum(y0); y1 = x32_sum(y1);
    if ((l & 24) == 0) *(LAS float*)(L + L_FIN + (u32)((w * 16 + (((l >> 2) & 1) * 2 + (l >> 5)) * 4 + (l & 3)) * 4)) = v[0];
    if (l < 8 && w < 4) *(LAS u32x2*)(L + L_HS + (u32)((w * 8 + l) * 8)) = u32x2{__float_as_uint(y0), __float_as_uint(y1)};
    BAR();
    if (w == 0) {
      const int mi = (l >> 2) & 3, b = l & 3;
      float sv[8], yv4[4];
#pragma unroll
      for (int ww = 0; ww < 8; ++ww) sv[ww] = *(LAS float*)(L + L_FIN + (u32)((ww * 16 + mi * 4 + b) * 4));
#pragma unroll
      for (int ww = 0; ww < 4; ++ww) yv4[ww] = *(LAS float*)(L + L_HS + (u32)(((ww * 8 + b * 2 + (mi >> 1)) * 2 + (mi & 1)) * 4));
      const float s = ((sv[0] + sv[1]) + (sv[2] + sv[3])) + ((sv[4] + sv[5]) + (sv[6] + sv[7]));
      const float y = (yv4[0] + yv4[1]) + (yv4[2] + yv4[3]);
      const float sb = rbf(s), yb = rbf(y);
      const float gg = rbf(yb * __builtin_amdgcn_rcpf(1.0f + __expf(-yb)));
      const float xv_ = (mi & 1) ? bf2f_hi(xfin) : bf2f_lo(xfin);
      float o = xv_ + rbf(sb * gg);
      if (fail) o = __uint_as_float(0x7fc00000u);
      if (l < 16) out[b * 512 + 4 * bid + mi] = (u16)(pack2(o, 0.f));
      if (bid == 0 && l == 0) st32_sc1(epoch, EPOCH_OFF, rs_ws);
    }
  }
}

extern "C" void kernel_launch(void* const* d_in, const int* in_sizes, int n_in, void* d_out, int out_size, void* d_ws, size_t ws_size, hipStream_t stream) {
  const u16* x = (const u16*)d_in[0]; const u16* ckv = (const u16*)d_in[1]; const u16* wq = (const u16*)d_in[2]; const u16* wuk = (const u16*)d_in[3];
  const u16* wuv = (const u16*)d_in[4]; const u16* wo = (const u16*)d_in[5]; const u16* wg = (const u16*)d_in[7]; const u16* wu = (const u16*)d_in[8]; const u16* wd = (const u16*)d_in[9];
  fused_decode<<<256, 512, 0, stream>>>(x, ckv, wq, wuk, wuv, wo, wg, wu, wd, (u16*)d_out, (unsigned char*)d_ws, (const u32*)d_ws);
}
```

```cpp
#include <hip/hip_runtime.h>
#include <stdint.h>

typedef unsigned u32;
typedef unsigned short u16;
typedef u32 u32x4 __attribute__((ext_vector_type(4)));
typedef u32 u32x2 __attribute__((ext_vector_type(2)));
typedef float f32x4 __attribute__((ext_vector_type(4)));
typedef short s16x4 __attribute__((ext_vector_type(4)));
typedef __bf16 bf16x8 __attribute__((ext_vector_type(8)));
typedef __bf16 bf16x2 __attribute__((ext_vector_type(2)));
#define LAS __attribute__((address_space(3)))
typedef __amdgpu_buffer_rsrc_t rsrc_t;

#define DIAG 0
#define NT_W 0

constexpr u32 EPOCH_OFF = 0;
constexpr u32 QA_OFF = 4096;
constexpr u32 ST_OFF = QA_OFF + 4 * 32 * 2048;
constexpr u32 CF_OFF = ST_OFF + 32 * 4 * 32 * 8;
constexpr u32 EF_OFF = CF_OFF + 4096;
constexpr u32 E5Y_OFF = EF_OFF + 32 * 32768;
constexpr u32 E3_OFF = E5Y_OFF + 128 * 4096;
constexpr u32 E5S_OFF = E3_OFF + 256 * 16384;
constexpr u32 WS_END = E5S_OFF + 128 * 16384;
constexpr u32 DBG_OFF = WS_END;
constexpr u32 WS_TOTAL = DBG_OFF + 256 * 64 * 8;
constexpr u32 SENT = 0xFFFFFFFFu;

constexpr u32 L_R = 0;
constexpr u32 L_U = 131072;
constexpr u32 L_WDN = L_U + 16384;
constexpr u32 L_M = 155648;
constexpr u32 L_XN = L_M;
constexpr u32 L_P = L_M;
constexpr u32 L_QB = L_M + 4096;
constexpr u32 L_CTX = L_QB;
constexpr u32 L_SM = L_M + 5120;
constexpr u32 L_SL = L_M + 5632;
constexpr u32 L_LAT = L_M + 6144;
constexpr u32 L_HF = L_M + 6656;
constexpr u32 L_FIN = L_M + 7168;
constexpr u32 L_HS = L_M;
constexpr u32 L_FP = L_WDN;
constexpr u32 L_CNT = L_M + 7680;
constexpr u32 L_XF = L_M + 7696;
constexpr u32 LDS_BYTES = 163840;

constexpr u32 SPIN_LIMIT = 1u << 16;
#define KD 6
constexpr int PD = 8;
#define KS 11
#define E2_SLEEP 8

__device__ __forceinline__ float bf2f_lo(u32 v) { return __uint_as_float(v << 16); }
__device__ __forceinline__ float bf2f_hi(u32 v) { return __uint_as_float(v & 0xffff0000u); }
__device__ __forceinline__ u32 pack2(float lo, float hi) { bf16x2 v = {(__bf16)lo, (__bf16)hi}; return __builtin_bit_cast(u32, v); }
__device__ __forceinline__ float rbf(float f) { return __uint_as_float(((u32)__builtin_bit_cast(u16, (__bf16)f)) << 16); }
__device__ __forceinline__ f32x4 mfma32(u32x4 a, u32x4 b, f32x4 c) {
  return __builtin_amdgcn_mfma_f32_16x16x32_bf16(__builtin_bit_cast(bf16x8, a), __builtin_bit_cast(bf16x8, b), c, 0, 0, 0);
}
__device__ __forceinline__ f32x4 mfma16(u32x2 a, u32x2 b, f32x4 c) {
  return __builtin_amdgcn_mfma_f32_16x16x16bf16_1k(__builtin_bit_cast(s16x4, a), __builtin_bit_cast(s16x4, b), c, 0, 0, 0);
}
template <int CTRL> __device__ __forceinline__ float dpp(float x) {
  return __builtin_bit_cast(float, __builtin_amdgcn_mov_dpp(__builtin_bit_cast(int, x), CTRL, 0xf, 0xf, true));
}
constexpr int XOR1 = 0xB1, XOR2 = 0x4E, XOR7 = 0x141, XOR15 = 0x140, XOR8 = 0x128;
__device__ __forceinline__ float x16_sum(float x) { auto s = __builtin_amdgcn_permlane16_swap(__float_as_uint(x), __float_as_uint(x), false, false); return __uint_as_float(s[0]) + __uint_as_float(s[1]); }
__device__ __forceinline__ float x32_sum(float x) { auto s = __builtin_amdgcn_permlane32_swap(__float_as_uint(x), __float_as_uint(x), false, false); return __uint_as_float(s[0]) + __uint_as_float(s[1]); }
__device__ __forceinline__ float x16_max(float x) { auto s = __builtin_amdgcn_permlane16_swap(__float_as_uint(x), __float_as_uint(x), false, false); return fmaxf(__uint_as_float(s[0]), __uint_as_float(s[1])); }
__device__ __forceinline__ float x32_max(float x) { auto s = __builtin_amdgcn_permlane32_swap(__float_as_uint(x), __float_as_uint(x), false, false); return fmaxf(__uint_as_float(s[0]), __uint_as_float(s[1])); }
__device__ __forceinline__ float wave_sum(float v) {
  v += dpp<XOR1>(v); v += dpp<XOR2>(v); v += dpp<XOR7>(v); v += dpp<XOR15>(v); v = x16_sum(v); v = x32_sum(v); return v;
}
__device__ __forceinline__ float half_sum32(float v) {
  v += dpp<XOR1>(v); v += dpp<XOR2>(v); v += dpp<XOR7>(v); v += dpp<XOR15>(v); v = x16_sum(v); return v;
}
__device__ __forceinline__ float half_max32(float v) {
  v = fmaxf(v, dpp<XOR1>(v)); v = fmaxf(v, dpp<XOR2>(v)); v = fmaxf(v, dpp<XOR7>(v)); v = fmaxf(v, dpp<XOR15>(v)); v = x16_max(v); return v;
}

#define BAR() asm volatile("s_waitcnt lgkmcnt(0)\n\ts_barrier" ::: "memory")
#define RAWBAR() asm volatile("s_barrier" ::: "memory")
#define VMWAIT(n) asm volatile("s_waitcnt vmcnt(" #n ")" ::: "memory")
#define SCHED() __builtin_amdgcn_sched_barrier(0)
template <int N> __device__ __forceinline__ void vmwait() { asm volatile("s_waitcnt vmcnt(%0)" :: "i"(N) : "memory"); }
#define PIN(a) asm volatile("" : "+v"(a))

#define NTS ""
__device__ __forceinline__ void ld128(u32x4& d, u32 voff, rsrc_t rs, u32 soff) {
  asm volatile("buffer_load_dwordx4 %0, %1, %2, %3 offen" : "=v"(d) : "v"(voff), "s"(rs), "s"(soff) : "memory");
}
__device__ __forceinline__ void ld128_w(u32x4& d, u32 voff, rsrc_t rs, u32 soff) {
  asm volatile("buffer_load_dwordx4 %0, %1, %2, %3 offen" NTS : "=v"(d) : "v"(voff), "s"(rs), "s"(soff) : "memory");
}
__device__ __forceinline__ void ld128_sc1(u32x4& d, u32 voff, rsrc_t rs, u32 soff) {
  asm volatile("buffer_load_dwordx4 %0, %1, %2, %3 offen sc1" : "=v"(d) : "v"(voff), "s"(rs), "s"(soff) : "memory");
}
__device__ __forceinline__ void ld64_sc1(u32x2& d, u32 voff, rsrc_t rs, u32 soff) {
  asm volatile("buffer_load_dwordx2 %0, %1, %2, %3 offen sc1" : "=v"(d) : "v"(voff), "s"(rs), "s"(soff) : "memory");
}
__device__ __forceinline__ void ld32_sc1(u32& d, u32 voff, rsrc_t rs, u32 soff) {
  asm volatile("buffer_load_dword %0, %1, %2, %3 offen sc1" : "=v"(d) : "v"(voff), "s"(rs), "s"(soff) : "memory");
}
__device__ __forceinline__ void ld32(u32& d, u32 voff, rsrc_t rs, u32 soff) {
  asm volatile("buffer_load_dword %0, %1, %2, %3 offen" : "=v"(d) : "v"(voff), "s"(rs), "s"(soff) : "memory");
}
__device__ __forceinline__ void st128_sc1(u32x4 d, u32 voff, rsrc_t rs, u32 soff) {
  asm volatile("buffer_store_dwordx4 %0, %1, %2, %3 offen sc1\n\ts_nop 1" :: "v"(d), "v"(voff), "s"(rs), "s"(soff) : "memory");
}
__device__ __forceinline__ void st64_sc1(u32x2 d, u32 voff, rsrc_t rs) {
  asm volatile("buffer_store_dwordx2 %0, %1, %2, 0 offen sc1\n\ts_nop 1" :: "v"(d), "v"(voff), "s"(rs) : "memory");
}
__device__ __forceinline__ void st32_sc1(u32 d, u32 voff, rsrc_t rs) {
  asm volatile("buffer_store_dword %0, %1, %2, 0 offen sc1\n\ts_nop 1" :: "v"(d), "v"(voff), "s"(rs) : "memory");
}
__device__ __forceinline__ void dma16(u32 lds_dst, u32 voff, rsrc_t rs, u32 soff) {
  asm volatile("s_mov_b32 m0, %0\n\ts_nop 0\n\tbuffer_load_dwordx4 %1, %2, %3 offen" NTS " lds" :: "s"(lds_dst), "v"(voff), "s"(rs), "s"(soff) : "memory");
}
__device__ __forceinline__ void dma16_def(u32 lds_dst, u32 voff, rsrc_t rs, u32 soff) {
  asm volatile("s_mov_b32 m0, %0\n\ts_nop 0\n\tbuffer_load_dwordx4 %1, %2, %3 offen lds" :: "s"(lds_dst), "v"(voff), "s"(rs), "s"(soff) : "memory");
}
#define KV_NT 1
#define KV_STAG 0
#define WO_NT 1
#define WQ_NT 1
#define dma16_kv dma16
#define dma16_wo dma16
#define ld128_wq ld128_w
__device__ __forceinline__ rsrc_t mk(const void* p, u32 bytes) { return __builtin_amdgcn_make_buffer_rsrc((void*)p, 0, bytes, 0x00020000); }
__device__ __forceinline__ u32x4 lds128(LAS unsigned char* L, u32 a) { return *(LAS u32x4*)(L + a); }
__device__ __forceinline__ u32x4 trfrag(LAS unsigned char* L, u32 a, u32 o0, u32 o1) {
  const s16x4 t0 = __builtin_amdgcn_ds_read_tr16_b64_v4i16((LAS s16x4*)(L + a + o0));
  const s16x4 t1 = __builtin_amdgcn_ds_read_tr16_b64_v4i16((LAS s16x4*)(L + a + o1));
  const u32x2 x = __builtin_bit_cast(u32x2, t0), y = __builtin_bit_cast(u32x2, t1);
  return u32x4{x.x, x.y, y.x, y.y};
}
__device__ __forceinline__ bool tags_ok(const u32x4* v, int n, u32 epoch) {
  u32 a = v[0].y & v[0].w, o = v[0].y | v[0].w;
#pragma unroll
  for (int k = 1; k < n; ++k) { a &= v[k].y & v[k].w; o |= v[k].y | v[k].w; }
  return a == epoch && o == epoch;
}

#define STAMP(i) do {} while (0)

__global__ void __launch_bounds__(512, 2)
fused_decode(const u16* __restrict__ x, const u16* __restrict__ ckv, const u16* __restrict__ wq, const u16* __restrict__ wuk,
             const u16* __restrict__ wuv, const u16* __restrict__ wo, const u16* __restrict__ wg, const u16* __restrict__ wu,
             const u16* __restrict__ wd, u16* __restrict__ out, unsigned char* __restrict__ ws) {
  __shared__ __attribute__((aligned(16))) unsigned char lds[LDS_BYTES];
  LAS unsigned char* L = (LAS unsigned char*)lds;
  const u32 lbase = (u32)(size_t)L;
  const int tid = threadIdx.x;
  const int w = __builtin_amdgcn_readfirstlane(tid >> 6);
  const int l = tid & 63, g = l >> 4, idx = l & 15;
  const int bid = blockIdx.x, xc = bid & 7, r = bid >> 3;
  const int h = xc + 8 * (r >> 3), j = r & 7;
  const int P = xc * 16 + (r & 15), ab = P >> 5, as = P & 31, ag = 1 - (r >> 4);
  const bool ffn_ok = h < 28;
  const int uc = ffn_ok ? h : 27;
  const int fmat = uc & 1, ftile = uc >> 1;
  STAMP(0);

  const rsrc_t rs_x = mk(x, 4096), rs_kv = mk(ckv, 4u * 4096 * 1024), rs_wq = mk(wq, 4u << 20), rs_wuk = mk(wuk, 4u << 20),
               rs_wuv = mk(wuv, 4u << 20), rs_wo = mk(wo, 4u << 20), rs_wg = mk(wg, 512 * 1792 * 2), rs_wu = mk(wu, 512 * 1792 * 2),
               rs_wd = mk(wd, 512 * 1792 * 2), rs_ws = mk(ws, WS_TOTAL);

  const bool chain = w < 4;
  const int sw = w & 3;
  u32x4 xv; u32x4 wqf[16]; u32x4 aux[4];
  const int wb = (w + j) & 7;
  const u32 vo_wq = (u32)((64 * wb + 16 * g) * 8192 + h * 256 + idx * 16);
  const rsrc_t rs_f1 = fmat ? rs_wu : rs_wg;
  if (chain) ld128(xv, (u32)(w * 1024 + l * 16), rs_x, 0u);
  if (tid == 0) { *(LAS u32*)(L + L_CNT) = 0u; *(LAS u32*)(L + L_XF) = 0u; }
  BAR();
#pragma unroll
  for (int t = 0; t < 4; ++t) ld128_wq(wqf[t], vo_wq, rs_wq, (u32)(t * 8192));
  const u32 vo_wuk = (u32)((64 * j + 16 * sw + idx) * 8192 + h * 256 + g * 16);
  const u32 vo_f1 = (u32)((64 * j + 16 * sw + 4 * g) * 3584 + ftile * 256 + idx * 16);
  const u32 kvrow0 = (u32)((ab * 4096 + as * 128 + 32 * sw) * 1024);
  u32 dvo[8];
#pragma unroll
  for (int i = 0; i < 8; ++i) { dvo[i] = (u32)((l ^ (2 * i)) * 16); PIN(dvo[i]); }
#define KV(i) dma16_kv(lbase + L_R + (u32)((32 * sw + (i)) * 1024), dvo[(i) & 7], rs_kv, kvrow0 + (u32)((i) * 1024))
  const int q_ = idx >> 2, p_ = idx & 3, jp = idx & 7;
  u32 a_xn0 = L_XN + (u32)((idx >> 3) * 1024 + (64 * wb + 16 * g) * 2);
  u32 a_fx = L_XN + (u32)((idx >> 3) * 1024 + (64 * j + 16 * sw + 4 * g) * 2);
  u32 sel_lo[4], sel_hi[4];
#pragma unroll
  for (int i = 0; i < 4; ++i) {
    const bool on = (i == (jp >> 1));
    sel_lo[i] = on ? ((jp & 1) ? 0x01000c0cu : 0x0c0c0100u) : 0x0c0c0c0cu;
    sel_hi[i] = on ? ((jp & 1) ? 0x03020c0cu : 0x0c0c0302u) : 0x0c0c0c0cu;
    PIN(sel_lo[i]); PIN(sel_hi[i]);
  }
  u32 a_qpw = L_U + (u32)(((wb * 4 + (g >> 1)) * 128 + 8 * idx + 4 * (g & 1)) * 4);
  u32 a_qpr = L_U + (u32)((tid & 255) * 8);
  u32 a_qbw = L_QB + (u32)((tid & 255) * 4);
  u32 a_qbr = L_QB + (u32)(((idx & 3) * 128 + 8 * g) * 2);
  u32 o_qa = QA_OFF + (u32)((((idx & 3) * 32 + h) * 256 + (64 * j + 16 * sw + 4 * g) / 2) * 8);
  PIN(a_xn0); PIN(a_fx); PIN(a_qpw); PIN(a_qpr); PIN(a_qbw); PIN(a_qbr); PIN(o_qa);

  STAMP(1);
  if (chain) {
    vmwait<4>();
    PIN(xv);
    float v[8];
    v[0] = bf2f_lo(xv.x); v[1] = bf2f_hi(xv.x); v[2] = bf2f_lo(xv.y); v[3] = bf2f_hi(xv.y);
    v[4] = bf2f_lo(xv.z); v[5] = bf2f_hi(xv.z); v[6] = bf2f_lo(xv.w); v[7] = bf2f_hi(xv.w);
    float s = ((v[0] + v[1]) + (v[2] + v[3])) + ((v[4] + v[5]) + (v[6] + v[7]));
    s = wave_sum(s);
    const float mean = s * (1.f / 512.f);
    float ss = 0.f;
#pragma unroll
    for (int i = 0; i < 8; ++i) { const float d = v[i] - mean; ss = fmaf(d, d, ss); }
    ss = wave_sum(ss);
    const float var = rbf(ss * (1.f / 512.f));
    const float mb = rbf(mean);
    const float den = rbf(__builtin_amdgcn_sqrtf(rbf(var + 1e-5f)));
    const float rd = __builtin_amdgcn_rcpf(den);
    u32x4 o;
    o.x = pack2(rbf(v[0] - mb) * rd, rbf(v[1] - mb) * rd);
    o.y = pack2(rbf(v[2] - mb) * rd, rbf(v[3] - mb) * rd);
    o.z = pack2(rbf(v[4] - mb) * rd, rbf(v[5] - mb) * rd);
    o.w = pack2(rbf(v[6] - mb) * rd, rbf(v[7] - mb) * rd);
    *(LAS u32x4*)(L + L_XN + w * 1024 + l * 16) = o;
    asm volatile("s_waitcnt lgkmcnt(0)" ::: "memory");
    if (l == 0) __hip_atomic_fetch_add((LAS u32*)(L + L_XF), 1u, __ATOMIC_RELAXED, __HIP_MEMORY_SCOPE_WORKGROUP);
  }
#pragma unroll
  for (int t = 4; t < 16; ++t) ld128_wq(wqf[t], vo_wq, rs_wq, (u32)(t * 8192));
#pragma unroll
  for (int t = 0; t < 4; ++t) { if (chain) ld128(aux[t], vo_wuk, rs_wuk, (u32)(t * 64)); else ld128_w(aux[t], vo_f1, rs_f1, (u32)(t * 3584)); }
  if (!chain) { KV(0); KV(1); KV(2); KV(3); }
  STAMP(2);

  u32x2 fx[2];
  u32 epoch;
  {
    asm volatile("s_load_dword %0, %1, 0x0\n\ts_waitcnt lgkmcnt(0)" : "=s"(epoch) : "s"(ws) : "memory");
    epoch += 1u;
    while (__hip_atomic_load((LAS u32*)(L + L_XF), __ATOMIC_RELAXED, __HIP_MEMORY_SCOPE_WORKGROUP) < 4u) __builtin_amdgcn_s_sleep(1);
    asm volatile("" ::: "memory");
    u32x4 xp[2][2];
    xp[0][0] = lds128(L, a_xn0); xp[0][1] = lds128(L, a_xn0 + 16); xp[1][0] = lds128(L, a_xn0 + 2048); xp[1][1] = lds128(L, a_xn0 + 2064);
    fx[0] = *(LAS u32x2*)(L + a_fx); fx[1] = *(LAS u32x2*)(L + a_fx + 2048);
    f32x4 qa0 = {0.f, 0.f, 0.f, 0.f}, qa1 = {0.f, 0.f, 0.f, 0.f};
#define DIAG_A(p, sel) u32x4{__builtin_amdgcn_perm(p, p, sel[0]), __builtin_amdgcn_perm(p, p, sel[1]), __builtin_amdgcn_perm(p, p, sel[2]), __builtin_amdgcn_perm(p, p, sel[3])}
#define QSTEP(t) do { \
      const u32 p0 = xp[0][(t) >> 3][((t) >> 1) & 3], p1 = xp[1][(t) >> 3][((t) >> 1) & 3]; \
      const u32* sel = ((t) & 1) ? sel_hi : sel_lo; \
      const u32x4 a0 = DIAG_A(p0, sel), a1 = DIAG_A(p1, sel); \
      vmwait<19 - (t)>(); PIN(wqf[t]); \
      qa0 = mfma32(a0, wqf[t], qa0); qa1 = mfma32(a1, wqf[t], qa1); } while (0)
    QSTEP(0); QSTEP(1); QSTEP(2); QSTEP(3); QSTEP(4); QSTEP(5); QSTEP(6); QSTEP(7);
    QSTEP(8); QSTEP(9); QSTEP(10); QSTEP(11); QSTEP(12); QSTEP(13); QSTEP(14); QSTEP(15);
    *(LAS f32x4*)(L + a_qpw) = qa0;
    *(LAS f32x4*)(L + a_qpw + 1024) = qa1;
  }
  if (chain) VMWAIT(0); else vmwait<4>();
  PIN(aux[0]); PIN(aux[1]); PIN(aux[2]); PIN(aux[3]);
  STAMP(3);
  BAR();
  if (chain) {
    u32x2 pv[8];
#pragma unroll
    for (int ww = 0; ww < 8; ++ww) pv[ww] = *(LAS u32x2*)(L + a_qpr + ww * 2048);
    float s0, s1;
    s0 = ((__uint_as_float(pv[0].x) + __uint_as_float(pv[1].x)) + (__uint_as_float(pv[2].x) + __uint_as_float(pv[3].x))) + ((__uint_as_float(pv[4].x) + __uint_as_float(pv[5].x)) + (__uint_as_float(pv[6].x) + __uint_as_float(pv[7].x)));
    s1 = ((__uint_as_float(pv[0].y) + __uint_as_float(pv[1].y)) + (__uint_as_float(pv[2].y) + __uint_as_float(pv[3].y))) + ((__uint_as_float(pv[4].y) + __uint_as_float(pv[5].y)) + (__uint_as_float(pv[6].y) + __uint_as_float(pv[7].y)));
    *(LAS u32*)(L + a_qbw) = pack2(s0, s1);
  } else {
    KV(4); KV(5); KV(6); KV(7);
    f32x4 f0a = {0.f, 0.f, 0.f, 0.f}, f1a = {0.f, 0.f, 0.f, 0.f};
#pragma unroll
    for (int t = 0; t < 4; ++t) {
      const u32 p0 = (t >> 1) ? fx[0].y : fx[0].x, p1 = (t >> 1) ? fx[1].y : fx[1].x;
      const u32* sel = (t & 1) ? sel_hi : sel_lo;
      f0a = mfma32(DIAG_A(p0, sel), aux[t], f0a); f1a = mfma32(DIAG_A(p1, sel), aux[t], f1a);
    }
    const u32 a_fpw = L_FP + (u32)(((sw * 4 + (g >> 1)) * 128 + 8 * idx + 4 * (g & 1)) * 4);
    *(LAS f32x4*)(L + a_fpw) = f0a;
    *(LAS f32x4*)(L + a_fpw + 1024) = f1a;
  }
  BAR();
  bool fail = false;
  u32 a_qir[4], a_kvr[4], trb[4], trb2[4];
  u32 a_smw, a_smr, a_pw, a_pr, o_e3w, bp_src, o_stw;
#define LATE_ADDR() do { \
    _Pragma("unroll") for (int c = 0; c < 4; ++c) { const u32 uu = (u32)(4 * c + g); \
      a_qir[c] = L_U + uu * 256 + (((u32)idx ^ uu) * 16); \
      a_kvr[c] = L_R + (u32)((16 * w + idx) * 1024) + ((uu ^ (u32)(2 * (idx & 7))) * 16); \
      const u32 pos = (u32)((8 * w + 2 * c + (p_ >> 1)) ^ (8 * (g & 1) + 2 * q_)); \
      trb[c] = L_R + (u32)((4 * g + q_) * 1024) + pos * 16 + (u32)(8 * (p_ & 1)); trb2[c] = trb[c] + 65536u; \
      PIN(a_qir[c]); PIN(a_kvr[c]); PIN(trb[c]); PIN(trb2[c]); } \
    a_smw = L_SM + (u32)((w * 16 + idx) * 4); a_smr = L_SM + (u32)(idx * 4); \
    a_pw = L_P + (u32)((((w >> 1) * 4 + g) * 16 + idx) * 16 + 8 * (w & 1)); a_pr = L_P + (u32)(l * 16); \
    o_e3w = E3_OFF + (u32)(((((16 * ag + (l >> 2)) * 8 + w) * 4 + ab) * 32 + as) * 128 + (l & 3) * 16); \
    bp_src = (u32)(((l >> 2) + 16 * (l & 3)) * 4); \
    o_stw = ST_OFF + (u32)((((16 * ag + idx) * 4 + ab) * 32 + as) * 8); \
    PIN(a_smw); PIN(a_smr); PIN(a_pw); PIN(a_pr); PIN(o_e3w); PIN(bp_src); PIN(o_stw); } while (0)
  if (chain) {
    u32x4 bq[4];
#pragma unroll
    for (int kk = 0; kk < 4; ++kk) bq[kk] = lds128(L, a_qbr + kk * 64);
    f32x4 acc = {0.f, 0.f, 0.f, 0.f}, acc2 = {0.f, 0.f, 0.f, 0.f};
    acc = mfma32(aux[0], bq[0], acc); acc2 = mfma32(aux[1], bq[1], acc2);
    acc = mfma32(aux[2], bq[2], acc); acc2 = mfma32(aux[3], bq[3], acc2);
    constexpr float SC = 0.08838834764831845f * 1.4426950408889634f;
    u32x4 o; o.x = pack2((acc[0] + acc2[0]) * SC, (acc[1] + acc2[1]) * SC); o.y = epoch; o.z = pack2((acc[2] + acc2[2]) * SC, (acc[3] + acc2[3]) * SC); o.w = epoch;
    if (idx < 4) st128_sc1(o, o_qa, rs_ws, 0u);
    STAMP(4);
    {
      const int t = tid;
      const u32 o_efw = EF_OFF + (u32)((2 * ftile + ((t & 63) >> 5)) * 32768 + ((fmat * 8 + j) * 4 + (t >> 6)) * 512 + (t & 31) * 16);
      u32x2 pv[4];
#pragma unroll
      for (int ww = 0; ww < 4; ++ww) pv[ww] = *(LAS u32x2*)(L + L_FP + t * 8 + ww * 2048);
      const float s0 = (__uint_as_float(pv[0].x) + __uint_as_float(pv[1].x)) + (__uint_as_float(pv[2].x) + __uint_as_float(pv[3].x));
      const float s1 = (__uint_as_float(pv[0].y) + __uint_as_float(pv[1].y)) + (__uint_as_float(pv[2].y) + __uint_as_float(pv[3].y));
      if (ffn_ok) st128_sc1(u32x4{__float_as_uint(s0), epoch, __float_as_uint(s1), epoch}, o_efw, rs_ws, 0u);
    }
    LATE_ADDR();
    const u32 o_e2 = QA_OFF + (u32)((ab * 32 + 16 * ag) * 2048 + w * 8192 + l * 16);
    u32 a_qiw[4];
#pragma unroll
    for (int k2 = 0; k2 < 4; ++k2) { const u32 uu = (u32)(l >> 1); a_qiw[k2] = L_U + uu * 256 + (((u32)(4 * w + k2) ^ (uu & 15)) * 16) + (u32)(8 * (l & 1)); PIN(a_qiw[k2]); }
    const u32 o_efr = EF_OFF + (u32)(uc * 32768 + (tid >> 7) * 16384 + (tid & 127) * 16);
    __builtin_amdgcn_s_sleep(E2_SLEEP);
    STAMP(13);
    u32x4 q4[8];
    for (u32 spin = 0;; ++spin) {
#pragma unroll
      for (int k = 0; k < 8; ++k) ld128_sc1(q4[k], o_e2, rs_ws, (u32)(k * 1024));
      VMWAIT(0);
#pragma unroll
      for (int k = 0; k < 8; ++k) PIN(q4[k]);
      if (__all(tags_ok(q4, 8, epoch))) break;
      if (spin > SPIN_LIMIT) { fail = true; break; }
    }
    STAMP(5);
#pragma unroll
    for (int k = 0; k < 8; ++k) {
      *(LAS u32*)(L + a_qiw[k >> 1] + (k & 1) * 8192) = q4[k].x;
      *(LAS u32*)(L + a_qiw[k >> 1] + (k & 1) * 8192 + 4) = q4[k].z;
    }
    for (u32 spin = 0; ffn_ok; ++spin) {
#pragma unroll
      for (int k = 0; k < 8; ++k) ld128_sc1(q4[k], o_efr, rs_ws, (u32)(k * 2048));
      VMWAIT(0);
#pragma unroll
      for (int k = 0; k < 8; ++k) PIN(q4[k]);
      if (__all(tags_ok(q4, 8, epoch))) break;
      if (spin > SPIN_LIMIT) { fail = true; break; }
    }
    float s0 = 0.f, s1 = 0.f;
#pragma unroll
    for (int k = 0; k < 8; ++k) { s0 += __uint_as_float(q4[k].x); s1 += __uint_as_float(q4[k].z); }
    *(LAS u32x2*)(L + L_HS + tid * 8) = u32x2{__float_as_uint(s0), __float_as_uint(s1)};
  } else {
    const u32x4 sv = {SENT, SENT, SENT, SENT};
    const u32 oc = E3_OFF + (u32)((h * 8 + j) * 16384 + (tid - 256) * 16);
    const u32 oc2 = E5S_OFF + (u32)((bid >> 1) * 16384 + (bid & 1) * 8192 + (tid - 256) * 16);
    KV(8); st128_sc1(sv, oc, rs_ws, 0u);
    KV(9); st128_sc1(sv, oc, rs_ws, 4096u);
    KV(10); st128_sc1(sv, oc, rs_ws, 8192u);
    KV(11); st128_sc1(sv, oc, rs_ws, 12288u);
    LATE_ADDR();
    vmwait<KS>(); KV(12); st128_sc1(sv, oc2, rs_ws, 0u);
    vmwait<KS>(); KV(13); st128_sc1(sv, oc2, rs_ws, 4096u);
    vmwait<KS>(); KV(14); vmwait<KS>(); KV(15); vmwait<KS>(); KV(16); vmwait<KS>(); KV(17);
    vmwait<KS>(); KV(18); vmwait<KS>(); KV(19); vmwait<KS>(); KV(20); vmwait<KS>(); KV(21);
    vmwait<KS>(); KV(22); vmwait<KS>(); KV(23); vmwait<KS>(); KV(24);
    vmwait<11>();
    {
      u32 old = 0;
      if (l == 0) old = __hip_atomic_fetch_add((LAS u32*)(L + L_CNT), 1u, __ATOMIC_RELAXED, __HIP_MEMORY_SCOPE_WORKGROUP);
      old = (u32)__builtin_amdgcn_readfirstlane(old);
      if (old == 3u && l == 0) st64_sc1(u32x2{epoch, epoch}, CF_OFF + (u32)(((h >> 4) * 128 + (h & 15) * 8 + j) * 8), rs_ws);
    }
    vmwait<KS>(); KV(25); vmwait<KS>(); KV(26); vmwait<KS>(); KV(27); vmwait<KS>(); KV(28);
    vmwait<KS>(); KV(29); vmwait<KS>(); KV(30); vmwait<KS>(); KV(31);
    VMWAIT(0);
  }
  BAR();
  u32x4 bq[16];
#pragma unroll
  for (int kk = 0; kk < 16; ++kk) bq[kk] = lds128(L, a_qir[kk & 3] + (kk >> 2) * 4096);
  if (w < 2) {
    const u32x2 g0 = *(LAS u32x2*)(L + L_HS + tid * 8), u0 = *(LAS u32x2*)(L + L_HS + 1024 + tid * 8);
    const float ga = rbf(__uint_as_float(g0.x)), gb = rbf(__uint_as_float(g0.y));
    const float ua = rbf(__uint_as_float(u0.x)), ub = rbf(__uint_as_float(u0.y));
    const float sa = rbf(ga * __builtin_amdgcn_rcpf(1.0f + __expf(-ga))), sb = rbf(gb * __builtin_amdgcn_rcpf(1.0f + __expf(-gb)));
    const int f = 2 * (tid & 31), b = tid >> 5;
    *(LAS u32*)(L + L_HF + (u32)(((((f >> 5) * 4 + ((f >> 2) & 3)) * 4 + b) * 8 + 4 * ((f >> 4) & 1) + (f & 3)) * 2)) = pack2(sa * ua, sb * ub);
  }
  f32x4 sc;
  {
    u32x4 ak[16];
#pragma unroll
    for (int kk = 0; kk < 16; ++kk) ak[kk] = lds128(L, a_kvr[kk & 3] + (kk >> 2) * 256);
    SCHED();
    f32x4 s0 = {0.f, 0.f, 0.f, 0.f}, s1 = {0.f, 0.f, 0.f, 0.f};
#pragma unroll
    for (int kk = 0; kk < 16; kk += 2) { s0 = mfma32(ak[kk], bq[kk], s0); s1 = mfma32(ak[kk + 1], bq[kk + 1], s1); }
    sc = s0 + s1;
    float m = fmaxf(fmaxf(sc[0], sc[1]), fmaxf(sc[2], sc[3]));
    m = x16_max(m); m = x32_max(m);
    if (g == 0) *(LAS float*)(L + a_smw) = m;
  }
  STAMP(6);
  BAR();
  u32x4 cf4;
  ld128_sc1(cf4, CF_OFF + (u32)(ag * 1024 + l * 16), rs_ws, 0u);
  float mall;
  {
    float mv[8];
#pragma unroll
    for (int ww = 0; ww < 8; ++ww) mv[ww] = *(LAS float*)(L + a_smr + ww * 64);
    const float m = fmaxf(fmaxf(fmaxf(mv[0], mv[1]), fmaxf(mv[2], mv[3])), fmaxf(fmaxf(mv[4], mv[5]), fmaxf(mv[6], mv[7])));
    mall = m;
    const float p0 = __builtin_amdgcn_exp2f(sc[0] - m), p1 = __builtin_amdgcn_exp2f(sc[1] - m), p2 = __builtin_amdgcn_exp2f(sc[2] - m), p3 = __builtin_amdgcn_exp2f(sc[3] - m);
    u32x2 pv; pv.x = pack2(p0, p1); pv.y = pack2(p2, p3);
    *(LAS u32x2*)(L + a_pw) = pv;
    float s = (p0 + p1) + (p2 + p3);
    s = x16_sum(s); s = x32_sum(s);
    if (g == 0) *(LAS float*)(L + a_smw + 512) = s;
  }
  for (u32 spin = 0;; ++spin) {
    VMWAIT(0);
    PIN(cf4);
    if (__all(cf4.x == epoch && cf4.y == epoch && cf4.z == epoch && cf4.w == epoch)) break;
    if (spin > SPIN_LIMIT) { fail = true; break; }
    ld128_sc1(cf4, CF_OFF + (u32)(ag * 1024 + l * 16), rs_ws, 0u);
  }
  BAR();
  {
#pragma unroll
    for (int i = 0; i < 2; ++i) { const int row = 8 * w + 4 * i + g; dma16(lbase + L_U + (u32)((8 * w + 4 * i) * 256), (u32)(row * 8192 + ((idx ^ (2 * (row & 7))) * 16)), rs_wuv, (u32)(64 * j * 8192 + h * 256)); }
    const u32 vo_wd = (u32)((l >> 3) * 1024 + (((l & 7) ^ (2 * ((l >> 4) & 3))) * 16));
    dma16(lbase + L_WDN + (u32)(w * 1024), vo_wd, rs_wd, (u32)((uc * 64 + 8 * w) * 1024 + j * 128));
  }
  {
    u32x4 pf4[4], bk[4][4];
    float sl[8];
#pragma unroll
    for (int ks = 0; ks < 4; ++ks) pf4[ks] = lds128(L, a_pr + ks * 1024);
#pragma unroll
    for (int ct = 0; ct < 4; ++ct) {
      bk[ct][0] = trfrag(L, trb[ct], 0, 16384); bk[ct][1] = trfrag(L, trb[ct], 32768, 49152);
      bk[ct][2] = trfrag(L, trb2[ct], 0, 16384); bk[ct][3] = trfrag(L, trb2[ct], 32768, 49152);
    }
#pragma unroll
    for (int ww = 0; ww < 8; ++ww) sl[ww] = *(LAS float*)(L + a_smr + 512 + ww * 64);
    SCHED();
    f32x4 o[4];
#pragma unroll
    for (int ct = 0; ct < 4; ++ct) o[ct] = f32x4{0.f, 0.f, 0.f, 0.f};
#pragma unroll
    for (int ks = 0; ks < 4; ++ks)
#pragma unroll
      for (int ct = 0; ct < 4; ++ct) o[ct] = mfma32(bk[ct][ks], pf4[ks], o[ct]);
    const float ls = ((sl[0] + sl[1]) + (sl[2] + sl[3])) + ((sl[4] + sl[5]) + (sl[6] + sl[7]));
    const float inv = __builtin_amdgcn_rcpf(ls);
    u32 pk[8];
#pragma unroll
    for (int ct = 0; ct < 4; ++ct) { pk[2 * ct] = pack2(o[ct][0] * inv, o[ct][1] * inv); pk[2 * ct + 1] = pack2(o[ct][2] * inv, o[ct][3] * inv); }
#pragma unroll
    for (int i = 0; i < 8; ++i) pk[i] = min((u32)__builtin_amdgcn_ds_bpermute((int)bp_src, (int)pk[i]), SENT - 1u);
    st128_sc1(u32x4{pk[0], pk[1], pk[2], pk[3]}, o_e3w, rs_ws, 0u);
    st128_sc1(u32x4{pk[4], pk[5], pk[6], pk[7]}, o_e3w, rs_ws, 64u);
    if (w == 0) {
      const float lse = mall + __builtin_amdgcn_logf(ls);
      u32x2 v; v.x = __float_as_uint(lse); v.y = epoch;
      if (g == 0) st64_sc1(v, o_stw, rs_ws);
    }
  }
  STAMP(7);
  BAR();
  {
    const int wr = (w + j) & 7;
#pragma unroll
    for (int i = 0; i < 8; ++i)
      dma16_wo(lbase + L_R + (u32)((8 * wr + i) * 1024), dvo[i], rs_wo, (u32)((h * 128 + 8 * wr + i) * 1024));
  }
  const int lb = w >> 1, lp4 = 4 * (w & 1) + (l & 3), lss = l >> 2;
  u32 o_e3r = E3_OFF + (u32)(((h * 8 + j) * 4 + lb) * 32 * 128 + lss * 128 + lp4 * 16);
  u32 o_str = ST_OFF + (u32)(((h * 4 + lb) * 32 + (l & 31)) * 8);
  u32 a_wuvr = L_U + (u32)((4 * g + q_) * 256) + (u32)(((2 * w + (p_ >> 1)) ^ (8 * (g & 1) + 2 * q_)) * 16) + (u32)(8 * (p_ & 1));
  u32 a_latr = L_LAT + (u32)((g * 4 + (idx & 3)) * 16);
  u32 a_ctxw = L_CTX + (u32)((((w >> 1) * 4 + g) * 4 + (idx & 3)) * 16 + 8 * (w & 1));
  u32 a_ctxr = L_CTX + (u32)((g * 4 + (idx & 3)) * 16);
  u32 o_e5s = E5S_OFF + (u32)(((16 * w + 4 * (idx >> 2) + g) * 256 + bid) * 64 + (idx & 3) * 16);
  u32 a_latw;
  {
    const int e = ((l >> 5) & 1) * 4 + ((l >> 4) & 1) * 2 + ((l >> 3) & 1), k = e >> 1;
    const int c = 16 * (2 * (lp4 >> 2) + (k >> 1)) + 4 * (lp4 & 3) + 2 * (k & 1) + (e & 1);
    a_latw = L_LAT + (u32)(((((c >> 5) * 4 + ((c >> 2) & 3)) * 4 + lb) * 8 + 4 * ((c >> 4) & 1) + (c & 3)) * 2);
  }
  PIN(o_e3r); PIN(o_str); PIN(a_wuvr); PIN(a_latr); PIN(a_ctxw); PIN(a_ctxr); PIN(o_e5s); PIN(a_latw);
  {
    const int wr = (w + j) & 7;
#pragma unroll
    for (int i = 0; i < 8; ++i)
      dma16_wo(lbase + L_R + (u32)((64 + 8 * wr + i) * 1024), dvo[i], rs_wo, (u32)((h * 128 + 64 + 8 * wr + i) * 1024));
  }
  STAMP(8);
  {
    u32x4 e4[2]; u32x2 st;
    for (u32 spin = 0;; ++spin) {
      ld64_sc1(st, o_str, rs_ws, 0u);
      ld128_sc1(e4[0], o_e3r, rs_ws, 0u); ld128_sc1(e4[1], o_e3r, rs_ws, 2048u);
      VMWAIT(0);
      PIN(e4[0]); PIN(e4[1]); PIN(st);
      const u32 mx = max(max(max(e4[0].x, e4[0].y), max(e4[0].z, e4[0].w)), max(max(e4[1].x, e4[1].y), max(e4[1].z, e4[1].w)));
      if (__all(mx != SENT && st.y == epoch)) break;
      if (spin > SPIN_LIMIT) { fail = true; break; }
    }
    STAMP(9);
    const float lse = __uint_as_float(st.x);
    const float M = half_max32(lse);
    const float e = __builtin_amdgcn_exp2f(lse - M);
    const float S = half_sum32(e);
    const float wgt = e * __builtin_amdgcn_rcpf(S);
    const float w0 = __builtin_bit_cast(float, __builtin_amdgcn_ds_bpermute(lss * 4, __builtin_bit_cast(int, wgt)));
    const float w1 = __builtin_bit_cast(float, __builtin_amdgcn_ds_bpermute((16 + lss) * 4, __builtin_bit_cast(int, wgt)));
    float v[8];
    v[0] = fmaf(w1, bf2f_lo(e4[1].x), w0 * bf2f_lo(e4[0].x)); v[1] = fmaf(w1, bf2f_hi(e4[1].x), w0 * bf2f_hi(e4[0].x));
    v[2] = fmaf(w1, bf2f_lo(e4[1].y), w0 * bf2f_lo(e4[0].y)); v[3] = fmaf(w1, bf2f_hi(e4[1].y), w0 * bf2f_hi(e4[0].y));
    v[4] = fmaf(w1, bf2f_lo(e4[1].z), w0 * bf2f_lo(e4[0].z)); v[5] = fmaf(w1, bf2f_hi(e4[1].z), w0 * bf2f_hi(e4[0].z));
    v[6] = fmaf(w1, bf2f_lo(e4[1].w), w0 * bf2f_lo(e4[0].w)); v[7] = fmaf(w1, bf2f_hi(e4[1].w), w0 * bf2f_hi(e4[0].w));
#pragma unroll
    for (int i = 0; i < 4; ++i) { auto s = __builtin_amdgcn_permlane32_swap(__float_as_uint(v[i]), __float_as_uint(v[i + 4]), false, false); v[i] = __uint_as_float(s[0]) + __uint_as_float(s[1]); }
#pragma unroll
    for (int i = 0; i < 2; ++i) { auto s = __builtin_amdgcn_permlane16_swap(__float_as_uint(v[i]), __float_as_uint(v[i + 2]), false, false); v[i] = __uint_as_float(s[0]) + __uint_as_float(s[1]); }
    { const bool b3 = (l & 8) != 0; const float snd = b3 ? v[0] : v[1], keep = b3 ? v[1] : v[0]; v[0] = keep + dpp<XOR8>(snd); }
    {
      int t1 = __builtin_amdgcn_update_dpp(0, __builtin_bit_cast(int, v[0]), 0x114, 0xf, 0xa, false);
      t1 = __builtin_amdgcn_update_dpp(t1, __builtin_bit_cast(int, v[0]), 0x104, 0xf, 0x5, false);
      v[0] += __builtin_bit_cast(float, t1);
    }
    *(LAS u16*)(L + a_latw) = (u16)pack2(v[0], v[0]);
  }
  BAR();
  u32x4 wk3[4][4];
  {
    const u32x4 wa0 = trfrag(L, a_wuvr, 0, 4096), wa1 = trfrag(L, a_wuvr, 8192, 12288);
    const u32x4 lb0 = lds128(L, a_latr), lb1 = lds128(L, a_latr + 256);
#pragma unroll
    for (int mt = 0; mt < 4; ++mt) {
      wk3[mt][0] = trfrag(L, trb[mt], 0, 16384); wk3[mt][1] = trfrag(L, trb[mt], 32768, 49152);
      wk3[mt][2] = trfrag(L, trb2[mt], 0, 16384); wk3[mt][3] = trfrag(L, trb2[mt], 32768, 49152);
    }
    f32x4 c = mfma32(wa0, lb0, f32x4{0.f, 0.f, 0.f, 0.f});
    c = mfma32(wa1, lb1, c);
    if (idx < 4) { u32x2 v; v.x = pack2(c[0], c[1]); v.y = pack2(c[2], c[3]); *(LAS u32x2*)(L + a_ctxw) = v; }
  }
  BAR();
  {
    u32x4 cf[4];
#pragma unroll
    for (int ks = 0; ks < 4; ++ks) cf[ks] = lds128(L, a_ctxr + ks * 256);
    f32x4 o[4];
#pragma unroll
    for (int mt = 0; mt < 4; ++mt) o[mt] = f32x4{0.f, 0.f, 0.f, 0.f};
#pragma unroll
    for (int ks = 0; ks < 4; ++ks)
#pragma unroll
      for (int mt = 0; mt < 4; ++mt) o[mt] = mfma32(wk3[mt][ks], cf[ks], o[mt]);
    const int rep = idx >> 2;
    f32x4 v = rep == 0 ? o[0] : rep == 1 ? o[1] : rep == 2 ? o[2] : o[3];
    u32x4 s0;
    s0.x = min(__float_as_uint(v[0]), SENT - 1u); s0.y = min(__float_as_uint(v[1]), SENT - 1u); s0.z = min(__float_as_uint(v[2]), SENT - 1u); s0.w = min(__float_as_uint(v[3]), SENT - 1u);
    st128_sc1(s0, o_e5s, rs_ws, 0u);
  }
  if (w < 4) {
    const int row = 4 * g + q_;
    const u32 a0 = L_WDN + (u32)(row * 128) + (u32)(((2 * w + (p_ >> 1)) ^ (2 * ((row >> 1) & 3))) * 16) + (u32)(8 * (p_ & 1));
    const u32x4 wa0 = trfrag(L, a0, 0, 2048), wa1 = trfrag(L, a0, 4096, 6144);
    const u32x4 hb0 = lds128(L, L_HF + (u32)((g * 4 + (idx & 3)) * 16)), hb1 = lds128(L, L_HF + 256 + (u32)((g * 4 + (idx & 3)) * 16));
    f32x4 y = mfma32(wa0, hb0, f32x4{0.f, 0.f, 0.f, 0.f});
    y = mfma32(wa1, hb1, y);
    const u32 off = E5Y_OFF + (u32)(((16 * j + 4 * w + g) * 32 + uc) * 128 + (idx & 3) * 32);
    if (ffn_ok && idx < 4) {
      st128_sc1(u32x4{__float_as_uint(y[0]), epoch, __float_as_uint(y[1]), epoch}, off, rs_ws, 0u);
      st128_sc1(u32x4{__float_as_uint(y[2]), epoch, __float_as_uint(y[3]), epoch}, off, rs_ws, 16u);
    }
  }
  STAMP(10);
  if (bid < 128) {
    u32x4 a4[2], y4;
    const u32 xfin = ((const u32*)x)[(l & 3) * 256 + bid * 2 + ((l >> 3) & 1)];
    const u32 o_r = E5S_OFF + (u32)(bid * 16384 + tid * 16);
    const u32 o_y = E5Y_OFF + (u32)(bid * 4096 + (tid & 255) * 16);
    const bool yv = tid < 224;
    for (u32 spin = 0;; ++spin) {
      ld128_sc1(y4, o_y, rs_ws, 0u);
      ld128_sc1(a4[0], o_r, rs_ws, 0u); ld128_sc1(a4[1], o_r, rs_ws, 8192u);
      VMWAIT(0);
      PIN(a4[0]); PIN(a4[1]); PIN(y4);
      const u32 mx = max(max(max(a4[0].x, a4[0].y), max(a4[0].z, a4[0].w)), max(max(a4[1].x, a4[1].y), max(a4[1].z, a4[1].w)));
      if (__all(mx != SENT && (!yv || (y4.y == epoch && y4.w == epoch)))) break;
      if (spin > SPIN_LIMIT) { fail = true; break; }
    }
    STAMP(11);
    float v[4];
    v[0] = __uint_as_float(a4[0].x) + __uint_as_float(a4[1].x); v[1] = __uint_as_float(a4[0].y) + __uint_as_float(a4[1].y);
    v[2] = __uint_as_float(a4[0].z) + __uint_as_float(a4[1].z); v[3] = __uint_as_float(a4[0].w) + __uint_as_float(a4[1].w);
#pragma unroll
    for (int i = 0; i < 2; ++i) { auto s = __builtin_amdgcn_permlane32_swap(__float_as_uint(v[i]), __float_as_uint(v[i + 2]), false, false); v[i] = __uint_as_float(s[0]) + __uint_as_float(s[1]); }
    { auto s = __builtin_amdgcn_permlane16_swap(__float_as_uint(v[0]), __float_as_uint(v[1]), false, false); v[0] = __uint_as_float(s[0]) + __uint_as_float(s[1]); }
    v[0] += dpp<0x124>(v[0]); v[0] += dpp<0x128>(v[0]);
    float y0 = yv ? __uint_as_float(y4.x) : 0.f, y1 = yv ? __uint_as_float(y4.z) : 0.f;
    y0 += dpp<XOR8>(y0); y1 += dpp<XOR8>(y1);
    y0 = x16_sum(y0); y1 = x16_sum(y1); y0 = x32_sum(y0); y1 = x32_sum(y1);
    if ((l & 12) == 0) *(LAS float*)(L + L_FIN + (u32)((w * 16 + (l >> 4) * 4 + (l & 3)) * 4)) = v[0];
    if (l < 8 && w < 4) *(LAS u32x2*)(L + L_HS + (u32)((w * 8 + l) * 8)) = u32x2{__float_as_uint(y0), __float_as_uint(y1)};
    BAR();
    if (w == 0) {
      const int mi = (l >> 2) & 3, b = l & 3;
      float sv[8], yv4[4];
#pragma unroll
      for (int ww = 0; ww < 8; ++ww) sv[ww] = *(LAS float*)(L + L_FIN + (u32)((ww * 16 + mi * 4 + b) * 4));
#pragma unroll
      for (int ww = 0; ww < 4; ++ww) yv4[ww] = *(LAS float*)(L + L_HS + (u32)(((ww * 8 + b * 2 + (mi >> 1)) * 2 + (mi & 1)) * 4));
      const float s = ((sv[0] + sv[1]) + (sv[2] + sv[3])) + ((sv[4] + sv[5]) + (sv[6] + sv[7]));
      const float y = (yv4[0] + yv4[1]) + (yv4[2] + yv4[3]);
      const float sb = rbf(s), yb = rbf(y);
      const float gg = rbf(yb * __builtin_amdgcn_rcpf(1.0f + __expf(-yb)));
      const float xv_ = (mi & 1) ? bf2f_hi(xfin) : bf2f_lo(xfin);
      float o = xv_ + rbf(sb * gg);
      if (fail) o = __uint_as_float(0x7fc00000u);
      if (l < 16) out[b * 512 + 4 * bid + mi] = (u16)(pack2(o, 0.f));
      if (bid == 0 && l == 0) st32_sc1(epoch, EPOCH_OFF, rs_ws);
    }
  }
}

extern "C" void kernel_launch(void* const* d_in, const int* in_sizes, int n_in, void* d_out, int out_size, void* d_ws, size_t ws_size, hipStream_t stream) {
  const u16* x = (const u16*)d_in[0]; const u16* ckv = (const u16*)d_in[1]; const u16* wq = (const u16*)d_in[2]; const u16* wuk = (const u16*)d_in[3];
  const u16* wuv = (const u16*)d_in[4]; const u16* wo = (const u16*)d_in[5]; const u16* wg = (const u16*)d_in[7]; const u16* wu = (const u16*)d_in[8]; const u16* wd = (const u16*)d_in[9];
  fused_decode<<<256, 512, 0, stream>>>(x, ckv, wq, wuk, wuv, wo, wg, wu, wd, (u16*)d_out, (unsigned char*)d_ws);
}
```

```cpp
#include <hip/hip_runtime.h>
#include <stdint.h>

typedef unsigned u32;
typedef unsigned short u16;
typedef u32 u32x4 __attribute__((ext_vector_type(4)));
typedef u32 u32x2 __attribute__((ext_vector_type(2)));
typedef float f32x4 __attribute__((ext_vector_type(4)));
typedef short s16x4 __attribute__((ext_vector_type(4)));
typedef __bf16 bf16x8 __attribute__((ext_vector_type(8)));
typedef __bf16 bf16x2 __attribute__((ext_vector_type(2)));
#define LAS __attribute__((address_space(3)))
typedef __amdgpu_buffer_rsrc_t rsrc_t;

#define DIAG 0
#define NT_W 0

constexpr u32 EPOCH_OFF = 0;
constexpr u32 QA_OFF = 4096;
constexpr u32 ST_OFF = QA_OFF + 4 * 32 * 2048;
constexpr u32 CF_OFF = ST_OFF + 32 * 4 * 32 * 8;
constexpr u32 EF_OFF = CF_OFF + 4096;
constexpr u32 E5Y_OFF = EF_OFF + 32 * 32768;
constexpr u32 E3_OFF = E5Y_OFF + 128 * 4096;
constexpr u32 E5S_OFF = E3_OFF + 256 * 16384;
constexpr u32 WS_END = E5S_OFF + 128 * 16384;
constexpr u32 DBG_OFF = WS_END;
constexpr u32 WS_TOTAL = DBG_OFF + 256 * 64 * 8;
constexpr u32 SENT = 0xFFFFFFFFu;

constexpr u32 L_R = 0;
constexpr u32 L_U = 131072;
constexpr u32 L_WDN = L_U + 16384;
constexpr u32 L_M = 155648;
constexpr u32 L_XN = L_M;
constexpr u32 L_P = L_M;
constexpr u32 L_QB = L_M + 4096;
constexpr u32 L_CTX = L_QB;
constexpr u32 L_SM = L_M + 5120;
constexpr u32 L_SL = L_M + 5632;
constexpr u32 L_LAT = L_M + 6144;
constexpr u32 L_HF = L_M + 6656;
constexpr u32 L_FIN = L_M + 7168;
constexpr u32 L_HS = L_M;
constexpr u32 L_FP = L_WDN;
constexpr u32 L_CNT = L_M + 7680;
constexpr u32 L_XF = L_M + 7696;
constexpr u32 LDS_BYTES = 163840;

constexpr u32 SPIN_LIMIT = 1u << 16;
#define KD 6
constexpr int PD = 8;
#define CPRE 10
#define KS 11
#define E2_SLEEP 8

__device__ __forceinline__ float bf2f_lo(u32 v) { return __uint_as_float(v << 16); }
__device__ __forceinline__ float bf2f_hi(u32 v) { return __uint_as_float(v & 0xffff0000u); }
__device__ __forceinline__ u32 pack2(float lo, float hi) { bf16x2 v = {(__bf16)lo, (__bf16)hi}; return __builtin_bit_cast(u32, v); }
__device__ __forceinline__ float rbf(float f) { return __uint_as_float(((u32)__builtin_bit_cast(u16, (__bf16)f)) << 16); }
__device__ __forceinline__ f32x4 mfma32(u32x4 a, u32x4 b, f32x4 c) {
  return __builtin_amdgcn_mfma_f32_16x16x32_bf16(__builtin_bit_cast(bf16x8, a), __builtin_bit_cast(bf16x8, b), c, 0, 0, 0);
}
__device__ __forceinline__ f32x4 mfma16(u32x2 a, u32x2 b, f32x4 c) {
  return __builtin_amdgcn_mfma_f32_16x16x16bf16_1k(__builtin_bit_cast(s16x4, a), __builtin_bit_cast(s16x4, b), c, 0, 0, 0);
}
template <int CTRL> __device__ __forceinline__ float dpp(float x) {
  return __builtin_bit_cast(float, __builtin_amdgcn_mov_dpp(__builtin_bit_cast(int, x), CTRL, 0xf, 0xf, true));
}
constexpr int XOR1 = 0xB1, XOR2 = 0x4E, XOR7 = 0x141, XOR15 = 0x140, XOR8 = 0x128;
__device__ __forceinline__ float x16_sum(float x) { auto s = __builtin_amdgcn_permlane16_swap(__float_as_uint(x), __float_as_uint(x), false, false); return __uint_as_float(s[0]) + __uint_as_float(s[1]); }
__device__ __forceinline__ float x32_sum(float x) { auto s = __builtin_amdgcn_permlane32_swap(__float_as_uint(x), __float_as_uint(x), false, false); return __uint_as_float(s[0]) + __uint_as_float(s[1]); }
__device__ __forceinline__ float x16_max(float x) { auto s = __builtin_amdgcn_permlane16_swap(__float_as_uint(x), __float_as_uint(x), false, false); return fmaxf(__uint_as_float(s[0]), __uint_as_float(s[1])); }
__device__ __forceinline__ float x32_max(float x) { auto s = __builtin_amdgcn_permlane32_swap(__float_as_uint(x), __float_as_uint(x), false, false); return fmaxf(__uint_as_float(s[0]), __uint_as_float(s[1])); }
__device__ __forceinline__ float wave_sum(float v) {
  v += dpp<XOR1>(v); v += dpp<XOR2>(v); v += dpp<XOR7>(v); v += dpp<XOR15>(v); v = x16_sum(v); v = x32_sum(v); return v;
}
__device__ __forceinline__ float half_sum32(float v) {
  v += dpp<XOR1>(v); v += dpp<XOR2>(v); v += dpp<XOR7>(v); v += dpp<XOR15>(v); v = x16_sum(v); return v;
}
__device__ __forceinline__ float half_max32(float v) {
  v = fmaxf(v, dpp<XOR1>(v)); v = fmaxf(v, dpp<XOR2>(v)); v = fmaxf(v, dpp<XOR7>(v)); v = fmaxf(v, dpp<XOR15>(v)); v = x16_max(v); return v;
}

#define BAR() asm volatile("s_waitcnt lgkmcnt(0)\n\ts_barrier" ::: "memory")
#define RAWBAR() asm volatile("s_barrier" ::: "memory")
#define VMWAIT(n) asm volatile("s_waitcnt vmcnt(" #n ")" ::: "memory")
#define SCHED() __builtin_amdgcn_sched_barrier(0)
template <int N> __device__ __forceinline__ void vmwait() { asm volatile("s_waitcnt vmcnt(%0)" :: "i"(N) : "memory"); }
#define PIN(a) asm volatile("" : "+v"(a))

#define NTS ""
__device__ __forceinline__ void ld128(u32x4& d, u32 voff, rsrc_t rs, u32 soff) {
  asm volatile("buffer_load_dwordx4 %0, %1, %2, %3 offen" : "=v"(d) : "v"(voff), "s"(rs), "s"(soff) : "memory");
}
__device__ __forceinline__ void ld128_w(u32x4& d, u32 voff, rsrc_t rs, u32 soff) {
  asm volatile("buffer_load_dwordx4 %0, %1, %2, %3 offen" NTS : "=v"(d) : "v"(voff), "s"(rs), "s"(soff) : "memory");
}
__device__ __forceinline__ void ld128_sc1(u32x4& d, u32 voff, rsrc_t rs, u32 soff) {
  asm volatile("buffer_load_dwordx4 %0, %1, %2, %3 offen sc1" : "=v"(d) : "v"(voff), "s"(rs), "s"(soff) : "memory");
}
__device__ __forceinline__ void ld64_sc1(u32x2& d, u32 voff, rsrc_t rs, u32 soff) {
  asm volatile("buffer_load_dwordx2 %0, %1, %2, %3 offen sc1" : "=v"(d) : "v"(voff), "s"(rs), "s"(soff) : "memory");
}
__device__ __forceinline__ void ld32_sc1(u32& d, u32 voff, rsrc_t rs, u32 soff) {
  asm volatile("buffer_load_dword %0, %1, %2, %3 offen sc1" : "=v"(d) : "v"(voff), "s"(rs), "s"(soff) : "memory");
}
__device__ __forceinline__ void ld32(u32& d, u32 voff, rsrc_t rs, u32 soff) {
  asm volatile("buffer_load_dword %0, %1, %2, %3 offen" : "=v"(d) : "v"(voff), "s"(rs), "s"(soff) : "memory");
}
__device__ __forceinline__ void st128_sc1(u32x4 d, u32 voff, rsrc_t rs, u32 soff) {
  asm volatile("buffer_store_dwordx4 %0, %1, %2, %3 offen sc1\n\ts_nop 1" :: "v"(d), "v"(voff), "s"(rs), "s"(soff) : "memory");
}
__device__ __forceinline__ void st64_sc1(u32x2 d, u32 voff, rsrc_t rs) {
  asm volatile("buffer_store_dwordx2 %0, %1, %2, 0 offen sc1\n\ts_nop 1" :: "v"(d), "v"(voff), "s"(rs) : "memory");
}
__device__ __forceinline__ void st32_sc1(u32 d, u32 voff, rsrc_t rs) {
  asm volatile("buffer_store_dword %0, %1, %2, 0 offen sc1\n\ts_nop 1" :: "v"(d), "v"(voff), "s"(rs) : "memory");
}
__device__ __forceinline__ void dma16(u32 lds_dst, u32 voff, rsrc_t rs, u32 soff) {
  asm volatile("s_mov_b32 m0, %0\n\ts_nop 0\n\tbuffer_load_dwordx4 %1, %2, %3 offen" NTS " lds" :: "s"(lds_dst), "v"(voff), "s"(rs), "s"(soff) : "memory");
}
__device__ __forceinline__ void dma16_def(u32 lds_dst, u32 voff, rsrc_t rs, u32 soff) {
  asm volatile("s_mov_b32 m0, %0\n\ts_nop 0\n\tbuffer_load_dwordx4 %1, %2, %3 offen lds" :: "s"(lds_dst), "v"(voff), "s"(rs), "s"(soff) : "memory");
}
#define KV_NT 1
#define KV_STAG 0
#define WO_NT 1
#define WQ_NT 1
#define dma16_kv dma16
#define dma16_wo dma16
#define ld128_wq ld128_w
__device__ __forceinline__ rsrc_t mk(const void* p, u32 bytes) { return __builtin_amdgcn_make_buffer_rsrc((void*)p, 0, bytes, 0x00020000); }
__device__ __forceinline__ u32x4 lds128(LAS unsigned char* L, u32 a) { return *(LAS u32x4*)(L + a); }
__device__ __forceinline__ u32x4 trfrag(LAS unsigned char* L, u32 a, u32 o0, u32 o1) {
  const s16x4 t0 = __builtin_amdgcn_ds_read_tr16_b64_v4i16((LAS s16x4*)(L + a + o0));
  const s16x4 t1 = __builtin_amdgcn_ds_read_tr16_b64_v4i16((LAS s16x4*)(L + a + o1));
  const u32x2 x = __builtin_bit_cast(u32x2, t0), y = __builtin_bit_cast(u32x2, t1);
  return u32x4{x.x, x.y, y.x, y.y};
}
__device__ __forceinline__ bool tags_ok(const u32x4* v, int n, u32 epoch) {
  u32 a = v[0].y & v[0].w, o = v[0].y | v[0].w;
#pragma unroll
  for (int k = 1; k < n; ++k) { a &= v[k].y & v[k].w; o |= v[k].y | v[k].w; }
  return a == epoch && o == epoch;
}

#define STAMP(i) do {} while (0)

__global__ void __launch_bounds__(512, 2)
fused_decode(const u16* __restrict__ x, const u16* __restrict__ ckv, const u16* __restrict__ wq, const u16* __restrict__ wuk,
             const u16* __restrict__ wuv, const u16* __restrict__ wo, const u16* __restrict__ wg, const u16* __restrict__ wu,
             const u16* __restrict__ wd, u16* __restrict__ out, unsigned char* __restrict__ ws) {
  __shared__ __attribute__((aligned(16))) unsigned char lds[LDS_BYTES];
  LAS unsigned char* L = (LAS unsigned char*)lds;
  const u32 lbase = (u32)(size_t)L;
  const int tid = threadIdx.x;
  const int w = __builtin_amdgcn_readfirstlane(tid >> 6);
  const int l = tid & 63, g = l >> 4, idx = l & 15;
  const int bid = blockIdx.x, xc = bid & 7, r = bid >> 3;
  const int h = xc + 8 * (r >> 3), j = r & 7;
  const int P = xc * 16 + (r & 15), ab = P >> 5, as = P & 31, ag = 1 - (r >> 4);
  const bool ffn_ok = h < 28;
  const int uc = ffn_ok ? h : 27;
  const int fmat = uc & 1, ftile = uc >> 1;
  STAMP(0);

  const rsrc_t rs_x = mk(x, 4096), rs_kv = mk(ckv, 4u * 4096 * 1024), rs_wq = mk(wq, 4u << 20), rs_wuk = mk(wuk, 4u << 20),
               rs_wuv = mk(wuv, 4u << 20), rs_wo = mk(wo, 4u << 20), rs_wg = mk(wg, 512 * 1792 * 2), rs_wu = mk(wu, 512 * 1792 * 2),
               rs_wd = mk(wd, 512 * 1792 * 2), rs_ws = mk(ws, WS_TOTAL);

  const bool chain = w < 4;
  const int sw = w & 3;
  const int q_ = idx >> 2, p_ = idx & 3, jp = idx & 7;
  const int blk2 = (j + 4 + sw) & 7;
  bool fail = false;
  u32 epoch;
  u32 dvo[8];
  u32 a_qir[4], a_kvr[4], trb[4], trb2[4];
  u32 a_smw, a_smr, a_pw, a_pr, o_e3w, bp_src, o_stw;
#define SEL_INIT() u32 sel_lo[4], sel_hi[4]; \
  _Pragma("unroll") for (int i = 0; i < 4; ++i) { const bool on = (i == (jp >> 1)); \
    sel_lo[i] = on ? ((jp & 1) ? 0x01000c0cu : 0x0c0c0100u) : 0x0c0c0c0cu; sel_hi[i] = on ? ((jp & 1) ? 0x03020c0cu : 0x0c0c0302u) : 0x0c0c0c0cu; \
    PIN(sel_lo[i]); PIN(sel_hi[i]); }
#define DIAG_A(p, sel) u32x4{__builtin_amdgcn_perm(p, p, sel[0]), __builtin_amdgcn_perm(p, p, sel[1]), __builtin_amdgcn_perm(p, p, sel[2]), __builtin_amdgcn_perm(p, p, sel[3])}
#define EPOCH_AND_XN() do { \
    asm volatile("s_load_dword %0, %1, 0x0\n\ts_waitcnt lgkmcnt(0)" : "=s"(epoch) : "s"(ws) : "memory"); epoch += 1u; \
    while (__hip_atomic_load((LAS u32*)(L + L_XF), __ATOMIC_RELAXED, __HIP_MEMORY_SCOPE_WORKGROUP) < 4u) __builtin_amdgcn_s_sleep(1); \
    asm volatile("" ::: "memory"); } while (0)
#define DSTEP(acc0, acc1, wf, p0, p1, par) do { const u32* sel_ = (par) ? sel_hi : sel_lo; \
    acc0 = mfma32(DIAG_A(p0, sel_), wf, acc0); acc1 = mfma32(DIAG_A(p1, sel_), wf, acc1); } while (0)
#define LATE_ADDR() do { \
    _Pragma("unroll") for (int c = 0; c < 4; ++c) { const u32 uu = (u32)(4 * c + g); \
      a_qir[c] = L_U + uu * 256 + (((u32)idx ^ uu) * 16); \
      a_kvr[c] = L_R + (u32)((16 * w + idx) * 1024) + ((uu ^ (u32)(2 * (idx & 7))) * 16); \
      const u32 pos = (u32)((8 * w + 2 * c + (p_ >> 1)) ^ (8 * (g & 1) + 2 * q_)); \
      trb[c] = L_R + (u32)((4 * g + q_) * 1024) + pos * 16 + (u32)(8 * (p_ & 1)); trb2[c] = trb[c] + 65536u; \
      PIN(a_qir[c]); PIN(a_kvr[c]); PIN(trb[c]); PIN(trb2[c]); } \
    _Pragma("unroll") for (int i = 0; i < 8; ++i) { dvo[i] = (u32)((l ^ (2 * i)) * 16); PIN(dvo[i]); } \
    a_smw = L_SM + (u32)((w * 16 + idx) * 4); a_smr = L_SM + (u32)(idx * 4); \
    a_pw = L_P + (u32)((((w >> 1) * 4 + g) * 16 + idx) * 16 + 8 * (w & 1)); a_pr = L_P + (u32)(l * 16); \
    o_e3w = E3_OFF + (u32)(((((16 * ag + (l >> 2)) * 8 + w) * 4 + ab) * 32 + as) * 128 + (l & 3) * 16); \
    bp_src = (u32)(((l >> 2) + 16 * (l & 3)) * 4); \
    o_stw = ST_OFF + (u32)((((16 * ag + idx) * 4 + ab) * 32 + as) * 8); \
    PIN(a_smw); PIN(a_smr); PIN(a_pw); PIN(a_pr); PIN(o_e3w); PIN(bp_src); PIN(o_stw); } while (0)
  const u32 a_qpw = L_U + (u32)(((w * 4 + (g >> 1)) * 128 + 8 * idx + 4 * (g & 1)) * 4);

  const int wb = (w + j) & 7;
  const u32 vo_wq = (u32)((64 * wb + 16 * g) * 8192 + h * 256 + idx * 16);
  const u32 a_xn = L_XN + (u32)((idx >> 3) * 1024 + (64 * wb + 16 * g) * 2);
  constexpr u32 L_WUK = L_R + 112 * 1024;
#define QLOOP(EXTRA) do { \
      const u32x4 xa0 = lds128(L, a_xn), xa1 = lds128(L, a_xn + 16), xb0 = lds128(L, a_xn + 2048), xb1 = lds128(L, a_xn + 2064); \
      f32x4 qa0 = {0.f, 0.f, 0.f, 0.f}, qa1 = {0.f, 0.f, 0.f, 0.f}; \
      QS(0, EXTRA, xa0[0], xb0[0]); QS(1, EXTRA, xa0[0], xb0[0]); QS(2, EXTRA, xa0[1], xb0[1]); QS(3, EXTRA, xa0[1], xb0[1]); \
      QS(4, EXTRA, xa0[2], xb0[2]); QS(5, EXTRA, xa0[2], xb0[2]); QS(6, EXTRA, xa0[3], xb0[3]); QS(7, EXTRA, xa0[3], xb0[3]); \
      QS(8, EXTRA, xa1[0], xb1[0]); QS(9, EXTRA, xa1[0], xb1[0]); QS(10, EXTRA, xa1[1], xb1[1]); QS(11, EXTRA, xa1[1], xb1[1]); \
      QS(12, EXTRA, xa1[2], xb1[2]); QS(13, EXTRA, xa1[2], xb1[2]); QS(14, EXTRA, xa1[3], xb1[3]); QS(15, EXTRA, xa1[3], xb1[3]); \
      *(LAS f32x4*)(L + a_qpw) = qa0; *(LAS f32x4*)(L + a_qpw + 1024) = qa1; } while (0)
#define QS(t, EXTRA, P0, P1) do { vmwait<15 - (t) + (EXTRA)>(); PIN(wqf[t]); DSTEP(qa0, qa1, wqf[t], P0, P1, (t) & 1); } while (0)
#define EPOCH() do { asm volatile("s_load_dword %0, %1, 0x0\n\ts_waitcnt lgkmcnt(0)" : "=s"(epoch) : "s"(ws) : "memory"); epoch += 1u; } while (0)

  if (chain) {
    u32x4 xv, wqf[16];
    __builtin_amdgcn_s_setprio(3);
    ld128(xv, (u32)(w * 1024 + l * 16), rs_x, 0u);
#pragma unroll
    for (int t = 0; t < CPRE; ++t) ld128_wq(wqf[t], vo_wq, rs_wq, (u32)(t * 8192));
    if (tid == 0) *(LAS u32*)(L + L_CNT) = 0u;
    SEL_INIT();
    const u32 a_qbr = L_QB + (u32)(((idx & 3) * 128 + 8 * g) * 2);
    const u32 o_qa = QA_OFF + (u32)((((idx & 3) * 32 + h) * 256 + (64 * j + 16 * sw + 4 * g) / 2) * 8);
    STAMP(1);
    {
      vmwait<CPRE>();
      PIN(xv);
      float v[8];
      v[0] = bf2f_lo(xv.x); v[1] = bf2f_hi(xv.x); v[2] = bf2f_lo(xv.y); v[3] = bf2f_hi(xv.y);
      v[4] = bf2f_lo(xv.z); v[5] = bf2f_hi(xv.z); v[6] = bf2f_lo(xv.w); v[7] = bf2f_hi(xv.w);
      float s = ((v[0] + v[1]) + (v[2] + v[3])) + ((v[4] + v[5]) + (v[6] + v[7]));
      s = wave_sum(s);
      const float mean = s * (1.f / 512.f);
      float ss = 0.f;
#pragma unroll
      for (int i = 0; i < 8; ++i) { const float d = v[i] - mean; ss = fmaf(d, d, ss); }
      ss = wave_sum(ss);
      const float var = rbf(ss * (1.f / 512.f));
      const float mb = rbf(mean);
      const float den = rbf(__builtin_amdgcn_sqrtf(rbf(var + 1e-5f)));
      const float rd = __builtin_amdgcn_rcpf(den);
      u32x4 o;
      o.x = pack2(rbf(v[0] - mb) * rd, rbf(v[1] - mb) * rd);
      o.y = pack2(rbf(v[2] - mb) * rd, rbf(v[3] - mb) * rd);
      o.z = pack2(rbf(v[4] - mb) * rd, rbf(v[5] - mb) * rd);
      o.w = pack2(rbf(v[6] - mb) * rd, rbf(v[7] - mb) * rd);
      *(LAS u32x4*)(L + L_XN + w * 1024 + l * 16) = o;
    }
#pragma unroll
    for (int t = CPRE; t < 16; ++t) ld128_wq(wqf[t], vo_wq, rs_wq, (u32)(t * 8192));
    EPOCH();
    BAR();
    STAMP(2);
    QLOOP(0);
    STAMP(3);
    BAR();
    u32x4 wuk[4];
    {
      u32x2 pv[8];
#pragma unroll
      for (int ww = 0; ww < 8; ++ww) pv[ww] = *(LAS u32x2*)(L + L_U + tid * 8 + ww * 2048);
      float s0, s1;
      s0 = ((__uint_as_float(pv[0].x) + __uint_as_float(pv[1].x)) + (__uint_as_float(pv[2].x) + __uint_as_float(pv[3].x))) + ((__uint_as_float(pv[4].x) + __uint_as_float(pv[5].x)) + (__uint_as_float(pv[6].x) + __uint_as_float(pv[7].x)));
      s1 = ((__uint_as_float(pv[0].y) + __uint_as_float(pv[1].y)) + (__uint_as_float(pv[2].y) + __uint_as_float(pv[3].y))) + ((__uint_as_float(pv[4].y) + __uint_as_float(pv[5].y)) + (__uint_as_float(pv[6].y) + __uint_as_float(pv[7].y)));
      *(LAS u32*)(L + L_QB + tid * 4) = pack2(s0, s1);
    }
    BAR();
    {
      u32x4 bq[4];
#pragma unroll
      for (int kk = 0; kk < 4; ++kk) bq[kk] = lds128(L, a_qbr + kk * 64);
#pragma unroll
      for (int kk = 0; kk < 4; ++kk) wuk[kk] = lds128(L, L_WUK + (u32)((16 * sw + idx) * 256) + (u32)(((4 * kk + g) ^ (2 * (idx & 7))) * 16));
      f32x4 acc = {0.f, 0.f, 0.f, 0.f}, acc2 = {0.f, 0.f, 0.f, 0.f};
      acc = mfma32(wuk[0], bq[0], acc); acc2 = mfma32(wuk[1], bq[1], acc2);
      acc = mfma32(wuk[2], bq[2], acc); acc2 = mfma32(wuk[3], bq[3], acc2);
      constexpr float SC = 0.08838834764831845f * 1.4426950408889634f;
      u32x4 o; o.x = pack2((acc[0] + acc2[0]) * SC, (acc[1] + acc2[1]) * SC); o.y = epoch; o.z = pack2((acc[2] + acc2[2]) * SC, (acc[3] + acc2[3]) * SC); o.w = epoch;
      if (idx < 4) st128_sc1(o, o_qa, rs_ws, 0u);
    }
    STAMP(4);
    BAR();
    {
      const int t = tid;
      const u32 o_efw = EF_OFF + (u32)((2 * ftile + ((t & 63) >> 5)) * 32768 + ((fmat * 8 + j) * 4 + (t >> 6)) * 512 + (t & 31) * 16);
      u32x2 pv[4];
#pragma unroll
      for (int ww = 0; ww < 4; ++ww) pv[ww] = *(LAS u32x2*)(L + L_FP + t * 8 + ww * 2048);
      const float s0 = (__uint_as_float(pv[0].x) + __uint_as_float(pv[1].x)) + (__uint_as_float(pv[2].x) + __uint_as_float(pv[3].x));
      const float s1 = (__uint_as_float(pv[0].y) + __uint_as_float(pv[1].y)) + (__uint_as_float(pv[2].y) + __uint_as_float(pv[3].y));
      if (ffn_ok) st128_sc1(u32x4{__float_as_uint(s0), epoch, __float_as_uint(s1), epoch}, o_efw, rs_ws, 0u);
    }
    LATE_ADDR();
    const u32 o_e2 = QA_OFF + (u32)((ab * 32 + 16 * ag) * 2048 + w * 8192 + l * 16);
    u32 a_qiw[4];
#pragma unroll
    for (int k2 = 0; k2 < 4; ++k2) { const u32 uu = (u32)(l >> 1); a_qiw[k2] = L_U + uu * 256 + (((u32)(4 * w + k2) ^ (uu & 15)) * 16) + (u32)(8 * (l & 1)); PIN(a_qiw[k2]); }
    __builtin_amdgcn_s_sleep(E2_SLEEP);
    STAMP(13);
    u32x4 q4[8];
    for (u32 spin = 0;; ++spin) {
#pragma unroll
      for (int k = 0; k < 8; ++k) ld128_sc1(q4[k], o_e2, rs_ws, (u32)(k * 1024));
      VMWAIT(0);
#pragma unroll
      for (int k = 0; k < 8; ++k) PIN(q4[k]);
      if (__all(tags_ok(q4, 8, epoch))) break;
      if (spin > SPIN_LIMIT) { fail = true; break; }
    }
    STAMP(5);
#pragma unroll
    for (int k = 0; k < 8; ++k) {
      *(LAS u32*)(L + a_qiw[k >> 1] + (k & 1) * 8192) = q4[k].x;
      *(LAS u32*)(L + a_qiw[k >> 1] + (k & 1) * 8192 + 4) = q4[k].z;
    }
  } else {
    u32x4 wqf[16], f1w[4];
    if (w == 7) {
      const u32x4 zz = {0u, 0u, 0u, 1u};
      st128_sc1(zz, QA_OFF + (u32)((((l >> 4) * 32 + h) * 256 + 32 * j) * 8 + (l & 15) * 16), rs_ws, 0u);
      if (j == 0) st128_sc1(zz, ST_OFF + (u32)(h * 1024 + l * 16), rs_ws, 0u);
      const u32 oe = EF_OFF + (u32)(2 * ftile * 32768 + (fmat * 8 + j) * 2048 + l * 16);
      if (ffn_ok) { st128_sc1(zz, oe, rs_ws, 0u); st128_sc1(zz, oe, rs_ws, 1024u); st128_sc1(zz, oe, rs_ws, 32768u); st128_sc1(zz, oe, rs_ws, 32768u + 1024u);
        const u32 oy = E5Y_OFF + (u32)(((16 * j + (l >> 3)) * 32 + uc) * 128 + (l & 7) * 16);
        st128_sc1(zz, oy, rs_ws, 0u); st128_sc1(zz, oy, rs_ws, 8u * 32u * 128u); }
    }
#pragma unroll
    for (int t = 0; t < 16; ++t) ld128_wq(wqf[t], vo_wq, rs_wq, (u32)(t * 8192));
    const u32 kvrow0 = (u32)((ab * 4096 + as * 128 + 32 * sw) * 1024);
#pragma unroll
    for (int i = 0; i < 8; ++i) { dvo[i] = (u32)((l ^ (2 * i)) * 16); PIN(dvo[i]); }
#define KV(i) dma16_kv(lbase + L_R + (u32)((32 * sw + (i)) * 1024), dvo[(i) & 7], rs_kv, kvrow0 + (u32)((i) * 1024))
    SEL_INIT();
    const u32 a_fx = L_XN + (u32)((idx >> 3) * 1024 + (64 * j + 16 * sw + 4 * g) * 2);
    const u32 vo3 = (u32)((64 * j + 16 * sw + 4 * g) * 3584 + ftile * 256 + idx * 16);
    const rsrc_t rs_f1 = fmat ? rs_wu : rs_wg;
    EPOCH();
    BAR();
#pragma unroll
    for (int i = 0; i < 4; ++i) { const int row = 16 * sw + 4 * i + g;
      dma16(lbase + L_WUK + (u32)((16 * sw + 4 * i) * 256), (u32)(row * 8192 + ((idx ^ (2 * (row & 7))) * 16)), rs_wuk, (u32)(64 * j * 8192 + h * 256)); }
#pragma unroll
    for (int t = 0; t < 4; ++t) ld128_w(f1w[t], vo3, rs_f1, (u32)(t * 3584));
    KV(0); KV(1); KV(2); KV(3);
    STAMP(2);
    const u32x2 fx0 = *(LAS u32x2*)(L + a_fx), fx1 = *(LAS u32x2*)(L + a_fx + 2048);
    QLOOP(12);
    STAMP(3);
    BAR();
    vmwait<8>();
    BAR();
    {
      vmwait<4>();
      PIN(f1w[0]); PIN(f1w[1]); PIN(f1w[2]); PIN(f1w[3]);
      f32x4 f0a = {0.f, 0.f, 0.f, 0.f}, f1a = {0.f, 0.f, 0.f, 0.f};
      DSTEP(f0a, f1a, f1w[0], fx0.x, fx1.x, 0); DSTEP(f0a, f1a, f1w[1], fx0.x, fx1.x, 1);
      DSTEP(f0a, f1a, f1w[2], fx0.y, fx1.y, 0); DSTEP(f0a, f1a, f1w[3], fx0.y, fx1.y, 1);
      const u32 a_fpw = L_FP + (u32)(((sw * 4 + (g >> 1)) * 128 + 8 * idx + 4 * (g & 1)) * 4);
      *(LAS f32x4*)(L + a_fpw) = f0a;
      *(LAS f32x4*)(L + a_fpw + 1024) = f1a;
    }
    BAR();
    const u32x4 sv = {SENT, SENT, SENT, SENT};
    const u32 oc = E3_OFF + (u32)((h * 8 + j) * 16384 + (tid - 256) * 16);
    const u32 oc2 = E5S_OFF + (u32)((bid >> 1) * 16384 + (bid & 1) * 8192 + (tid - 256) * 16);
    KV(4); st128_sc1(sv, oc, rs_ws, 0u);
    KV(5); st128_sc1(sv, oc, rs_ws, 4096u);
    KV(6); st128_sc1(sv, oc, rs_ws, 8192u);
    KV(7); st128_sc1(sv, oc, rs_ws, 12288u);
    KV(8); st128_sc1(sv, oc2, rs_ws, 0u);
    KV(9); st128_sc1(sv, oc2, rs_ws, 4096u);
    KV(10); KV(11);
    LATE_ADDR();
    vmwait<KS>(); KV(12); vmwait<KS>(); KV(13);
    vmwait<KS>(); KV(14); vmwait<KS>(); KV(15);
    vmwait<6>();
    {
      u32 old = 0;
      if (l == 0) old = __hip_atomic_fetch_add((LAS u32*)(L + L_CNT), 1u, __ATOMIC_RELAXED, __HIP_MEMORY_SCOPE_WORKGROUP);
      old = (u32)__builtin_amdgcn_readfirstlane(old);
      if (old == 3u && l == 0) st64_sc1(u32x2{epoch, epoch}, CF_OFF + (u32)(((h >> 4) * 128 + (h & 15) * 8 + j) * 8), rs_ws);
    }
    KV(16); KV(17); KV(18); KV(19); KV(20);
    vmwait<KS>(); KV(21); vmwait<KS>(); KV(22); vmwait<KS>(); KV(23); vmwait<KS>(); KV(24);
    vmwait<KS>(); KV(25); vmwait<KS>(); KV(26); vmwait<KS>(); KV(27); vmwait<KS>(); KV(28);
    vmwait<KS>(); KV(29); vmwait<KS>(); KV(30); vmwait<KS>(); KV(31);
    VMWAIT(0);
  }
  BAR();
  u32x4 bq[16];
#pragma unroll
  for (int kk = 0; kk < 16; ++kk) bq[kk] = lds128(L, a_qir[kk & 3] + (kk >> 2) * 4096);
  u32x4 cf4;
  ld128_sc1(cf4, CF_OFF + (u32)(ag * 1024 + l * 16), rs_ws, 0u);
  f32x4 sc;
  {
    u32x4 ak[16];
#pragma unroll
    for (int kk = 0; kk < 16; ++kk) ak[kk] = lds128(L, a_kvr[kk & 3] + (kk >> 2) * 256);
    SCHED();
    f32x4 s0 = {0.f, 0.f, 0.f, 0.f}, s1 = {0.f, 0.f, 0.f, 0.f};
#pragma unroll
    for (int kk = 0; kk < 16; kk += 2) { s0 = mfma32(ak[kk], bq[kk], s0); s1 = mfma32(ak[kk + 1], bq[kk + 1], s1); }
    sc = s0 + s1;
    float m = fmaxf(fmaxf(sc[0], sc[1]), fmaxf(sc[2], sc[3]));
    m = x16_max(m); m = x32_max(m);
    if (g == 0) *(LAS float*)(L + a_smw) = m;
  }
  STAMP(6);
  BAR();
  float mall;
  {
    float mv[8];
#pragma unroll
    for (int ww = 0; ww < 8; ++ww) mv[ww] = *(LAS float*)(L + a_smr + ww * 64);
    const float m = fmaxf(fmaxf(fmaxf(mv[0], mv[1]), fmaxf(mv[2], mv[3])), fmaxf(fmaxf(mv[4], mv[5]), fmaxf(mv[6], mv[7])));
    mall = m;
    const float p0 = __builtin_amdgcn_exp2f(sc[0] - m), p1 = __builtin_amdgcn_exp2f(sc[1] - m), p2 = __builtin_amdgcn_exp2f(sc[2] - m), p3 = __builtin_amdgcn_exp2f(sc[3] - m);
    u32x2 pv; pv.x = pack2(p0, p1); pv.y = pack2(p2, p3);
    *(LAS u32x2*)(L + a_pw) = pv;
    float s = (p0 + p1) + (p2 + p3);
    s = x16_sum(s); s = x32_sum(s);
    if (g == 0) *(LAS float*)(L + a_smw + 512) = s;
  }
  for (u32 spin = 0;; ++spin) {
    VMWAIT(0);
    PIN(cf4);
    if (__all(cf4.x == epoch && cf4.y == epoch && cf4.z == epoch && cf4.w == epoch)) break;
    if (spin > SPIN_LIMIT) { fail = true; break; }
    ld128_sc1(cf4, CF_OFF + (u32)(ag * 1024 + l * 16), rs_ws, 0u);
  }
  BAR();
  {
#pragma unroll
    for (int i = 0; i < 2; ++i) { const int row = 8 * w + 4 * i + g; dma16(lbase + L_U + (u32)((8 * w + 4 * i) * 256), (u32)(row * 8192 + ((idx ^ (2 * (row & 7))) * 16)), rs_wuv, (u32)(64 * j * 8192 + h * 256)); }
    const u32 vo_wd = (u32)((l >> 3) * 1024 + (((l & 7) ^ (2 * ((l >> 4) & 3))) * 16));
    dma16(lbase + L_WDN + (u32)(w * 1024), vo_wd, rs_wd, (u32)((uc * 64 + 8 * w) * 1024 + j * 128));
  }
  {
    u32x4 pf4[4], bk[4][4];
    float sl[8];
#pragma unroll
    for (int ks = 0; ks < 4; ++ks) pf4[ks] = lds128(L, a_pr + ks * 1024);
#pragma unroll
    for (int ct = 0; ct < 4; ++ct) {
      bk[ct][0] = trfrag(L, trb[ct], 0, 16384); bk[ct][1] = trfrag(L, trb[ct], 32768, 49152);
      bk[ct][2] = trfrag(L, trb2[ct], 0, 16384); bk[ct][3] = trfrag(L, trb2[ct], 32768, 49152);
    }
#pragma unroll
    for (int ww = 0; ww < 8; ++ww) sl[ww] = *(LAS float*)(L + a_smr + 512 + ww * 64);
    SCHED();
    f32x4 o[4];
#pragma unroll
    for (int ct = 0; ct < 4; ++ct) o[ct] = f32x4{0.f, 0.f, 0.f, 0.f};
#pragma unroll
    for (int ks = 0; ks < 4; ++ks)
#pragma unroll
      for (int ct = 0; ct < 4; ++ct) o[ct] = mfma32(bk[ct][ks], pf4[ks], o[ct]);
    const float ls = ((sl[0] + sl[1]) + (sl[2] + sl[3])) + ((sl[4] + sl[5]) + (sl[6] + sl[7]));
    const float inv = __builtin_amdgcn_rcpf(ls);
    u32 pk[8];
#pragma unroll
    for (int ct = 0; ct < 4; ++ct) { pk[2 * ct] = pack2(o[ct][0] * inv, o[ct][1] * inv); pk[2 * ct + 1] = pack2(o[ct][2] * inv, o[ct][3] * inv); }
#pragma unroll
    for (int i = 0; i < 8; ++i) pk[i] = min((u32)__builtin_amdgcn_ds_bpermute((int)bp_src, (int)pk[i]), SENT - 1u);
    st128_sc1(u32x4{pk[0], pk[1], pk[2], pk[3]}, o_e3w, rs_ws, 0u);
    st128_sc1(u32x4{pk[4], pk[5], pk[6], pk[7]}, o_e3w, rs_ws, 64u);
    if (w == 0) {
      const float lse = mall + __builtin_amdgcn_logf(ls);
      u32x2 v; v.x = __float_as_uint(lse); v.y = epoch;
      if (g == 0) st64_sc1(v, o_stw, rs_ws);
    }
  }
  STAMP(7);
  BAR();
  if (!chain) {
    const int wr = (2 * sw + 2 * j) & 7;
#pragma unroll
    for (int i = 0; i < 32; ++i) {
      const int row = ((32 * sw + 16 * j) & 127) + i;
      const int rr = row & 127;
      dma16_wo(lbase + L_R + (u32)(rr * 1024), dvo[(i) & 7], rs_wo, (u32)((h * 128 + rr) * 1024));
    }
    (void)wr;
  }
  if (chain) {
    const u32 o_efr = EF_OFF + (u32)(uc * 32768 + (tid >> 7) * 16384 + (tid & 127) * 16);
    u32x4 e4[8];
#pragma unroll
    for (int k = 0; k < 8; ++k) e4[k] = u32x4{0u, 0u, 0u, 0u};
    for (u32 spin = 0; ffn_ok; ++spin) {
#pragma unroll
      for (int k = 0; k < 8; ++k) ld128_sc1(e4[k], o_efr, rs_ws, (u32)(k * 2048));
      VMWAIT(0);
#pragma unroll
      for (int k = 0; k < 8; ++k) PIN(e4[k]);
      if (__all(tags_ok(e4, 8, epoch))) break;
      if (spin > SPIN_LIMIT) { fail = true; break; }
    }
    float s0 = 0.f, s1 = 0.f;
#pragma unroll
    for (int k = 0; k < 8; ++k) { s0 += __uint_as_float(e4[k].x); s1 += __uint_as_float(e4[k].z); }
    *(LAS u32x2*)(L + L_HS + tid * 8) = u32x2{__float_as_uint(s0), __float_as_uint(s1)};
  }
  const int lb = w >> 1, lp4 = 4 * (w & 1) + (l & 3), lss = l >> 2;
  u32 o_e3r = E3_OFF + (u32)(((h * 8 + j) * 4 + lb) * 32 * 128 + lss * 128 + lp4 * 16);
  u32 o_str = ST_OFF + (u32)(((h * 4 + lb) * 32 + (l & 31)) * 8);
  u32 a_wuvr = L_U + (u32)((4 * g + q_) * 256) + (u32)(((2 * w + (p_ >> 1)) ^ (8 * (g & 1) + 2 * q_)) * 16) + (u32)(8 * (p_ & 1));
  u32 a_latr = L_LAT + (u32)((g * 4 + (idx & 3)) * 16);
  u32 a_ctxw = L_CTX + (u32)((((w >> 1) * 4 + g) * 4 + (idx & 3)) * 16 + 8 * (w & 1));
  u32 a_ctxr = L_CTX + (u32)((g * 4 + (idx & 3)) * 16);
  u32 o_e5s = E5S_OFF + (u32)(((16 * w + 4 * (idx >> 2) + g) * 256 + bid) * 64 + (idx & 3) * 16);
  u32 a_latw;
  {
    const int e = ((l >> 5) & 1) * 4 + ((l >> 4) & 1) * 2 + ((l >> 3) & 1), k = e >> 1;
    const int c = 16 * (2 * (lp4 >> 2) + (k >> 1)) + 4 * (lp4 & 3) + 2 * (k & 1) + (e & 1);
    a_latw = L_LAT + (u32)(((((c >> 5) * 4 + ((c >> 2) & 3)) * 4 + lb) * 8 + 4 * ((c >> 4) & 1) + (c & 3)) * 2);
  }
  PIN(o_e3r); PIN(o_str); PIN(a_wuvr); PIN(a_latr); PIN(a_ctxw); PIN(a_ctxr); PIN(o_e5s); PIN(a_latw);
  STAMP(8);
  {
    u32x4 e4[2]; u32x2 st;
    for (u32 spin = 0;; ++spin) {
      ld64_sc1(st, o_str, rs_ws, 0u);
      ld128_sc1(e4[0], o_e3r, rs_ws, 0u); ld128_sc1(e4[1], o_e3r, rs_ws, 2048u);
      VMWAIT(0);
      PIN(e4[0]); PIN(e4[1]); PIN(st);
      const u32 mx = max(max(max(e4[0].x, e4[0].y), max(e4[0].z, e4[0].w)), max(max(e4[1].x, e4[1].y), max(e4[1].z, e4[1].w)));
      if (__all(mx != SENT && st.y == epoch)) break;
      if (spin > SPIN_LIMIT) { fail = true; break; }
    }
    STAMP(9);
    const float lse = __uint_as_float(st.x);
    const float M = half_max32(lse);
    const float e = __builtin_amdgcn_exp2f(lse - M);
    const float S = half_sum32(e);
    const float wgt = e * __builtin_amdgcn_rcpf(S);
    const float w0 = __builtin_bit_cast(float, __builtin_amdgcn_ds_bpermute(lss * 4, __builtin_bit_cast(int, wgt)));
    const float w1 = __builtin_bit_cast(float, __builtin_amdgcn_ds_bpermute((16 + lss) * 4, __builtin_bit_cast(int, wgt)));
    float v[8];
    v[0] = fmaf(w1, bf2f_lo(e4[1].x), w0 * bf2f_lo(e4[0].x)); v[1] = fmaf(w1, bf2f_hi(e4[1].x), w0 * bf2f_hi(e4[0].x));
    v[2] = fmaf(w1, bf2f_lo(e4[1].y), w0 * bf2f_lo(e4[0].y)); v[3] = fmaf(w1, bf2f_hi(e4[1].y), w0 * bf2f_hi(e4[0].y));
    v[4] = fmaf(w1, bf2f_lo(e4[1].z), w0 * bf2f_lo(e4[0].z)); v[5] = fmaf(w1, bf2f_hi(e4[1].z), w0 * bf2f_hi(e4[0].z));
    v[6] = fmaf(w1, bf2f_lo(e4[1].w), w0 * bf2f_lo(e4[0].w)); v[7] = fmaf(w1, bf2f_hi(e4[1].w), w0 * bf2f_hi(e4[0].w));
#pragma unroll
    for (int i = 0; i < 4; ++i) { auto s = __builtin_amdgcn_permlane32_swap(__float_as_uint(v[i]), __float_as_uint(v[i + 4]), false, false); v[i] = __uint_as_float(s[0]) + __uint_as_float(s[1]); }
#pragma unroll
    for (int i = 0; i < 2; ++i) { auto s = __builtin_amdgcn_permlane16_swap(__float_as_uint(v[i]), __float_as_uint(v[i + 2]), false, false); v[i] = __uint_as_float(s[0]) + __uint_as_float(s[1]); }
    { const bool b3 = (l & 8) != 0; const float snd = b3 ? v[0] : v[1], keep = b3 ? v[1] : v[0]; v[0] = keep + dpp<XOR8>(snd); }
    {
      int t1 = __builtin_amdgcn_update_dpp(0, __builtin_bit_cast(int, v[0]), 0x114, 0xf, 0xa, false);
      t1 = __builtin_amdgcn_update_dpp(t1, __builtin_bit_cast(int, v[0]), 0x104, 0xf, 0x5, false);
      v[0] += __builtin_bit_cast(float, t1);
    }
    *(LAS u16*)(L + a_latw) = (u16)pack2(v[0], v[0]);
  }
  BAR();
  if (w < 2) {
    const u32x2 g0 = *(LAS u32x2*)(L + L_HS + tid * 8), u0 = *(LAS u32x2*)(L + L_HS + 1024 + tid * 8);
    const float ga = rbf(__uint_as_float(g0.x)), gb = rbf(__uint_as_float(g0.y));
    const float ua = rbf(__uint_as_float(u0.x)), ub = rbf(__uint_as_float(u0.y));
    const float sa = rbf(ga * __builtin_amdgcn_rcpf(1.0f + __expf(-ga))), sb = rbf(gb * __builtin_amdgcn_rcpf(1.0f + __expf(-gb)));
    const int f = 2 * (tid & 31), b = tid >> 5;
    *(LAS u32*)(L + L_HF + (u32)(((((f >> 5) * 4 + ((f >> 2) & 3)) * 4 + b) * 8 + 4 * ((f >> 4) & 1) + (f & 3)) * 2)) = pack2(sa * ua, sb * ub);
  }
  u32x4 wk3[4][4];
  {
    const u32x4 wa0 = trfrag(L, a_wuvr, 0, 4096), wa1 = trfrag(L, a_wuvr, 8192, 12288);
    const u32x4 lb0 = lds128(L, a_latr), lb1 = lds128(L, a_latr + 256);
#pragma unroll
    for (int mt = 0; mt < 4; ++mt) {
      wk3[mt][0] = trfrag(L, trb[mt], 0, 16384); wk3[mt][1] = trfrag(L, trb[mt], 32768, 49152);
      wk3[mt][2] = trfrag(L, trb2[mt], 0, 16384); wk3[mt][3] = trfrag(L, trb2[mt], 32768, 49152);
    }
    f32x4 c = mfma32(wa0, lb0, f32x4{0.f, 0.f, 0.f, 0.f});
    c = mfma32(wa1, lb1, c);
    if (idx < 4) { u32x2 v; v.x = pack2(c[0], c[1]); v.y = pack2(c[2], c[3]); *(LAS u32x2*)(L + a_ctxw) = v; }
  }
  BAR();
  {
    u32x4 cf[4];
#pragma unroll
    for (int ks = 0; ks < 4; ++ks) cf[ks] = lds128(L, a_ctxr + ks * 256);
    f32x4 o[4];
#pragma unroll
    for (int mt = 0; mt < 4; ++mt) o[mt] = f32x4{0.f, 0.f, 0.f, 0.f};
#pragma unroll
    for (int ks = 0; ks < 4; ++ks)
#pragma unroll
      for (int mt = 0; mt < 4; ++mt) o[mt] = mfma32(wk3[mt][ks], cf[ks], o[mt]);
    const int rep = idx >> 2;
    f32x4 v = rep == 0 ? o[0] : rep == 1 ? o[1] : rep == 2 ? o[2] : o[3];
    u32x4 s0;
    s0.x = min(__float_as_uint(v[0]), SENT - 1u); s0.y = min(__float_as_uint(v[1]), SENT - 1u); s0.z = min(__float_as_uint(v[2]), SENT - 1u); s0.w = min(__float_as_uint(v[3]), SENT - 1u);
    st128_sc1(s0, o_e5s, rs_ws, 0u);
  }
  if (w < 4) {
    const int row = 4 * g + q_;
    const u32 a0 = L_WDN + (u32)(row * 128) + (u32)(((2 * w + (p_ >> 1)) ^ (2 * ((row >> 1) & 3))) * 16) + (u32)(8 * (p_ & 1));
    const u32x4 wa0 = trfrag(L, a0, 0, 2048), wa1 = trfrag(L, a0, 4096, 6144);
    const u32x4 hb0 = lds128(L, L_HF + (u32)((g * 4 + (idx & 3)) * 16)), hb1 = lds128(L, L_HF + 256 + (u32)((g * 4 + (idx & 3)) * 16));
    f32x4 y = mfma32(wa0, hb0, f32x4{0.f, 0.f, 0.f, 0.f});
    y = mfma32(wa1, hb1, y);
    const u32 off = E5Y_OFF + (u32)(((16 * j + 4 * w + g) * 32 + uc) * 128 + (idx & 3) * 32);
    if (ffn_ok && idx < 4) {
      st128_sc1(u32x4{__float_as_uint(y[0]), epoch, __float_as_uint(y[1]), epoch}, off, rs_ws, 0u);
      st128_sc1(u32x4{__float_as_uint(y[2]), epoch, __float_as_uint(y[3]), epoch}, off, rs_ws, 16u);
    }
  }
  STAMP(10);
  if (bid < 128) {
    u32x4 a4[2], y4;
    const u32 xfin = ((const u32*)x)[(l & 3) * 256 + bid * 2 + ((l >> 3) & 1)];
    const u32 o_r = E5S_OFF + (u32)(bid * 16384 + tid * 16);
    const u32 o_y = E5Y_OFF + (u32)(bid * 4096 + (tid & 255) * 16);
    const bool yv = tid < 224;
    for (u32 spin = 0;; ++spin) {
      ld128_sc1(y4, o_y, rs_ws, 0u);
      ld128_sc1(a4[0], o_r, rs_ws, 0u); ld128_sc1(a4[1], o_r, rs_ws, 8192u);
      VMWAIT(0);
      PIN(a4[0]); PIN(a4[1]); PIN(y4);
      const u32 mx = max(max(max(a4[0].x, a4[0].y), max(a4[0].z, a4[0].w)), max(max(a4[1].x, a4[1].y), max(a4[1].z, a4[1].w)));
      if (__all(mx != SENT && (!yv || (y4.y == epoch && y4.w == epoch)))) break;
      if (spin > SPIN_LIMIT) { fail = true; break; }
    }
    STAMP(11);
    float v[4];
    v[0] = __uint_as_float(a4[0].x) + __uint_as_float(a4[1].x); v[1] = __uint_as_float(a4[0].y) + __uint_as_float(a4[1].y);
    v[2] = __uint_as_float(a4[0].z) + __uint_as_float(a4[1].z); v[3] = __uint_as_float(a4[0].w) + __uint_as_float(a4[1].w);
#pragma unroll
    for (int i = 0; i < 2; ++i) { auto s = __builtin_amdgcn_permlane32_swap(__float_as_uint(v[i]), __float_as_uint(v[i + 2]), false, false); v[i] = __uint_as_float(s[0]) + __uint_as_float(s[1]); }
    { auto s = __builtin_amdgcn_permlane16_swap(__float_as_uint(v[0]), __float_as_uint(v[1]), false, false); v[0] = __uint_as_float(s[0]) + __uint_as_float(s[1]); }
    v[0] += dpp<0x124>(v[0]); v[0] += dpp<0x128>(v[0]);
    float y0 = yv ? __uint_as_float(y4.x) : 0.f, y1 = yv ? __uint_as_float(y4.z) : 0.f;
    y0 += dpp<XOR8>(y0); y1 += dpp<XOR8>(y1);
    y0 = x16_sum(y0); y1 = x16_sum(y1); y0 = x32_sum(y0); y1 = x32_sum(y1);
    if ((l & 12) == 0) *(LAS float*)(L + L_FIN + (u32)((w * 16 + (l >> 4) * 4 + (l & 3)) * 4)) = v[0];
    if (l < 8 && w < 4) *(LAS u32x2*)(L + L_HS + (u32)((w * 8 + l) * 8)) = u32x2{__float_as_uint(y0), __float_as_uint(y1)};
    BAR();
    if (w == 0) {
      const int mi = (l >> 2) & 3, b = l & 3;
      float sv[8], yv4[4];
#pragma unroll
      for (int ww = 0; ww < 8; ++ww) sv[ww] = *(LAS float*)(L + L_FIN + (u32)((ww * 16 + mi * 4 + b) * 4));
#pragma unroll
      for (int ww = 0; ww < 4; ++ww) yv4[ww] = *(LAS float*)(L + L_HS + (u32)(((ww * 8 + b * 2 + (mi >> 1)) * 2 + (mi & 1)) * 4));
      const float s = ((sv[0] + sv[1]) + (sv[2] + sv[3])) + ((sv[4] + sv[5]) + (sv[6] + sv[7]));
      const float y = (yv4[0] + yv4[1]) + (yv4[2] + yv4[3]);
      const float sb = rbf(s), yb = rbf(y);
      const float gg = rbf(yb * __builtin_amdgcn_rcpf(1.0f + __expf(-yb)));
      const float xv_ = (mi & 1) ? bf2f_hi(xfin) : bf2f_lo(xfin);
      float o = xv_ + rbf(sb * gg);
      if (fail) o = __uint_as_float(0x7fc00000u);
      if (l < 16) out[b * 512 + 4 * bid + mi] = (u16)(pack2(o, 0.f));
      if (bid == 0 && l == 0) st32_sc1(epoch, EPOCH_OFF, rs_ws);
    }
  }
}

extern "C" void kernel_launch(void* const* d_in, const int* in_sizes, int n_in, void* d_out, int out_size, void* d_ws, size_t ws_size, hipStream_t stream) {
  const u16* x = (const u16*)d_in[0]; const u16* ckv = (const u16*)d_in[1]; const u16* wq = (const u16*)d_in[2]; const u16* wuk = (const u16*)d_in[3];
  const u16* wuv = (const u16*)d_in[4]; const u16* wo = (const u16*)d_in[5]; const u16* wg = (const u16*)d_in[7]; const u16* wu = (const u16*)d_in[8]; const u16* wd = (const u16*)d_in[9];
  fused_decode<<<256, 512, 0, stream>>>(x, ckv, wq, wuk, wuv, wo, wg, wu, wd, (u16*)d_out, (unsigned char*)d_ws);
}
```

```cpp
#include <hip/hip_runtime.h>
#include <stdint.h>

typedef unsigned u32;
typedef unsigned short u16;
typedef u32 u32x4 __attribute__((ext_vector_type(4)));
typedef u32 u32x2 __attribute__((ext_vector_type(2)));
typedef float f32x4 __attribute__((ext_vector_type(4)));
typedef short s16x4 __attribute__((ext_vector_type(4)));
typedef __bf16 bf16x8 __attribute__((ext_vector_type(8)));
typedef __bf16 bf16x2 __attribute__((ext_vector_type(2)));
#define LAS __attribute__((address_space(3)))
typedef __amdgpu_buffer_rsrc_t rsrc_t;

#define DIAG 0
#define NT_W 0

constexpr u32 EPOCH_OFF = 0;
constexpr u32 QA_OFF = 4096;
constexpr u32 ST_OFF = QA_OFF + 4 * 32 * 2048;
constexpr u32 CF_OFF = ST_OFF + 32 * 4 * 32 * 8;
constexpr u32 EF_OFF = CF_OFF + 4096;
constexpr u32 E5Y_OFF = EF_OFF + 32 * 32768;
constexpr u32 E3_OFF = E5Y_OFF + 128 * 4096;
constexpr u32 E5S_OFF = E3_OFF + 256 * 16384;
constexpr u32 WS_END = E5S_OFF + 128 * 16384;
constexpr u32 DBG_OFF = WS_END;
constexpr u32 WS_TOTAL = DBG_OFF + 256 * 64 * 8;
constexpr u32 SENT = 0xFFFFFFFFu;

constexpr u32 L_R = 0;
constexpr u32 L_U = 131072;
constexpr u32 L_WDN = L_U + 16384;
constexpr u32 L_M = 155648;
constexpr u32 L_XN = L_M;
constexpr u32 L_P = L_M;
constexpr u32 L_QB = L_M + 4096;
constexpr u32 L_CTX = L_QB;
constexpr u32 L_SM = L_M + 5120;
constexpr u32 L_SL = L_M + 5632;
constexpr u32 L_LAT = L_M + 6144;
constexpr u32 L_HF = L_M + 6656;
constexpr u32 L_FIN = L_M + 7168;
constexpr u32 L_HS = L_M;
constexpr u32 L_FP = L_WDN;
constexpr u32 L_CNT = L_M + 7680;
constexpr u32 L_XF = L_M + 7696;
constexpr u32 LDS_BYTES = 163840;

constexpr u32 SPIN_LIMIT = 1u << 16;
#define KD 6
constexpr int PD = 8;
#define CPRE 10
#define KS 11
#define E2_SLEEP 8

__device__ __forceinline__ float bf2f_lo(u32 v) { return __uint_as_float(v << 16); }
__device__ __forceinline__ float bf2f_hi(u32 v) { return __uint_as_float(v & 0xffff0000u); }
__device__ __forceinline__ u32 pack2(float lo, float hi) { bf16x2 v = {(__bf16)lo, (__bf16)hi}; return __builtin_bit_cast(u32, v); }
__device__ __forceinline__ float rbf(float f) { return __uint_as_float(((u32)__builtin_bit_cast(u16, (__bf16)f)) << 16); }
__device__ __forceinline__ f32x4 mfma32(u32x4 a, u32x4 b, f32x4 c) {
  return __builtin_amdgcn_mfma_f32_16x16x32_bf16(__builtin_bit_cast(bf16x8, a), __builtin_bit_cast(bf16x8, b), c, 0, 0, 0);
}
__device__ __forceinline__ f32x4 mfma16(u32x2 a, u32x2 b, f32x4 c) {
  return __builtin_amdgcn_mfma_f32_16x16x16bf16_1k(__builtin_bit_cast(s16x4, a), __builtin_bit_cast(s16x4, b), c, 0, 0, 0);
}
template <int CTRL> __device__ __forceinline__ float dpp(float x) {
  return __builtin_bit_cast(float, __builtin_amdgcn_mov_dpp(__builtin_bit_cast(int, x), CTRL, 0xf, 0xf, true));
}
constexpr int XOR1 = 0xB1, XOR2 = 0x4E, XOR7 = 0x141, XOR15 = 0x140, XOR8 = 0x128;
__device__ __forceinline__ float x16_sum(float x) { auto s = __builtin_amdgcn_permlane16_swap(__float_as_uint(x), __float_as_uint(x), false, false); return __uint_as_float(s[0]) + __uint_as_float(s[1]); }
__device__ __forceinline__ float x32_sum(float x) { auto s = __builtin_amdgcn_permlane32_swap(__float_as_uint(x), __float_as_uint(x), false, false); return __uint_as_float(s[0]) + __uint_as_float(s[1]); }
__device__ __forceinline__ float x16_max(float x) { auto s = __builtin_amdgcn_permlane16_swap(__float_as_uint(x), __float_as_uint(x), false, false); return fmaxf(__uint_as_float(s[0]), __uint_as_float(s[1])); }
__device__ __forceinline__ float x32_max(float x) { auto s = __builtin_amdgcn_permlane32_swap(__float_as_uint(x), __float_as_uint(x), false, false); return fmaxf(__uint_as_float(s[0]), __uint_as_float(s[1])); }
__device__ __forceinline__ float wave_sum(float v) {
  v += dpp<XOR1>(v); v += dpp<XOR2>(v); v += dpp<XOR7>(v); v += dpp<XOR15>(v); v = x16_sum(v); v = x32_sum(v); return v;
}
__device__ __forceinline__ float half_sum32(float v) {
  v += dpp<XOR1>(v); v += dpp<XOR2>(v); v += dpp<XOR7>(v); v += dpp<XOR15>(v); v = x16_sum(v); return v;
}
__device__ __forceinline__ float half_max32(float v) {
  v = fmaxf(v, dpp<XOR1>(v)); v = fmaxf(v, dpp<XOR2>(v)); v = fmaxf(v, dpp<XOR7>(v)); v = fmaxf(v, dpp<XOR15>(v)); v = x16_max(v); return v;
}

#define BAR() asm volatile("s_waitcnt lgkmcnt(0)\n\ts_barrier" ::: "memory")
#define RAWBAR() asm volatile("s_barrier" ::: "memory")
#define VMWAIT(n) asm volatile("s_waitcnt vmcnt(" #n ")" ::: "memory")
#define SCHED() __builtin_amdgcn_sched_barrier(0)
template <int N> __device__ __forceinline__ void vmwait() { asm volatile("s_waitcnt vmcnt(%0)" :: "i"(N) : "memory"); }
#define PIN(a) asm volatile("" : "+v"(a))

#define NTS ""
__device__ __forceinline__ void ld128(u32x4& d, u32 voff, rsrc_t rs, u32 soff) {
  asm volatile("buffer_load_dwordx4 %0, %1, %2, %3 offen" : "=v"(d) : "v"(voff), "s"(rs), "s"(soff) : "memory");
}
__device__ __forceinline__ void ld128_w(u32x4& d, u32 voff, rsrc_t rs, u32 soff) {
  asm volatile("buffer_load_dwordx4 %0, %1, %2, %3 offen" NTS : "=v"(d) : "v"(voff), "s"(rs), "s"(soff) : "memory");
}
__device__ __forceinline__ void ld128_sc1(u32x4& d, u32 voff, rsrc_t rs, u32 soff) {
  asm volatile("buffer_load_dwordx4 %0, %1, %2, %3 offen sc1" : "=v"(d) : "v"(voff), "s"(rs), "s"(soff) : "memory");
}
__device__ __forceinline__ void ld64_sc1(u32x2& d, u32 voff, rsrc_t rs, u32 soff) {
  asm volatile("buffer_load_dwordx2 %0, %1, %2, %3 offen sc1" : "=v"(d) : "v"(voff), "s"(rs), "s"(soff) : "memory");
}
__device__ __forceinline__ void ld32_sc1(u32& d, u32 voff, rsrc_t rs, u32 soff) {
  asm volatile("buffer_load_dword %0, %1, %2, %3 offen sc1" : "=v"(d) : "v"(voff), "s"(rs), "s"(soff) : "memory");
}
__device__ __forceinline__ void ld32(u32& d, u32 voff, rsrc_t rs, u32 soff) {
  asm volatile("buffer_load_dword %0, %1, %2, %3 offen" : "=v"(d) : "v"(voff), "s"(rs), "s"(soff) : "memory");
}
__device__ __forceinline__ void st128_sc1(u32x4 d, u32 voff, rsrc_t rs, u32 soff) {
  asm volatile("buffer_store_dwordx4 %0, %1, %2, %3 offen sc1\n\ts_nop 1" :: "v"(d), "v"(voff), "s"(rs), "s"(soff) : "memory");
}
__device__ __forceinline__ void st64_sc1(u32x2 d, u32 voff, rsrc_t rs) {
  asm volatile("buffer_store_dwordx2 %0, %1, %2, 0 offen sc1\n\ts_nop 1" :: "v"(d), "v"(voff), "s"(rs) : "memory");
}
__device__ __forceinline__ void st32_sc1(u32 d, u32 voff, rsrc_t rs) {
  asm volatile("buffer_store_dword %0, %1, %2, 0 offen sc1\n\ts_nop 1" :: "v"(d), "v"(voff), "s"(rs) : "memory");
}
__device__ __forceinline__ void dma16(u32 lds_dst, u32 voff, rsrc_t rs, u32 soff) {
  asm volatile("s_mov_b32 m0, %0\n\ts_nop 0\n\tbuffer_load_dwordx4 %1, %2, %3 offen" NTS " lds" :: "s"(lds_dst), "v"(voff), "s"(rs), "s"(soff) : "memory");
}
__device__ __forceinline__ void dma16_def(u32 lds_dst, u32 voff, rsrc_t rs, u32 soff) {
  asm volatile("s_mov_b32 m0, %0\n\ts_nop 0\n\tbuffer_load_dwordx4 %1, %2, %3 offen lds" :: "s"(lds_dst), "v"(voff), "s"(rs), "s"(soff) : "memory");
}
#define KV_NT 1
#define KV_STAG 0
#define WO_NT 1
#define WQ_NT 1
#define dma16_kv dma16
#define dma16_wo dma16
#define ld128_wq ld128_w
__device__ __forceinline__ rsrc_t mk(const void* p, u32 bytes) { return __builtin_amdgcn_make_buffer_rsrc((void*)p, 0, bytes, 0x00020000); }
__device__ __forceinline__ u32x4 lds128(LAS unsigned char* L, u32 a) { return *(LAS u32x4*)(L + a); }
__device__ __forceinline__ u32x4 trfrag(LAS unsigned char* L, u32 a, u32 o0, u32 o1) {
  const s16x4 t0 = __builtin_amdgcn_ds_read_tr16_b64_v4i16((LAS s16x4*)(L + a + o0));
  const s16x4 t1 = __builtin_amdgcn_ds_read_tr16_b64_v4i16((LAS s16x4*)(L + a + o1));
  const u32x2 x = __builtin_bit_cast(u32x2, t0), y = __builtin_bit_cast(u32x2, t1);
  return u32x4{x.x, x.y, y.x, y.y};
}
__device__ __forceinline__ bool tags_ok(const u32x4* v, int n, u32 epoch) {
  u32 a = v[0].y & v[0].w, o = v[0].y | v[0].w;
#pragma unroll
  for (int k = 1; k < n; ++k) { a &= v[k].y & v[k].w; o |= v[k].y | v[k].w; }
  return a == epoch && o == epoch;
}

#define STAMP(i) do {} while (0)

__global__ void __launch_bounds__(512, 2)
fused_decode(const u16* __restrict__ x, const u16* __restrict__ ckv, const u16* __restrict__ wq, const u16* __restrict__ wuk,
             const u16* __restrict__ wuv, const u16* __restrict__ wo, const u16* __restrict__ wg, const u16* __restrict__ wu,
             const u16* __restrict__ wd, u16* __restrict__ out, unsigned char* __restrict__ ws) {
  __shared__ __attribute__((aligned(16))) unsigned char lds[LDS_BYTES];
  LAS unsigned char* L = (LAS unsigned char*)lds;
  const u32 lbase = (u32)(size_t)L;
  const int tid = threadIdx.x;
  const int w = __builtin_amdgcn_readfirstlane(tid >> 6);
  const int l = tid & 63, g = l >> 4, idx = l & 15;
  const int bid = blockIdx.x, xc = bid & 7, r = bid >> 3;
  const int h = xc + 8 * (r >> 3), j = r & 7;
  const int P = xc * 16 + (r & 15), ab = P >> 5, as = P & 31, ag = 1 - (r >> 4);
  const bool ffn_ok = h < 28;
  const int uc = ffn_ok ? h : 27;
  const int fmat = uc & 1, ftile = uc >> 1;
  STAMP(0);

  const rsrc_t rs_x = mk(x, 4096), rs_kv = mk(ckv, 4u * 4096 * 1024), rs_wq = mk(wq, 4u << 20), rs_wuk = mk(wuk, 4u << 20),
               rs_wuv = mk(wuv, 4u << 20), rs_wo = mk(wo, 4u << 20), rs_wg = mk(wg, 512 * 1792 * 2), rs_wu = mk(wu, 512 * 1792 * 2),
               rs_wd = mk(wd, 512 * 1792 * 2), rs_ws = mk(ws, WS_TOTAL);

  const bool chain = w < 4;
  const int sw = w & 3;
  const int q_ = idx >> 2, p_ = idx & 3, jp = idx & 7;
  const int blk2 = (j + 4 + sw) & 7;
  bool fail = false;
  u32 epoch;
  u32 dvo[8];
  u32 a_qir[4], a_kvr[4], trb[4], trb2[4];
  u32 a_smw, a_smr, a_pw, a_pr, o_e3w, bp_src, o_stw;
#define SEL_INIT() u32 sel_lo[4], sel_hi[4]; \
  _Pragma("unroll") for (int i = 0; i < 4; ++i) { const bool on = (i == (jp >> 1)); \
    sel_lo[i] = on ? ((jp & 1) ? 0x01000c0cu : 0x0c0c0100u) : 0x0c0c0c0cu; sel_hi[i] = on ? ((jp & 1) ? 0x03020c0cu : 0x0c0c0302u) : 0x0c0c0c0cu; \
    PIN(sel_lo[i]); PIN(sel_hi[i]); }
#define DIAG_A(p, sel) u32x4{__builtin_amdgcn_perm(p, p, sel[0]), __builtin_amdgcn_perm(p, p, sel[1]), __builtin_amdgcn_perm(p, p, sel[2]), __builtin_amdgcn_perm(p, p, sel[3])}
#define EPOCH_AND_XN() do { \
    asm volatile("s_load_dword %0, %1, 0x0\n\ts_waitcnt lgkmcnt(0)" : "=s"(epoch) : "s"(ws) : "memory"); epoch += 1u; \
    while (__hip_atomic_load((LAS u32*)(L + L_XF), __ATOMIC_RELAXED, __HIP_MEMORY_SCOPE_WORKGROUP) < 4u) __builtin_amdgcn_s_sleep(1); \
    asm volatile("" ::: "memory"); } while (0)
#define DSTEP(acc0, acc1, wf, p0, p1, par) do { const u32* sel_ = (par) ? sel_hi : sel_lo; \
    acc0 = mfma32(DIAG_A(p0, sel_), wf, acc0); acc1 = mfma32(DIAG_A(p1, sel_), wf, acc1); } while (0)
#define LATE_ADDR() do { \
    _Pragma("unroll") for (int c = 0; c < 4; ++c) { const u32 uu = (u32)(4 * c + g); \
      a_qir[c] = L_U + uu * 256 + (((u32)idx ^ uu) * 16); \
      a_kvr[c] = L_R + (u32)((16 * w + idx) * 1024) + ((uu ^ (u32)(2 * (idx & 7))) * 16); \
      const u32 pos = (u32)((8 * w + 2 * c + (p_ >> 1)) ^ (8 * (g & 1) + 2 * q_)); \
      trb[c] = L_R + (u32)((4 * g + q_) * 1024) + pos * 16 + (u32)(8 * (p_ & 1)); trb2[c] = trb[c] + 65536u; \
      PIN(a_qir[c]); PIN(a_kvr[c]); PIN(trb[c]); PIN(trb2[c]); } \
    _Pragma("unroll") for (int i = 0; i < 8; ++i) { dvo[i] = (u32)((l ^ (2 * i)) * 16); PIN(dvo[i]); } \
    a_smw = L_SM + (u32)((w * 16 + idx) * 4); a_smr = L_SM + (u32)(idx * 4); \
    a_pw = L_P + (u32)((((w >> 1) * 4 + g) * 16 + idx) * 16 + 8 * (w & 1)); a_pr = L_P + (u32)(l * 16); \
    o_e3w = E3_OFF + (u32)(((((16 * ag + (l >> 2)) * 8 + w) * 4 + ab) * 32 + as) * 128 + (l & 3) * 16); \
    bp_src = (u32)(((l >> 2) + 16 * (l & 3)) * 4); \
    o_stw = ST_OFF + (u32)((((16 * ag + idx) * 4 + ab) * 32 + as) * 8); \
    PIN(a_smw); PIN(a_smr); PIN(a_pw); PIN(a_pr); PIN(o_e3w); PIN(bp_src); PIN(o_stw); } while (0)
  const u32 a_qpw = L_U + (u32)(((w * 4 + (g >> 1)) * 128 + 8 * idx + 4 * (g & 1)) * 4);

  const int wb = (w + j) & 7;
  const u32 vo_wq = (u32)((64 * wb + 16 * g) * 8192 + h * 256 + idx * 16);
  const u32 a_xn = L_XN + (u32)((idx >> 3) * 1024 + (64 * wb + 16 * g) * 2);
  constexpr u32 L_WUK = L_R + 112 * 1024;
#define QLOOP(EXTRA) do { \
      const u32x4 xa0 = lds128(L, a_xn), xa1 = lds128(L, a_xn + 16), xb0 = lds128(L, a_xn + 2048), xb1 = lds128(L, a_xn + 2064); \
      f32x4 qa0 = {0.f, 0.f, 0.f, 0.f}, qa1 = {0.f, 0.f, 0.f, 0.f}; \
      QS(0, EXTRA, xa0[0], xb0[0]); QS(1, EXTRA, xa0[0], xb0[0]); QS(2, EXTRA, xa0[1], xb0[1]); QS(3, EXTRA, xa0[1], xb0[1]); \
      QS(4, EXTRA, xa0[2], xb0[2]); QS(5, EXTRA, xa0[2], xb0[2]); QS(6, EXTRA, xa0[3], xb0[3]); QS(7, EXTRA, xa0[3], xb0[3]); \
      QS(8, EXTRA, xa1[0], xb1[0]); QS(9, EXTRA, xa1[0], xb1[0]); QS(10, EXTRA, xa1[1], xb1[1]); QS(11, EXTRA, xa1[1], xb1[1]); \
      QS(12, EXTRA, xa1[2], xb1[2]); QS(13, EXTRA, xa1[2], xb1[2]); QS(14, EXTRA, xa1[3], xb1[3]); QS(15, EXTRA, xa1[3], xb1[3]); \
      *(LAS f32x4*)(L + a_qpw) = qa0; *(LAS f32x4*)(L + a_qpw + 1024) = qa1; } while (0)
#define QS(t, EXTRA, P0, P1) do { vmwait<15 - (t) + (EXTRA)>(); PIN(wqf[t]); DSTEP(qa0, qa1, wqf[t], P0, P1, (t) & 1); } while (0)
#define EPOCH() do { asm volatile("s_load_dword %0, %1, 0x0\n\ts_waitcnt lgkmcnt(0)" : "=s"(epoch) : "s"(ws) : "memory"); epoch += 1u; } while (0)

  if (chain) {
    u32x4 xv, wqf[16];
    __builtin_amdgcn_s_setprio(3);
    ld128(xv, (u32)(w * 1024 + l * 16), rs_x, 0u);
#pragma unroll
    for (int t = 0; t < CPRE; ++t) ld128_wq(wqf[t], vo_wq, rs_wq, (u32)(t * 8192));
    if (tid == 0) *(LAS u32*)(L + L_CNT) = 0u;
    SEL_INIT();
    const u32 a_qbr = L_QB + (u32)(((idx & 3) * 128 + 8 * g) * 2);
    const u32 o_qa = QA_OFF + (u32)((((idx & 3) * 32 + h) * 256 + (64 * j + 16 * sw + 4 * g) / 2) * 8);
    STAMP(1);
    {
      vmwait<CPRE>();
      PIN(xv);
      float v[8];
      v[0] = bf2f_lo(xv.x); v[1] = bf2f_hi(xv.x); v[2] = bf2f_lo(xv.y); v[3] = bf2f_hi(xv.y);
      v[4] = bf2f_lo(xv.z); v[5] = bf2f_hi(xv.z); v[6] = bf2f_lo(xv.w); v[7] = bf2f_hi(xv.w);
      float s = ((v[0] + v[1]) + (v[2] + v[3])) + ((v[4] + v[5]) + (v[6] + v[7]));
      s = wave_sum(s);
      const float mean = s * (1.f / 512.f);
      float ss = 0.f;
#pragma unroll
      for (int i = 0; i < 8; ++i) { const float d = v[i] - mean; ss = fmaf(d, d, ss); }
      ss = wave_sum(ss);
      const float var = rbf(ss * (1.f / 512.f));
      const float mb = rbf(mean);
      const float den = rbf(__builtin_amdgcn_sqrtf(rbf(var + 1e-5f)));
      const float rd = __builtin_amdgcn_rcpf(den);
      u32x4 o;
      o.x = pack2(rbf(v[0] - mb) * rd, rbf(v[1] - mb) * rd);
      o.y = pack2(rbf(v[2] - mb) * rd, rbf(v[3] - mb) * rd);
      o.z = pack2(rbf(v[4] - mb) * rd, rbf(v[5] - mb) * rd);
      o.w = pack2(rbf(v[6] - mb) * rd, rbf(v[7] - mb) * rd);
      *(LAS u32x4*)(L + L_XN + w * 1024 + l * 16) = o;
    }
#pragma unroll
    for (int t = CPRE; t < 16; ++t) ld128_wq(wqf[t], vo_wq, rs_wq, (u32)(t * 8192));
    EPOCH();
    BAR();
    STAMP(2);
    QLOOP(0);
    STAMP(3);
    BAR();
    u32x4 wuk[4];
    {
      u32x2 pv[8];
#pragma unroll
      for (int ww = 0; ww < 8; ++ww) pv[ww] = *(LAS u32x2*)(L + L_U + tid * 8 + ww * 2048);
      float s0, s1;
      s0 = ((__uint_as_float(pv[0].x) + __uint_as_float(pv[1].x)) + (__uint_as_float(pv[2].x) + __uint_as_float(pv[3].x))) + ((__uint_as_float(pv[4].x) + __uint_as_float(pv[5].x)) + (__uint_as_float(pv[6].x) + __uint_as_float(pv[7].x)));
      s1 = ((__uint_as_float(pv[0].y) + __uint_as_float(pv[1].y)) + (__uint_as_float(pv[2].y) + __uint_as_float(pv[3].y))) + ((__uint_as_float(pv[4].y) + __uint_as_float(pv[5].y)) + (__uint_as_float(pv[6].y) + __uint_as_float(pv[7].y)));
      *(LAS u32*)(L + L_QB + tid * 4) = pack2(s0, s1);
    }
    BAR();
    {
      u32x4 bq[4];
#pragma unroll
      for (int kk = 0; kk < 4; ++kk) bq[kk] = lds128(L, a_qbr + kk * 64);
#pragma unroll
      for (int kk = 0; kk < 4; ++kk) wuk[kk] = lds128(L, L_WUK + (u32)((16 * sw + idx) * 256) + (u32)(((4 * kk + g) ^ (2 * (idx & 7))) * 16));
      f32x4 acc = {0.f, 0.f, 0.f, 0.f}, acc2 = {0.f, 0.f, 0.f, 0.f};
      acc = mfma32(wuk[0], bq[0], acc); acc2 = mfma32(wuk[1], bq[1], acc2);
      acc = mfma32(wuk[2], bq[2], acc); acc2 = mfma32(wuk[3], bq[3], acc2);
      constexpr float SC = 0.08838834764831845f * 1.4426950408889634f;
      u32x4 o; o.x = pack2((acc[0] + acc2[0]) * SC, (acc[1] + acc2[1]) * SC); o.y = epoch; o.z = pack2((acc[2] + acc2[2]) * SC, (acc[3] + acc2[3]) * SC); o.w = epoch;
      if (idx < 4) st128_sc1(o, o_qa, rs_ws, 0u);
    }
    STAMP(4);
    BAR();
    {
      const int t = tid;
      const u32 o_efw = EF_OFF + (u32)((2 * ftile + ((t & 63) >> 5)) * 32768 + ((fmat * 8 + j) * 4 + (t >> 6)) * 512 + (t & 31) * 16);
      u32x2 pv[4];
#pragma unroll
      for (int ww = 0; ww < 4; ++ww) pv[ww] = *(LAS u32x2*)(L + L_FP + t * 8 + ww * 2048);
      const float s0 = (__uint_as_float(pv[0].x) + __uint_as_float(pv[1].x)) + (__uint_as_float(pv[2].x) + __uint_as_float(pv[3].x));
      const float s1 = (__uint_as_float(pv[0].y) + __uint_as_float(pv[1].y)) + (__uint_as_float(pv[2].y) + __uint_as_float(pv[3].y));
      if (ffn_ok) st128_sc1(u32x4{__float_as_uint(s0), epoch, __float_as_uint(s1), epoch}, o_efw, rs_ws, 0u);
    }
    LATE_ADDR();
    const u32 o_e2 = QA_OFF + (u32)((ab * 32 + 16 * ag) * 2048 + w * 8192 + l * 16);
    u32 a_qiw[4];
#pragma unroll
    for (int k2 = 0; k2 < 4; ++k2) { const u32 uu = (u32)(l >> 1); a_qiw[k2] = L_U + uu * 256 + (((u32)(4 * w + k2) ^ (uu & 15)) * 16) + (u32)(8 * (l & 1)); PIN(a_qiw[k2]); }
    __builtin_amdgcn_s_sleep(E2_SLEEP);
    STAMP(13);
    u32x4 q4[8];
    for (u32 spin = 0;; ++spin) {
#pragma unroll
      for (int k = 0; k < 8; ++k) ld128_sc1(q4[k], o_e2, rs_ws, (u32)(k * 1024));
      VMWAIT(0);
#pragma unroll
      for (int k = 0; k < 8; ++k) PIN(q4[k]);
      if (__all(tags_ok(q4, 8, epoch))) break;
      if (spin > SPIN_LIMIT) { fail = true; break; }
    }
    STAMP(5);
#pragma unroll
    for (int k = 0; k < 8; ++k) {
      *(LAS u32*)(L + a_qiw[k >> 1] + (k & 1) * 8192) = q4[k].x;
      *(LAS u32*)(L + a_qiw[k >> 1] + (k & 1) * 8192 + 4) = q4[k].z;
    }
  } else {
    u32x4 wqf[16], f1w[4];
    if (w == 7) {
      const u32x4 zz = {0u, 0u, 0u, 1u};
      st128_sc1(zz, QA_OFF + (u32)((((l >> 4) * 32 + h) * 256 + 32 * j) * 8 + (l & 15) * 16), rs_ws, 0u);
      if (j == 0) st128_sc1(zz, ST_OFF + (u32)(h * 1024 + l * 16), rs_ws, 0u);
      if (j == 0 && l == 0) st128_sc1(zz, CF_OFF + (u32)((h >> 4) * 2048 + (h & 15) * 128 + 64), rs_ws, 0u);
      const u32 oe = EF_OFF + (u32)(2 * ftile * 32768 + (fmat * 8 + j) * 2048 + l * 16);
      if (ffn_ok) { st128_sc1(zz, oe, rs_ws, 0u); st128_sc1(zz, oe, rs_ws, 1024u); st128_sc1(zz, oe, rs_ws, 32768u); st128_sc1(zz, oe, rs_ws, 32768u + 1024u);
        const u32 oy = E5Y_OFF + (u32)(((16 * j + (l >> 3)) * 32 + uc) * 128 + (l & 7) * 16);
        st128_sc1(zz, oy, rs_ws, 0u); st128_sc1(zz, oy, rs_ws, 8u * 32u * 128u); }
    }
#pragma unroll
    for (int t = 0; t < 16; ++t) ld128_wq(wqf[t], vo_wq, rs_wq, (u32)(t * 8192));
    const u32 kvrow0 = (u32)((ab * 4096 + as * 128 + 32 * sw) * 1024);
#pragma unroll
    for (int i = 0; i < 8; ++i) { dvo[i] = (u32)((l ^ (2 * i)) * 16); PIN(dvo[i]); }
#define KV(i) dma16_kv(lbase + L_R + (u32)((32 * sw + (i)) * 1024), dvo[(i) & 7], rs_kv, kvrow0 + (u32)((i) * 1024))
    SEL_INIT();
    const u32 a_fx = L_XN + (u32)((idx >> 3) * 1024 + (64 * j + 16 * sw + 4 * g) * 2);
    const u32 vo3 = (u32)((64 * j + 16 * sw + 4 * g) * 3584 + ftile * 256 + idx * 16);
    const rsrc_t rs_f1 = fmat ? rs_wu : rs_wg;
    EPOCH();
    BAR();
#pragma unroll
    for (int i = 0; i < 4; ++i) { const int row = 16 * sw + 4 * i + g;
      dma16(lbase + L_WUK + (u32)((16 * sw + 4 * i) * 256), (u32)(row * 8192 + ((idx ^ (2 * (row & 7))) * 16)), rs_wuk, (u32)(64 * j * 8192 + h * 256)); }
#pragma unroll
    for (int t = 0; t < 4; ++t) ld128_w(f1w[t], vo3, rs_f1, (u32)(t * 3584));
    KV(0); KV(1); KV(2); KV(3);
    STAMP(2);
    const u32x2 fx0 = *(LAS u32x2*)(L + a_fx), fx1 = *(LAS u32x2*)(L + a_fx + 2048);
    QLOOP(12);
    STAMP(3);
    BAR();
    vmwait<8>();
    BAR();
    {
      vmwait<4>();
      PIN(f1w[0]); PIN(f1w[1]); PIN(f1w[2]); PIN(f1w[3]);
      f32x4 f0a = {0.f, 0.f, 0.f, 0.f}, f1a = {0.f, 0.f, 0.f, 0.f};
      DSTEP(f0a, f1a, f1w[0], fx0.x, fx1.x, 0); DSTEP(f0a, f1a, f1w[1], fx0.x, fx1.x, 1);
      DSTEP(f0a, f1a, f1w[2], fx0.y, fx1.y, 0); DSTEP(f0a, f1a, f1w[3], fx0.y, fx1.y, 1);
      const u32 a_fpw = L_FP + (u32)(((sw * 4 + (g >> 1)) * 128 + 8 * idx + 4 * (g & 1)) * 4);
      *(LAS f32x4*)(L + a_fpw) = f0a;
      *(LAS f32x4*)(L + a_fpw + 1024) = f1a;
    }
    BAR();
    const u32x4 sv = {SENT, SENT, SENT, SENT};
    const u32 oc = E3_OFF + (u32)((h * 8 + j) * 16384 + (tid - 256) * 16);
    const u32 oc2 = E5S_OFF + (u32)((bid >> 1) * 16384 + (bid & 1) * 8192 + (tid - 256) * 16);
    KV(4); st128_sc1(sv, oc, rs_ws, 0u);
    KV(5); st128_sc1(sv, oc, rs_ws, 4096u);
    KV(6); st128_sc1(sv, oc, rs_ws, 8192u);
    KV(7); st128_sc1(sv, oc, rs_ws, 12288u);
    KV(8); st128_sc1(sv, oc2, rs_ws, 0u);
    KV(9); st128_sc1(sv, oc2, rs_ws, 4096u);
    KV(10); KV(11);
    LATE_ADDR();
    vmwait<KS>(); KV(12); vmwait<KS>(); KV(13);
    vmwait<KS>(); KV(14); vmwait<KS>(); KV(15);
    vmwait<6>();
    {
      u32 old = 0;
      if (l == 0) old = __hip_atomic_fetch_add((LAS u32*)(L + L_CNT), 1u, __ATOMIC_RELAXED, __HIP_MEMORY_SCOPE_WORKGROUP);
      old = (u32)__builtin_amdgcn_readfirstlane(old);
      if (old == 3u && l == 0) st64_sc1(u32x2{epoch, epoch}, CF_OFF + (u32)((h >> 4) * 2048 + (h & 15) * 128 + j * 8), rs_ws);
    }
    KV(16); KV(17); KV(18); KV(19); KV(20);
    vmwait<KS>(); KV(21); vmwait<KS>(); KV(22); vmwait<KS>(); KV(23); vmwait<KS>(); KV(24);
    vmwait<KS>(); KV(25); vmwait<KS>(); KV(26); vmwait<KS>(); KV(27); vmwait<KS>(); KV(28);
    vmwait<KS>(); KV(29); vmwait<KS>(); KV(30); vmwait<KS>(); KV(31);
    VMWAIT(0);
  }
  BAR();
  u32x4 bq[16];
#pragma unroll
  for (int kk = 0; kk < 16; ++kk) bq[kk] = lds128(L, a_qir[kk & 3] + (kk >> 2) * 4096);
  u32x4 cf4;
  ld128_sc1(cf4, CF_OFF + (u32)(ag * 2048 + (l >> 2) * 128 + (l & 3) * 16), rs_ws, 0u);
  f32x4 sc;
  {
    u32x4 ak[16];
#pragma unroll
    for (int kk = 0; kk < 16; ++kk) ak[kk] = lds128(L, a_kvr[kk & 3] + (kk >> 2) * 256);
    SCHED();
    f32x4 s0 = {0.f, 0.f, 0.f, 0.f}, s1 = {0.f, 0.f, 0.f, 0.f};
#pragma unroll
    for (int kk = 0; kk < 16; kk += 2) { s0 = mfma32(ak[kk], bq[kk], s0); s1 = mfma32(ak[kk + 1], bq[kk + 1], s1); }
    sc = s0 + s1;
    float m = fmaxf(fmaxf(sc[0], sc[1]), fmaxf(sc[2], sc[3]));
    m = x16_max(m); m = x32_max(m);
    if (g == 0) *(LAS float*)(L + a_smw) = m;
  }
  STAMP(6);
  BAR();
  float mall;
  {
    float mv[8];
#pragma unroll
    for (int ww = 0; ww < 8; ++ww) mv[ww] = *(LAS float*)(L + a_smr + ww * 64);
    const float m = fmaxf(fmaxf(fmaxf(mv[0], mv[1]), fmaxf(mv[2], mv[3])), fmaxf(fmaxf(mv[4], mv[5]), fmaxf(mv[6], mv[7])));
    mall = m;
    const float p0 = __builtin_amdgcn_exp2f(sc[0] - m), p1 = __builtin_amdgcn_exp2f(sc[1] - m), p2 = __builtin_amdgcn_exp2f(sc[2] - m), p3 = __builtin_amdgcn_exp2f(sc[3] - m);
    u32x2 pv; pv.x = pack2(p0, p1); pv.y = pack2(p2, p3);
    *(LAS u32x2*)(L + a_pw) = pv;
    float s = (p0 + p1) + (p2 + p3);
    s = x16_sum(s); s = x32_sum(s);
    if (g == 0) *(LAS float*)(L + a_smw + 512) = s;
  }
  for (u32 spin = 0;; ++spin) {
    VMWAIT(0);
    PIN(cf4);
    if (__all(cf4.x == epoch && cf4.y == epoch && cf4.z == epoch && cf4.w == epoch)) break;
    if (spin > SPIN_LIMIT) { fail = true; break; }
    ld128_sc1(cf4, CF_OFF + (u32)(ag * 2048 + (l >> 2) * 128 + (l & 3) * 16), rs_ws, 0u);
  }
  BAR();
  {
#pragma unroll
    for (int i = 0; i < 2; ++i) { const int row = 8 * w + 4 * i + g; dma16(lbase + L_U + (u32)((8 * w + 4 * i) * 256), (u32)(row * 8192 + ((idx ^ (2 * (row & 7))) * 16)), rs_wuv, (u32)(64 * j * 8192 + h * 256)); }
    const u32 vo_wd = (u32)((l >> 3) * 1024 + (((l & 7) ^ (2 * ((l >> 4) & 3))) * 16));
    dma16(lbase + L_WDN + (u32)(w * 1024), vo_wd, rs_wd, (u32)((uc * 64 + 8 * w) * 1024 + j * 128));
  }
  {
    u32x4 pf4[4], bk[4][4];
    float sl[8];
#pragma unroll
    for (int ks = 0; ks < 4; ++ks) pf4[ks] = lds128(L, a_pr + ks * 1024);
#pragma unroll
    for (int ct = 0; ct < 4; ++ct) {
      bk[ct][0] = trfrag(L, trb[ct], 0, 16384); bk[ct][1] = trfrag(L, trb[ct], 32768, 49152);
      bk[ct][2] = trfrag(L, trb2[ct], 0, 16384); bk[ct][3] = trfrag(L, trb2[ct], 32768, 49152);
    }
#pragma unroll
    for (int ww = 0; ww < 8; ++ww) sl[ww] = *(LAS float*)(L + a_smr + 512 + ww * 64);
    SCHED();
    f32x4 o[4];
#pragma unroll
    for (int ct = 0; ct < 4; ++ct) o[ct] = f32x4{0.f, 0.f, 0.f, 0.f};
#pragma unroll
    for (int ks = 0; ks < 4; ++ks)
#pragma unroll
      for (int ct = 0; ct < 4; ++ct) o[ct] = mfma32(bk[ct][ks], pf4[ks], o[ct]);
    const float ls = ((sl[0] + sl[1]) + (sl[2] + sl[3])) + ((sl[4] + sl[5]) + (sl[6] + sl[7]));
    const float inv = __builtin_amdgcn_rcpf(ls);
    u32 pk[8];
#pragma unroll
    for (int ct = 0; ct < 4; ++ct) { pk[2 * ct] = pack2(o[ct][0] * inv, o[ct][1] * inv); pk[2 * ct + 1] = pack2(o[ct][2] * inv, o[ct][3] * inv); }
#pragma unroll
    for (int i = 0; i < 8; ++i) pk[i] = min((u32)__builtin_amdgcn_ds_bpermute((int)bp_src, (int)pk[i]), SENT - 1u);
    st128_sc1(u32x4{pk[0], pk[1], pk[2], pk[3]}, o_e3w, rs_ws, 0u);
    st128_sc1(u32x4{pk[4], pk[5], pk[6], pk[7]}, o_e3w, rs_ws, 64u);
    if (w == 0) {
      const float lse = mall + __builtin_amdgcn_logf(ls);
      u32x2 v; v.x = __float_as_uint(lse); v.y = epoch;
      if (g == 0) st64_sc1(v, o_stw, rs_ws);
    }
  }
  STAMP(7);
  BAR();
  if (!chain) {
    const int wr = (2 * sw + 2 * j) & 7;
#pragma unroll
    for (int i = 0; i < 32; ++i) {
      const int row = ((32 * sw + 16 * j) & 127) + i;
      const int rr = row & 127;
      dma16_wo(lbase + L_R + (u32)(rr * 1024), dvo[(i) & 7], rs_wo, (u32)((h * 128 + rr) * 1024));
    }
    (void)wr;
  }
  if (chain) {
    const u32 o_efr = EF_OFF + (u32)(uc * 32768 + (tid >> 7) * 16384 + (tid & 127) * 16);
    u32x4 e4[8];
#pragma unroll
    for (int k = 0; k < 8; ++k) e4[k] = u32x4{0u, 0u, 0u, 0u};
    for (u32 spin = 0; ffn_ok; ++spin) {
#pragma unroll
      for (int k = 0; k < 8; ++k) ld128_sc1(e4[k], o_efr, rs_ws, (u32)(k * 2048));
      VMWAIT(0);
#pragma unroll
      for (int k = 0; k < 8; ++k) PIN(e4[k]);
      if (__all(tags_ok(e4, 8, epoch))) break;
      if (spin > SPIN_LIMIT) { fail = true; break; }
    }
    float s0 = 0.f, s1 = 0.f;
#pragma unroll
    for (int k = 0; k < 8; ++k) { s0 += __uint_as_float(e4[k].x); s1 += __uint_as_float(e4[k].z); }
    *(LAS u32x2*)(L + L_HS + tid * 8) = u32x2{__float_as_uint(s0), __float_as_uint(s1)};
  }
  const int lb = w >> 1, lp4 = 4 * (w & 1) + (l & 3), lss = l >> 2;
  u32 o_e3r = E3_OFF + (u32)(((h * 8 + j) * 4 + lb) * 32 * 128 + lss * 128 + lp4 * 16);
  u32 o_str = ST_OFF + (u32)(((h * 4 + lb) * 32 + (l & 31)) * 8);
  u32 a_wuvr = L_U + (u32)((4 * g + q_) * 256) + (u32)(((2 * w + (p_ >> 1)) ^ (8 * (g & 1) + 2 * q_)) * 16) + (u32)(8 * (p_ & 1));
  u32 a_latr = L_LAT + (u32)((g * 4 + (idx & 3)) * 16);
  u32 a_ctxw = L_CTX + (u32)((((w >> 1) * 4 + g) * 4 + (idx & 3)) * 16 + 8 * (w & 1));
  u32 a_ctxr = L_CTX + (u32)((g * 4 + (idx & 3)) * 16);
  u32 o_e5s = E5S_OFF + (u32)(((16 * w + 4 * (idx >> 2) + g) * 256 + bid) * 64 + (idx & 3) * 16);
  u32 a_latw;
  {
    const int e = ((l >> 5) & 1) * 4 + ((l >> 4) & 1) * 2 + ((l >> 3) & 1), k = e >> 1;
    const int c = 16 * (2 * (lp4 >> 2) + (k >> 1)) + 4 * (lp4 & 3) + 2 * (k & 1) + (e & 1);
    a_latw = L_LAT + (u32)(((((c >> 5) * 4 + ((c >> 2) & 3)) * 4 + lb) * 8 + 4 * ((c >> 4) & 1) + (c & 3)) * 2);
  }
  PIN(o_e3r); PIN(o_str); PIN(a_wuvr); PIN(a_latr); PIN(a_ctxw); PIN(a_ctxr); PIN(o_e5s); PIN(a_latw);
  STAMP(8);
  {
    u32x4 e4[2]; u32x2 st;
    for (u32 spin = 0;; ++spin) {
      ld64_sc1(st, o_str, rs_ws, 0u);
      ld128_sc1(e4[0], o_e3r, rs_ws, 0u); ld128_sc1(e4[1], o_e3r, rs_ws, 2048u);
      VMWAIT(0);
      PIN(e4[0]); PIN(e4[1]); PIN(st);
      const u32 mx = max(max(max(e4[0].x, e4[0].y), max(e4[0].z, e4[0].w)), max(max(e4[1].x, e4[1].y), max(e4[1].z, e4[1].w)));
      if (__all(mx != SENT && st.y == epoch)) break;
      if (spin > SPIN_LIMIT) { fail = true; break; }
    }
    STAMP(9);
    const float lse = __uint_as_float(st.x);
    const float M = half_max32(lse);
    const float e = __builtin_amdgcn_exp2f(lse - M);
    const float S = half_sum32(e);
    const float wgt = e * __builtin_amdgcn_rcpf(S);
    const float w0 = __builtin_bit_cast(float, __builtin_amdgcn_ds_bpermute(lss * 4, __builtin_bit_cast(int, wgt)));
    const float w1 = __builtin_bit_cast(float, __builtin_amdgcn_ds_bpermute((16 + lss) * 4, __builtin_bit_cast(int, wgt)));
    float v[8];
    v[0] = fmaf(w1, bf2f_lo(e4[1].x), w0 * bf2f_lo(e4[0].x)); v[1] = fmaf(w1, bf2f_hi(e4[1].x), w0 * bf2f_hi(e4[0].x));
    v[2] = fmaf(w1, bf2f_lo(e4[1].y), w0 * bf2f_lo(e4[0].y)); v[3] = fmaf(w1, bf2f_hi(e4[1].y), w0 * bf2f_hi(e4[0].y));
    v[4] = fmaf(w1, bf2f_lo(e4[1].z), w0 * bf2f_lo(e4[0].z)); v[5] = fmaf(w1, bf2f_hi(e4[1].z), w0 * bf2f_hi(e4[0].z));
    v[6] = fmaf(w1, bf2f_lo(e4[1].w), w0 * bf2f_lo(e4[0].w)); v[7] = fmaf(w1, bf2f_hi(e4[1].w), w0 * bf2f_hi(e4[0].w));
#pragma unroll
    for (int i = 0; i < 4; ++i) { auto s = __builtin_amdgcn_permlane32_swap(__float_as_uint(v[i]), __float_as_uint(v[i + 4]), false, false); v[i] = __uint_as_float(s[0]) + __uint_as_float(s[1]); }
#pragma unroll
    for (int i = 0; i < 2; ++i) { auto s = __builtin_amdgcn_permlane16_swap(__float_as_uint(v[i]), __float_as_uint(v[i + 2]), false, false); v[i] = __uint_as_float(s[0]) + __uint_as_float(s[1]); }
    { const bool b3 = (l & 8) != 0; const float snd = b3 ? v[0] : v[1], keep = b3 ? v[1] : v[0]; v[0] = keep + dpp<XOR8>(snd); }
    {
      int t1 = __builtin_amdgcn_update_dpp(0, __builtin_bit_cast(int, v[0]), 0x114, 0xf, 0xa, false);
      t1 = __builtin_amdgcn_update_dpp(t1, __builtin_bit_cast(int, v[0]), 0x104, 0xf, 0x5, false);
      v[0] += __builtin_bit_cast(float, t1);
    }
    *(LAS u16*)(L + a_latw) = (u16)pack2(v[0], v[0]);
  }
  BAR();
  if (w < 2) {
    const u32x2 g0 = *(LAS u32x2*)(L + L_HS + tid * 8), u0 = *(LAS u32x2*)(L + L_HS + 1024 + tid * 8);
    const float ga = rbf(__uint_as_float(g0.x)), gb = rbf(__uint_as_float(g0.y));
    const float ua = rbf(__uint_as_float(u0.x)), ub = rbf(__uint_as_float(u0.y));
    const float sa = rbf(ga * __builtin_amdgcn_rcpf(1.0f + __expf(-ga))), sb = rbf(gb * __builtin_amdgcn_rcpf(1.0f + __expf(-gb)));
    const int f = 2 * (tid & 31), b = tid >> 5;
    *(LAS u32*)(L + L_HF + (u32)(((((f >> 5) * 4 + ((f >> 2) & 3)) * 4 + b) * 8 + 4 * ((f >> 4) & 1) + (f & 3)) * 2)) = pack2(sa * ua, sb * ub);
  }
  u32x4 wk3[4][4];
  {
    const u32x4 wa0 = trfrag(L, a_wuvr, 0, 4096), wa1 = trfrag(L, a_wuvr, 8192, 12288);
    const u32x4 lb0 = lds128(L, a_latr), lb1 = lds128(L, a_latr + 256);
#pragma unroll
    for (int mt = 0; mt < 4; ++mt) {
      wk3[mt][0] = trfrag(L, trb[mt], 0, 16384); wk3[mt][1] = trfrag(L, trb[mt], 32768, 49152);
      wk3[mt][2] = trfrag(L, trb2[mt], 0, 16384); wk3[mt][3] = trfrag(L, trb2[mt], 32768, 49152);
    }
    f32x4 c = mfma32(wa0, lb0, f32x4{0.f, 0.f, 0.f, 0.f});
    c = mfma32(wa1, lb1, c);
    if (idx < 4) { u32x2 v; v.x = pack2(c[0], c[1]); v.y = pack2(c[2], c[3]); *(LAS u32x2*)(L + a_ctxw) = v; }
  }
  BAR();
  {
    u32x4 cf[4];
#pragma unroll
    for (int ks = 0; ks < 4; ++ks) cf[ks] = lds128(L, a_ctxr + ks * 256);
    f32x4 o[4];
#pragma unroll
    for (int mt = 0; mt < 4; ++mt) o[mt] = f32x4{0.f, 0.f, 0.f, 0.f};
#pragma unroll
    for (int ks = 0; ks < 4; ++ks)
#pragma unroll
      for (int mt = 0; mt < 4; ++mt) o[mt] = mfma32(wk3[mt][ks], cf[ks], o[mt]);
    const int rep = idx >> 2;
    f32x4 v = rep == 0 ? o[0] : rep == 1 ? o[1] : rep == 2 ? o[2] : o[3];
    u32x4 s0;
    s0.x = min(__float_as_uint(v[0]), SENT - 1u); s0.y = min(__float_as_uint(v[1]), SENT - 1u); s0.z = min(__float_as_uint(v[2]), SENT - 1u); s0.w = min(__float_as_uint(v[3]), SENT - 1u);
    st128_sc1(s0, o_e5s, rs_ws, 0u);
  }
  if (w < 4) {
    const int row = 4 * g + q_;
    const u32 a0 = L_WDN + (u32)(row * 128) + (u32)(((2 * w + (p_ >> 1)) ^ (2 * ((row >> 1) & 3))) * 16) + (u32)(8 * (p_ & 1));
    const u32x4 wa0 = trfrag(L, a0, 0, 2048), wa1 = trfrag(L, a0, 4096, 6144);
    const u32x4 hb0 = lds128(L, L_HF + (u32)((g * 4 + (idx & 3)) * 16)), hb1 = lds128(L, L_HF + 256 + (u32)((g * 4 + (idx & 3)) * 16));
    f32x4 y = mfma32(wa0, hb0, f32x4{0.f, 0.f, 0.f, 0.f});
    y = mfma32(wa1, hb1, y);
    const u32 off = E5Y_OFF + (u32)(((16 * j + 4 * w + g) * 32 + uc) * 128 + (idx & 3) * 32);
    if (ffn_ok && idx < 4) {
      st128_sc1(u32x4{__float_as_uint(y[0]), epoch, __float_as_uint(y[1]), epoch}, off, rs_ws, 0u);
      st128_sc1(u32x4{__float_as_uint(y[2]), epoch, __float_as_uint(y[3]), epoch}, off, rs_ws, 16u);
    }
  }
  STAMP(10);
  if (bid < 128) {
    u32x4 a4[2], y4;
    const u32 xfin = ((const u32*)x)[(l & 3) * 256 + bid * 2 + ((l >> 3) & 1)];
    const u32 o_r = E5S_OFF + (u32)(bid * 16384 + tid * 16);
    const u32 o_y = E5Y_OFF + (u32)(bid * 4096 + (tid & 255) * 16);
    const bool yv = tid < 224;
    for (u32 spin = 0;; ++spin) {
      ld128_sc1(y4, o_y, rs_ws, 0u);
      ld128_sc1(a4[0], o_r, rs_ws, 0u); ld128_sc1(a4[1], o_r, rs_ws, 8192u);
      VMWAIT(0);
      PIN(a4[0]); PIN(a4[1]); PIN(y4);
      const u32 mx = max(max(max(a4[0].x, a4[0].y), max(a4[0].z, a4[0].w)), max(max(a4[1].x, a4[1].y), max(a4[1].z, a4[1].w)));
      if (__all(mx != SENT && (!yv || (y4.y == epoch && y4.w == epoch)))) break;
      if (spin > SPIN_LIMIT) { fail = true; break; }
    }
    STAMP(11);
    float v[4];
    v[0] = __uint_as_float(a4[0].x) + __uint_as_float(a4[1].x); v[1] = __uint_as_float(a4[0].y) + __uint_as_float(a4[1].y);
    v[2] = __uint_as_float(a4[0].z) + __uint_as_float(a4[1].z); v[3] = __uint_as_float(a4[0].w) + __uint_as_float(a4[1].w);
#pragma unroll
    for (int i = 0; i < 2; ++i) { auto s = __builtin_amdgcn_permlane32_swap(__float_as_uint(v[i]), __float_as_uint(v[i + 2]), false, false); v[i] = __uint_as_float(s[0]) + __uint_as_float(s[1]); }
    { auto s = __builtin_amdgcn_permlane16_swap(__float_as_uint(v[0]), __float_as_uint(v[1]), false, false); v[0] = __uint_as_float(s[0]) + __uint_as_float(s[1]); }
    v[0] += dpp<0x124>(v[0]); v[0] += dpp<0x128>(v[0]);
    float y0 = yv ? __uint_as_float(y4.x) : 0.f, y1 = yv ? __uint_as_float(y4.z) : 0.f;
    y0 += dpp<XOR8>(y0); y1 += dpp<XOR8>(y1);
    y0 = x16_sum(y0); y1 = x16_sum(y1); y0 = x32_sum(y0); y1 = x32_sum(y1);
    if ((l & 12) == 0) *(LAS float*)(L + L_FIN + (u32)((w * 16 + (l >> 4) * 4 + (l & 3)) * 4)) = v[0];
    if (l < 8 && w < 4) *(LAS u32x2*)(L + L_HS + (u32)((w * 8 + l) * 8)) = u32x2{__float_as_uint(y0), __float_as_uint(y1)};
    BAR();
    if (w == 0) {
      const int mi = (l >> 2) & 3, b = l & 3;
      float sv[8], yv4[4];
#pragma unroll
      for (int ww = 0; ww < 8; ++ww) sv[ww] = *(LAS float*)(L + L_FIN + (u32)((ww * 16 + mi * 4 + b) * 4));
#pragma unroll
      for (int ww = 0; ww < 4; ++ww) yv4[ww] = *(LAS float*)(L + L_HS + (u32)(((ww * 8 + b * 2 + (mi >> 1)) * 2 + (mi & 1)) * 4));
      const float s = ((sv[0] + sv[1]) + (sv[2] + sv[3])) + ((sv[4] + sv[5]) + (sv[6] + sv[7]));
      const float y = (yv4[0] + yv4[1]) + (yv4[2] + yv4[3]);
      const float sb = rbf(s), yb = rbf(y);
      const float gg = rbf(yb * __builtin_amdgcn_rcpf(1.0f + __expf(-yb)));
      const float xv_ = (mi & 1) ? bf2f_hi(xfin) : bf2f_lo(xfin);
      float o = xv_ + rbf(sb * gg);
      if (fail) o = __uint_as_float(0x7fc00000u);
      if (l < 16) out[b * 512 + 4 * bid + mi] = (u16)(pack2(o, 0.f));
      if (bid == 0 && l == 0) st32_sc1(epoch, EPOCH_OFF, rs_ws);
    }
  }
}

extern "C" void kernel_launch(void* const* d_in, const int* in_sizes, int n_in, void* d_out, int out_size, void* d_ws, size_t ws_size, hipStream_t stream) {
  const u16* x = (const u16*)d_in[0]; const u16* ckv = (const u16*)d_in[1]; const u16* wq = (const u16*)d_in[2]; const u16* wuk = (const u16*)d_in[3];
  const u16* wuv = (const u16*)d_in[4]; const u16* wo = (const u16*)d_in[5]; const u16* wg = (const u16*)d_in[7]; const u16* wu = (const u16*)d_in[8]; const u16* wd = (const u16*)d_in[9];
  fused_decode<<<256, 512, 0, stream>>>(x, ckv, wq, wuk, wuv, wo, wg, wu, wd, (u16*)d_out, (unsigned char*)d_ws);
}
```

```cpp
#include <hip/hip_runtime.h>
#include <stdint.h>

typedef unsigned u32;
typedef unsigned short u16;
typedef u32 u32x4 __attribute__((ext_vector_type(4)));
typedef u32 u32x2 __attribute__((ext_vector_type(2)));
typedef float f32x4 __attribute__((ext_vector_type(4)));
typedef short s16x4 __attribute__((ext_vector_type(4)));
typedef __bf16 bf16x8 __attribute__((ext_vector_type(8)));
typedef __bf16 bf16x2 __attribute__((ext_vector_type(2)));
#define LAS __attribute__((address_space(3)))
typedef __amdgpu_buffer_rsrc_t rsrc_t;


constexpr u32 EPOCH_OFF = 0;
constexpr u32 QA_OFF = 4096;
constexpr u32 ST_OFF = QA_OFF + 4 * 32 * 2048;
constexpr u32 CF_OFF = ST_OFF + 32 * 4 * 32 * 16;
constexpr u32 EF_OFF = CF_OFF + 4096;
constexpr u32 E5Y_OFF = EF_OFF + 32 * 32768;
constexpr u32 E3_OFF = E5Y_OFF + 128 * 4096;
constexpr u32 E5S_OFF = E3_OFF + 256 * 16384;
constexpr u32 WS_END = E5S_OFF + 128 * 16384;
constexpr u32 WS_TOTAL = WS_END;
constexpr u32 SENT = 0xFFFFFFFFu;

constexpr u32 L_R = 0;
constexpr u32 L_U = 131072;
constexpr u32 L_WDN = L_U + 16384;
constexpr u32 L_M = 155648;
constexpr u32 L_XN = L_M;
constexpr u32 L_P = L_M;
constexpr u32 L_QB = L_M + 4096;
constexpr u32 L_CTX = L_QB;
constexpr u32 L_SM = L_M + 5120;
constexpr u32 L_LAT = L_M + 6144;
constexpr u32 L_HF = L_M + 6656;
constexpr u32 L_FIN = L_M + 7168;
constexpr u32 L_HS = L_M;
constexpr u32 L_FP = L_WDN;
constexpr u32 L_CNT = L_M + 7680;
constexpr u32 LDS_BYTES = 163840;

constexpr u32 SPIN_LIMIT = 1u << 16;
constexpr int CPRE = 10;
constexpr int KS = 11;
constexpr int E2_SLEEP = 8;

__device__ __forceinline__ float bf2f_lo(u32 v) { return __uint_as_float(v << 16); }
__device__ __forceinline__ float bf2f_hi(u32 v) { return __uint_as_float(v & 0xffff0000u); }
__device__ __forceinline__ u32 pack2(float lo, float hi) { bf16x2 v = {(__bf16)lo, (__bf16)hi}; return __builtin_bit_cast(u32, v); }
__device__ __forceinline__ float rbf(float f) { return __uint_as_float(((u32)__builtin_bit_cast(u16, (__bf16)f)) << 16); }
__device__ __forceinline__ f32x4 mfma32(u32x4 a, u32x4 b, f32x4 c) {
  return __builtin_amdgcn_mfma_f32_16x16x32_bf16(__builtin_bit_cast(bf16x8, a), __builtin_bit_cast(bf16x8, b), c, 0, 0, 0);
}
template <int CTRL> __device__ __forceinline__ float dpp(float x) {
  return __builtin_bit_cast(float, __builtin_amdgcn_mov_dpp(__builtin_bit_cast(int, x), CTRL, 0xf, 0xf, true));
}
constexpr int XOR1 = 0xB1, XOR2 = 0x4E, XOR7 = 0x141, XOR15 = 0x140, XOR8 = 0x128;
__device__ __forceinline__ float x16_sum(float x) { auto s = __builtin_amdgcn_permlane16_swap(__float_as_uint(x), __float_as_uint(x), false, false); return __uint_as_float(s[0]) + __uint_as_float(s[1]); }
__device__ __forceinline__ float x32_sum(float x) { auto s = __builtin_amdgcn_permlane32_swap(__float_as_uint(x), __float_as_uint(x), false, false); return __uint_as_float(s[0]) + __uint_as_float(s[1]); }
__device__ __forceinline__ float x16_max(float x) { auto s = __builtin_amdgcn_permlane16_swap(__float_as_uint(x), __float_as_uint(x), false, false); return fmaxf(__uint_as_float(s[0]), __uint_as_float(s[1])); }
__device__ __forceinline__ float x32_max(float x) { auto s = __builtin_amdgcn_permlane32_swap(__float_as_uint(x), __float_as_uint(x), false, false); return fmaxf(__uint_as_float(s[0]), __uint_as_float(s[1])); }
__device__ __forceinline__ float wave_sum(float v) {
  v += dpp<XOR1>(v); v += dpp<XOR2>(v); v += dpp<XOR7>(v); v += dpp<XOR15>(v); v = x16_sum(v); v = x32_sum(v); return v;
}
__device__ __forceinline__ float half_sum32(float v) {
  v += dpp<XOR1>(v); v += dpp<XOR2>(v); v += dpp<XOR7>(v); v += dpp<XOR15>(v); v = x16_sum(v); return v;
}
__device__ __forceinline__ float half_max32(float v) {
  v = fmaxf(v, dpp<XOR1>(v)); v = fmaxf(v, dpp<XOR2>(v)); v = fmaxf(v, dpp<XOR7>(v)); v = fmaxf(v, dpp<XOR15>(v)); v = x16_max(v); return v;
}

#define BAR() asm volatile("s_waitcnt lgkmcnt(0)\n\ts_barrier" ::: "memory")
#define VMWAIT(n) asm volatile("s_waitcnt vmcnt(" #n ")" ::: "memory")
#define SCHED() __builtin_amdgcn_sched_barrier(0)
template <int N> __device__ __forceinline__ void vmwait() { asm volatile("s_waitcnt vmcnt(%0)" :: "i"(N) : "memory"); }
#define PIN(a) asm volatile("" : "+v"(a))

__device__ __forceinline__ void ld128(u32x4& d, u32 voff, rsrc_t rs, u32 soff) {
  asm volatile("buffer_load_dwordx4 %0, %1, %2, %3 offen" : "=v"(d) : "v"(voff), "s"(rs), "s"(soff) : "memory");
}
__device__ __forceinline__ void ld128_sc1(u32x4& d, u32 voff, rsrc_t rs, u32 soff) {
  asm volatile("buffer_load_dwordx4 %0, %1, %2, %3 offen sc1" : "=v"(d) : "v"(voff), "s"(rs), "s"(soff) : "memory");
}
__device__ __forceinline__ void st128_sc1(u32x4 d, u32 voff, rsrc_t rs, u32 soff) {
  asm volatile("buffer_store_dwordx4 %0, %1, %2, %3 offen sc1\n\ts_nop 1" :: "v"(d), "v"(voff), "s"(rs), "s"(soff) : "memory");
}
__device__ __forceinline__ void st64_sc1(u32x2 d, u32 voff, rsrc_t rs) {
  asm volatile("buffer_store_dwordx2 %0, %1, %2, 0 offen sc1\n\ts_nop 1" :: "v"(d), "v"(voff), "s"(rs) : "memory");
}
__device__ __forceinline__ void st32_sc1(u32 d, u32 voff, rsrc_t rs) {
  asm volatile("buffer_store_dword %0, %1, %2, 0 offen sc1\n\ts_nop 1" :: "v"(d), "v"(voff), "s"(rs) : "memory");
}
__device__ __forceinline__ void dma16(u32 lds_dst, u32 voff, rsrc_t rs, u32 soff) {
  asm volatile("s_mov_b32 m0, %0\n\ts_nop 0\n\tbuffer_load_dwordx4 %1, %2, %3 offen lds" :: "s"(lds_dst), "v"(voff), "s"(rs), "s"(soff) : "memory");
}
__device__ __forceinline__ rsrc_t mk(const void* p, u32 bytes) { return __builtin_amdgcn_make_buffer_rsrc((void*)p, 0, bytes, 0x00020000); }
__device__ __forceinline__ u32x4 lds128(LAS unsigned char* L, u32 a) { return *(LAS u32x4*)(L + a); }
__device__ __forceinline__ u32x4 trfrag(LAS unsigned char* L, u32 a, u32 o0, u32 o1) {
  const s16x4 t0 = __builtin_amdgcn_ds_read_tr16_b64_v4i16((LAS s16x4*)(L + a + o0));
  const s16x4 t1 = __builtin_amdgcn_ds_read_tr16_b64_v4i16((LAS s16x4*)(L + a + o1));
  const u32x2 x = __builtin_bit_cast(u32x2, t0), y = __builtin_bit_cast(u32x2, t1);
  return u32x4{x.x, x.y, y.x, y.y};
}
__device__ __forceinline__ bool tags_ok(const u32x4* v, int n, u32 epoch) {
  u32 a = v[0].y & v[0].w, o = v[0].y | v[0].w;
#pragma unroll
  for (int k = 1; k < n; ++k) { a &= v[k].y & v[k].w; o |= v[k].y | v[k].w; }
  return a == epoch && o == epoch;
}


__global__ void __launch_bounds__(512, 2)
fused_decode(const u16* __restrict__ x, const u16* __restrict__ ckv, const u16* __restrict__ wq, const u16* __restrict__ wuk,
             const u16* __restrict__ wuv, const u16* __restrict__ wo, const u16* __restrict__ wg, const u16* __restrict__ wu,
             const u16* __restrict__ wd, u16* __restrict__ out, unsigned char* __restrict__ ws) {
  __shared__ __attribute__((aligned(16))) unsigned char lds[LDS_BYTES];
  LAS unsigned char* L = (LAS unsigned char*)lds;
  const u32 lbase = (u32)(size_t)L;
  const int tid = threadIdx.x;
  const int w = __builtin_amdgcn_readfirstlane(tid >> 6);
  const int l = tid & 63, g = l >> 4, idx = l & 15;
  const int bid = blockIdx.x, xc = bid & 7, r = bid >> 3;
  const int h = xc + 8 * (r >> 3), j = r & 7;
  const int P = xc * 16 + (r & 15), ab = P >> 5, as = P & 31, ag = 1 - (r >> 4);
  const bool ffn_ok = h < 28;
  const int uc = ffn_ok ? h : 27;
  const int fmat = uc & 1, ftile = uc >> 1;

  const rsrc_t rs_x = mk(x, 4096), rs_kv = mk(ckv, 4u * 4096 * 1024), rs_wq = mk(wq, 4u << 20), rs_wuk = mk(wuk, 4u << 20),
               rs_wuv = mk(wuv, 4u << 20), rs_wo = mk(wo, 4u << 20), rs_wg = mk(wg, 512 * 1792 * 2), rs_wu = mk(wu, 512 * 1792 * 2),
               rs_wd = mk(wd, 512 * 1792 * 2), rs_ws = mk(ws, WS_TOTAL);

  const bool chain = w < 4;
  const int sw = w & 3;
  const int q_ = idx >> 2, p_ = idx & 3, jp = idx & 7;
  bool fail = false;
  u32 epoch;
  u32 dvo[8];
  u32 a_qir[4], a_kvr[4], trb[4], trb2[4];
  u32 a_smw, a_smr, a_pw, a_pr, o_e3w, bp_src, o_stw;
#define SEL_INIT() u32 sel_lo[4], sel_hi[4]; \
  _Pragma("unroll") for (int i = 0; i < 4; ++i) { const bool on = (i == (jp >> 1)); \
    sel_lo[i] = on ? ((jp & 1) ? 0x01000c0cu : 0x0c0c0100u) : 0x0c0c0c0cu; sel_hi[i] = on ? ((jp & 1) ? 0x03020c0cu : 0x0c0c0302u) : 0x0c0c0c0cu; \
    PIN(sel_lo[i]); PIN(sel_hi[i]); }
#define DIAG_A(p, sel) u32x4{__builtin_amdgcn_perm(p, p, sel[0]), __builtin_amdgcn_perm(p, p, sel[1]), __builtin_amdgcn_perm(p, p, sel[2]), __builtin_amdgcn_perm(p, p, sel[3])}
#define DSTEP(acc0, acc1, wf, p0, p1, par) do { const u32* sel_ = (par) ? sel_hi : sel_lo; \
    acc0 = mfma32(DIAG_A(p0, sel_), wf, acc0); acc1 = mfma32(DIAG_A(p1, sel_), wf, acc1); } while (0)
#define LATE_ADDR() do { \
    _Pragma("unroll") for (int c = 0; c < 4; ++c) { const u32 uu = (u32)(4 * c + g); \
      a_qir[c] = L_U + uu * 256 + (((u32)idx ^ uu) * 16); \
      a_kvr[c] = L_R + (u32)((16 * w + idx) * 1024) + ((uu ^ (u32)(2 * (idx & 7))) * 16); \
      const u32 pos = (u32)((8 * w + 2 * c + (p_ >> 1)) ^ (8 * (g & 1) + 2 * q_)); \
      trb[c] = L_R + (u32)((4 * g + q_) * 1024) + pos * 16 + (u32)(8 * (p_ & 1)); trb2[c] = trb[c] + 65536u; \
      PIN(a_qir[c]); PIN(a_kvr[c]); PIN(trb[c]); PIN(trb2[c]); } \
    _Pragma("unroll") for (int i = 0; i < 8; ++i) { dvo[i] = (u32)((l ^ (2 * i)) * 16); PIN(dvo[i]); } \
    a_smw = L_SM + (u32)((w * 16 + idx) * 4); a_smr = L_SM + (u32)(idx * 4); \
    a_pw = L_P + (u32)((((w >> 1) * 4 + g) * 16 + idx) * 16 + 8 * (w & 1)); a_pr = L_P + (u32)(l * 16); \
    o_e3w = E3_OFF + (u32)(((((16 * ag + (l >> 2)) * 8 + w) * 4 + ab) * 32 + as) * 128 + (l & 3) * 16); \
    bp_src = (u32)(((l >> 2) + 16 * (l & 3)) * 4); \
    o_stw = ST_OFF + (u32)((((16 * ag + idx) * 4 + ab) * 32 + as) * 16); \
    PIN(a_smw); PIN(a_smr); PIN(a_pw); PIN(a_pr); PIN(o_e3w); PIN(bp_src); PIN(o_stw); } while (0)
  const u32 a_qpw = L_U + (u32)(((w * 4 + (g >> 1)) * 128 + 8 * idx + 4 * (g & 1)) * 4);

  const int wb = (w + j) & 7;
  const u32 vo_wq = (u32)((64 * wb + 16 * g) * 8192 + h * 256 + idx * 16);
  const u32 a_xn = L_XN + (u32)((idx >> 3) * 1024 + (64 * wb + 16 * g) * 2);
  constexpr u32 L_WUK = L_R + 112 * 1024;
#define QLOOP(EXTRA) do { \
      const u32x4 xa0 = lds128(L, a_xn), xa1 = lds128(L, a_xn + 16), xb0 = lds128(L, a_xn + 2048), xb1 = lds128(L, a_xn + 2064); \
      f32x4 qa0 = {0.f, 0.f, 0.f, 0.f}, qa1 = {0.f, 0.f, 0.f, 0.f}; \
      QS(0, EXTRA, xa0[0], xb0[0]); QS(1, EXTRA, xa0[0], xb0[0]); QS(2, EXTRA, xa0[1], xb0[1]); QS(3, EXTRA, xa0[1], xb0[1]); \
      QS(4, EXTRA, xa0[2], xb0[2]); QS(5, EXTRA, xa0[2], xb0[2]); QS(6, EXTRA, xa0[3], xb0[3]); QS(7, EXTRA, xa0[3], xb0[3]); \
      QS(8, EXTRA, xa1[0], xb1[0]); QS(9, EXTRA, xa1[0], xb1[0]); QS(10, EXTRA, xa1[1], xb1[1]); QS(11, EXTRA, xa1[1], xb1[1]); \
      QS(12, EXTRA, xa1[2], xb1[2]); QS(13, EXTRA, xa1[2], xb1[2]); QS(14, EXTRA, xa1[3], xb1[3]); QS(15, EXTRA, xa1[3], xb1[3]); \
      *(LAS f32x4*)(L + a_qpw) = qa0; *(LAS f32x4*)(L + a_qpw + 1024) = qa1; } while (0)
#define QS(t, EXTRA, P0, P1) do { vmwait<15 - (t) + (EXTRA)>(); PIN(wqf[t]); DSTEP(qa0, qa1, wqf[t], P0, P1, (t) & 1); } while (0)
#define EPOCH() do { asm volatile("s_load_dword %0, %1, 0x0\n\ts_waitcnt lgkmcnt(0)" : "=s"(epoch) : "s"(ws) : "memory"); epoch += 1u; } while (0)

  if (chain) {
    u32x4 xv, wqf[16];
    __builtin_amdgcn_s_setprio(3);
    ld128(xv, (u32)(w * 1024 + l * 16), rs_x, 0u);
#pragma unroll
    for (int t = 0; t < CPRE; ++t) ld128(wqf[t], vo_wq, rs_wq, (u32)(t * 8192));
    if (tid == 0) *(LAS u32*)(L + L_CNT) = 0u;
    SEL_INIT();
    const u32 a_qbr = L_QB + (u32)(((idx & 3) * 128 + 8 * g) * 2);
    const u32 o_qa = QA_OFF + (u32)((((idx & 3) * 32 + h) * 256 + (64 * j + 16 * sw + 4 * g) / 2) * 8);
    {
      vmwait<CPRE>();
      PIN(xv);
      float v[8];
      v[0] = bf2f_lo(xv.x); v[1] = bf2f_hi(xv.x); v[2] = bf2f_lo(xv.y); v[3] = bf2f_hi(xv.y);
      v[4] = bf2f_lo(xv.z); v[5] = bf2f_hi(xv.z); v[6] = bf2f_lo(xv.w); v[7] = bf2f_hi(xv.w);
      float s = ((v[0] + v[1]) + (v[2] + v[3])) + ((v[4] + v[5]) + (v[6] + v[7]));
      s = wave_sum(s);
      const float mean = s * (1.f / 512.f);
      float ss = 0.f;
#pragma unroll
      for (int i = 0; i < 8; ++i) { const float d = v[i] - mean; ss = fmaf(d, d, ss); }
      ss = wave_sum(ss);
      const float var = rbf(ss * (1.f / 512.f));
      const float mb = rbf(mean);
      const float den = rbf(__builtin_amdgcn_sqrtf(rbf(var + 1e-5f)));
      const float rd = __builtin_amdgcn_rcpf(den);
      u32x4 o;
      o.x = pack2(rbf(v[0] - mb) * rd, rbf(v[1] - mb) * rd);
      o.y = pack2(rbf(v[2] - mb) * rd, rbf(v[3] - mb) * rd);
      o.z = pack2(rbf(v[4] - mb) * rd, rbf(v[5] - mb) * rd);
      o.w = pack2(rbf(v[6] - mb) * rd, rbf(v[7] - mb) * rd);
      *(LAS u32x4*)(L + L_XN + w * 1024 + l * 16) = o;
    }
#pragma unroll
    for (int t = CPRE; t < 16; ++t) ld128(wqf[t], vo_wq, rs_wq, (u32)(t * 8192));
    EPOCH();
    BAR();
    QLOOP(0);
    BAR();
    u32x4 wuk[4];
    {
      u32x2 pv[8];
#pragma unroll
      for (int ww = 0; ww < 8; ++ww) pv[ww] = *(LAS u32x2*)(L + L_U + tid * 8 + ww * 2048);
      float s0, s1;
      s0 = ((__uint_as_float(pv[0].x) + __uint_as_float(pv[1].x)) + (__uint_as_float(pv[2].x) + __uint_as_float(pv[3].x))) + ((__uint_as_float(pv[4].x) + __uint_as_float(pv[5].x)) + (__uint_as_float(pv[6].x) + __uint_as_float(pv[7].x)));
      s1 = ((__uint_as_float(pv[0].y) + __uint_as_float(pv[1].y)) + (__uint_as_float(pv[2].y) + __uint_as_float(pv[3].y))) + ((__uint_as_float(pv[4].y) + __uint_as_float(pv[5].y)) + (__uint_as_float(pv[6].y) + __uint_as_float(pv[7].y)));
      *(LAS u32*)(L + L_QB + tid * 4) = pack2(s0, s1);
    }
    BAR();
    {
      u32x4 bq[4];
#pragma unroll
      for (int kk = 0; kk < 4; ++kk) bq[kk] = lds128(L, a_qbr + kk * 64);
#pragma unroll
      for (int kk = 0; kk < 4; ++kk) wuk[kk] = lds128(L, L_WUK + (u32)((16 * sw + idx) * 256) + (u32)(((4 * kk + g) ^ (2 * (idx & 7))) * 16));
      f32x4 acc = {0.f, 0.f, 0.f, 0.f}, acc2 = {0.f, 0.f, 0.f, 0.f};
      acc = mfma32(wuk[0], bq[0], acc); acc2 = mfma32(wuk[1], bq[1], acc2);
      acc = mfma32(wuk[2], bq[2], acc); acc2 = mfma32(wuk[3], bq[3], acc2);
      constexpr float SC = 0.08838834764831845f * 1.4426950408889634f;
      u32x4 o; o.x = pack2((acc[0] + acc2[0]) * SC, (acc[1] + acc2[1]) * SC); o.y = epoch; o.z = pack2((acc[2] + acc2[2]) * SC, (acc[3] + acc2[3]) * SC); o.w = epoch;
      if (idx < 4) st128_sc1(o, o_qa, rs_ws, 0u);
    }
    BAR();
    {
      const int t = tid;
      const u32 o_efw = EF_OFF + (u32)((2 * ftile + ((t & 63) >> 5)) * 32768 + ((fmat * 8 + j) * 4 + (t >> 6)) * 512 + (t & 31) * 16);
      u32x2 pv[4];
#pragma unroll
      for (int ww = 0; ww < 4; ++ww) pv[ww] = *(LAS u32x2*)(L + L_FP + t * 8 + ww * 2048);
      const float s0 = (__uint_as_float(pv[0].x) + __uint_as_float(pv[1].x)) + (__uint_as_float(pv[2].x) + __uint_as_float(pv[3].x));
      const float s1 = (__uint_as_float(pv[0].y) + __uint_as_float(pv[1].y)) + (__uint_as_float(pv[2].y) + __uint_as_float(pv[3].y));
      if (ffn_ok) st128_sc1(u32x4{__float_as_uint(s0), epoch, __float_as_uint(s1), epoch}, o_efw, rs_ws, 0u);
    }
    LATE_ADDR();
    const u32 o_e2 = QA_OFF + (u32)((ab * 32 + 16 * ag) * 2048 + w * 8192 + l * 16);
    u32 a_qiw[4];
#pragma unroll
    for (int k2 = 0; k2 < 4; ++k2) { const u32 uu = (u32)(l >> 1); a_qiw[k2] = L_U + uu * 256 + (((u32)(4 * w + k2) ^ (uu & 15)) * 16) + (u32)(8 * (l & 1)); PIN(a_qiw[k2]); }
    __builtin_amdgcn_s_sleep(E2_SLEEP);
    u32x4 q4[8];
    for (u32 spin = 0;; ++spin) {
#pragma unroll
      for (int k = 0; k < 8; ++k) ld128_sc1(q4[k], o_e2, rs_ws, (u32)(k * 1024));
      VMWAIT(0);
#pragma unroll
      for (int k = 0; k < 8; ++k) PIN(q4[k]);
      if (__all(tags_ok(q4, 8, epoch))) break;
      if (spin > SPIN_LIMIT) { fail = true; break; }
    }
#pragma unroll
    for (int k = 0; k < 8; ++k) {
      *(LAS u32*)(L + a_qiw[k >> 1] + (k & 1) * 8192) = q4[k].x;
      *(LAS u32*)(L + a_qiw[k >> 1] + (k & 1) * 8192 + 4) = q4[k].z;
    }
  } else {
    u32x4 wqf[16], f1w[4];
    if (w == 7) {
      const u32x4 zz = {0u, 0u, 0u, 1u};
      st128_sc1(zz, QA_OFF + (u32)((((l >> 4) * 32 + h) * 256 + 32 * j) * 8 + (l & 15) * 16), rs_ws, 0u);
      if (j == 0) { st128_sc1(zz, ST_OFF + (u32)(h * 2048 + l * 16), rs_ws, 0u); st128_sc1(zz, ST_OFF + (u32)(h * 2048 + l * 16), rs_ws, 1024u); }
      if (j == 0 && l == 0) st128_sc1(zz, CF_OFF + (u32)((h >> 4) * 2048 + (h & 15) * 128 + 64), rs_ws, 0u);
      const u32 oe = EF_OFF + (u32)(2 * ftile * 32768 + (fmat * 8 + j) * 2048 + l * 16);
      if (ffn_ok) { st128_sc1(zz, oe, rs_ws, 0u); st128_sc1(zz, oe, rs_ws, 1024u); st128_sc1(zz, oe, rs_ws, 32768u); st128_sc1(zz, oe, rs_ws, 32768u + 1024u);
        const u32 oy = E5Y_OFF + (u32)(((16 * j + (l >> 3)) * 32 + uc) * 128 + (l & 7) * 16);
        st128_sc1(zz, oy, rs_ws, 0u); st128_sc1(zz, oy, rs_ws, 8u * 32u * 128u); }
    }
#pragma unroll
    for (int t = 0; t < 16; ++t) ld128(wqf[t], vo_wq, rs_wq, (u32)(t * 8192));
    const u32 kvrow0 = (u32)((ab * 4096 + as * 128 + 32 * sw) * 1024);
#pragma unroll
    for (int i = 0; i < 8; ++i) { dvo[i] = (u32)((l ^ (2 * i)) * 16); PIN(dvo[i]); }
#define KV(i) dma16(lbase + L_R + (u32)((32 * sw + (i)) * 1024), dvo[(i) & 7], rs_kv, kvrow0 + (u32)((i) * 1024))
    SEL_INIT();
    const u32 a_fx = L_XN + (u32)((idx >> 3) * 1024 + (64 * j + 16 * sw + 4 * g) * 2);
    const u32 vo3 = (u32)((64 * j + 16 * sw + 4 * g) * 3584 + ftile * 256 + idx * 16);
    const rsrc_t rs_f1 = fmat ? rs_wu : rs_wg;
    EPOCH();
    BAR();
#pragma unroll
    for (int i = 0; i < 4; ++i) { const int row = 16 * sw + 4 * i + g;
      dma16(lbase + L_WUK + (u32)((16 * sw + 4 * i) * 256), (u32)(row * 8192 + ((idx ^ (2 * (row & 7))) * 16)), rs_wuk, (u32)(64 * j * 8192 + h * 256)); }
#pragma unroll
    for (int t = 0; t < 4; ++t) ld128(f1w[t], vo3, rs_f1, (u32)(t * 3584));
    KV(0); KV(1); KV(2); KV(3);
    const u32x2 fx0 = *(LAS u32x2*)(L + a_fx), fx1 = *(LAS u32x2*)(L + a_fx + 2048);
    QLOOP(12);
    BAR();
    vmwait<8>();
    BAR();
    {
      vmwait<4>();
      PIN(f1w[0]); PIN(f1w[1]); PIN(f1w[2]); PIN(f1w[3]);
      f32x4 f0a = {0.f, 0.f, 0.f, 0.f}, f1a = {0.f, 0.f, 0.f, 0.f};
      DSTEP(f0a, f1a, f1w[0], fx0.x, fx1.x, 0); DSTEP(f0a, f1a, f1w[1], fx0.x, fx1.x, 1);
      DSTEP(f0a, f1a, f1w[2], fx0.y, fx1.y, 0); DSTEP(f0a, f1a, f1w[3], fx0.y, fx1.y, 1);
      const u32 a_fpw = L_FP + (u32)(((sw * 4 + (g >> 1)) * 128 + 8 * idx + 4 * (g & 1)) * 4);
      *(LAS f32x4*)(L + a_fpw) = f0a;
      *(LAS f32x4*)(L + a_fpw + 1024) = f1a;
    }
    BAR();
    const u32x4 sv = {SENT, SENT, SENT, SENT};
    const u32 oc = E3_OFF + (u32)((h * 8 + j) * 16384 + (tid - 256) * 16);
    const u32 oc2 = E5S_OFF + (u32)((bid >> 1) * 16384 + (bid & 1) * 8192 + (tid - 256) * 16);
    KV(4); st128_sc1(sv, oc, rs_ws, 0u);
    KV(5); st128_sc1(sv, oc, rs_ws, 4096u);
    KV(6); st128_sc1(sv, oc, rs_ws, 8192u);
    KV(7); st128_sc1(sv, oc, rs_ws, 12288u);
    KV(8); st128_sc1(sv, oc2, rs_ws, 0u);
    KV(9); st128_sc1(sv, oc2, rs_ws, 4096u);
    KV(10); KV(11);
    LATE_ADDR();
    vmwait<KS>(); KV(12); vmwait<KS>(); KV(13);
    vmwait<KS>(); KV(14); vmwait<KS>(); KV(15);
    vmwait<6>();
    {
      u32 old = 0;
      if (l == 0) old = __hip_atomic_fetch_add((LAS u32*)(L + L_CNT), 1u, __ATOMIC_RELAXED, __HIP_MEMORY_SCOPE_WORKGROUP);
      old = (u32)__builtin_amdgcn_readfirstlane(old);
      if (old == 3u && l == 0) st64_sc1(u32x2{epoch, epoch}, CF_OFF + (u32)((h >> 4) * 2048 + (h & 15) * 128 + j * 8), rs_ws);
    }
    KV(16); KV(17); KV(18); KV(19); KV(20);
    vmwait<KS>(); KV(21); vmwait<KS>(); KV(22); vmwait<KS>(); KV(23); vmwait<KS>(); KV(24);
    vmwait<KS>(); KV(25); vmwait<KS>(); KV(26); vmwait<KS>(); KV(27); vmwait<KS>(); KV(28);
    vmwait<KS>(); KV(29); vmwait<KS>(); KV(30); vmwait<KS>(); KV(31);
    VMWAIT(0);
  }
  BAR();
  u32x4 bq[16];
#pragma unroll
  for (int kk = 0; kk < 16; ++kk) bq[kk] = lds128(L, a_qir[kk & 3] + (kk >> 2) * 4096);
  u32x4 cf4;
  ld128_sc1(cf4, CF_OFF + (u32)(ag * 2048 + (l >> 2) * 128 + (l & 3) * 16), rs_ws, 0u);
  f32x4 sc;
  {
    u32x4 ak[16];
#pragma unroll
    for (int kk = 0; kk < 16; ++kk) ak[kk] = lds128(L, a_kvr[kk & 3] + (kk >> 2) * 256);
    SCHED();
    f32x4 s0 = {0.f, 0.f, 0.f, 0.f}, s1 = {0.f, 0.f, 0.f, 0.f};
#pragma unroll
    for (int kk = 0; kk < 16; kk += 2) { s0 = mfma32(ak[kk], bq[kk], s0); s1 = mfma32(ak[kk + 1], bq[kk + 1], s1); }
    sc = s0 + s1;
    float m = fmaxf(fmaxf(sc[0], sc[1]), fmaxf(sc[2], sc[3]));
    m = x16_max(m); m = x32_max(m);
    if (g == 0) *(LAS float*)(L + a_smw) = m;
  }
  BAR();
  float mall;
  {
    float mv[8];
#pragma unroll
    for (int ww = 0; ww < 8; ++ww) mv[ww] = *(LAS float*)(L + a_smr + ww * 64);
    const float m = fmaxf(fmaxf(fmaxf(mv[0], mv[1]), fmaxf(mv[2], mv[3])), fmaxf(fmaxf(mv[4], mv[5]), fmaxf(mv[6], mv[7])));
    mall = m;
    const float p0 = __builtin_amdgcn_exp2f(sc[0] - m), p1 = __builtin_amdgcn_exp2f(sc[1] - m), p2 = __builtin_amdgcn_exp2f(sc[2] - m), p3 = __builtin_amdgcn_exp2f(sc[3] - m);
    u32x2 pv; pv.x = pack2(p0, p1); pv.y = pack2(p2, p3);
    *(LAS u32x2*)(L + a_pw) = pv;
    float s = (p0 + p1) + (p2 + p3);
    s = x16_sum(s); s = x32_sum(s);
    if (g == 0) *(LAS float*)(L + a_smw + 512) = s;
  }
  for (u32 spin = 0;; ++spin) {
    VMWAIT(0);
    PIN(cf4);
    if (__all(cf4.x == epoch && cf4.y == epoch && cf4.z == epoch && cf4.w == epoch)) break;
    if (spin > SPIN_LIMIT) { fail = true; break; }
    ld128_sc1(cf4, CF_OFF + (u32)(ag * 2048 + (l >> 2) * 128 + (l & 3) * 16), rs_ws, 0u);
  }
  BAR();
  {
#pragma unroll
    for (int i = 0; i < 2; ++i) { const int row = 8 * w + 4 * i + g; dma16(lbase + L_U + (u32)((8 * w + 4 * i) * 256), (u32)(row * 8192 + ((idx ^ (2 * (row & 7))) * 16)), rs_wuv, (u32)(64 * j * 8192 + h * 256)); }
    const u32 vo_wd = (u32)((l >> 3) * 1024 + (((l & 7) ^ (2 * ((l >> 4) & 3))) * 16));
    dma16(lbase + L_WDN + (u32)(w * 1024), vo_wd, rs_wd, (u32)((uc * 64 + 8 * w) * 1024 + j * 128));
  }
  {
    u32x4 pf4[4], bk[4][4];
    float sl[8];
#pragma unroll
    for (int ks = 0; ks < 4; ++ks) pf4[ks] = lds128(L, a_pr + ks * 1024);
#pragma unroll
    for (int ct = 0; ct < 4; ++ct) {
      bk[ct][0] = trfrag(L, trb[ct], 0, 16384); bk[ct][1] = trfrag(L, trb[ct], 32768, 49152);
      bk[ct][2] = trfrag(L, trb2[ct], 0, 16384); bk[ct][3] = trfrag(L, trb2[ct], 32768, 49152);
    }
#pragma unroll
    for (int ww = 0; ww < 8; ++ww) sl[ww] = *(LAS float*)(L + a_smr + 512 + ww * 64);
    SCHED();
    f32x4 o[4];
#pragma unroll
    for (int ct = 0; ct < 4; ++ct) o[ct] = f32x4{0.f, 0.f, 0.f, 0.f};
#pragma unroll
    for (int ks = 0; ks < 4; ++ks)
#pragma unroll
      for (int ct = 0; ct < 4; ++ct) o[ct] = mfma32(bk[ct][ks], pf4[ks], o[ct]);
    const float ls = ((sl[0] + sl[1]) + (sl[2] + sl[3])) + ((sl[4] + sl[5]) + (sl[6] + sl[7]));
    const float inv = __builtin_amdgcn_rcpf(ls);
    u32 pk[8];
#pragma unroll
    for (int ct = 0; ct < 4; ++ct) { pk[2 * ct] = pack2(o[ct][0] * inv, o[ct][1] * inv); pk[2 * ct + 1] = pack2(o[ct][2] * inv, o[ct][3] * inv); }
#pragma unroll
    for (int i = 0; i < 8; ++i) pk[i] = min((u32)__builtin_amdgcn_ds_bpermute((int)bp_src, (int)pk[i]), SENT - 1u);
    st128_sc1(u32x4{pk[0], pk[1], pk[2], pk[3]}, o_e3w, rs_ws, 0u);
    st128_sc1(u32x4{pk[4], pk[5], pk[6], pk[7]}, o_e3w, rs_ws, 64u);
    if (w == 0) {
      const float lse = mall + __builtin_amdgcn_logf(ls);
      if (g == 0) st128_sc1(u32x4{__float_as_uint(lse), epoch, __float_as_uint(lse), epoch}, o_stw, rs_ws, 0u);
    }
  }
  BAR();
  if (!chain) {
#pragma unroll
    for (int i = 0; i < 32; ++i) {
      const int row = ((32 * sw + 16 * j) & 127) + i;
      const int rr = row & 127;
      dma16(lbase + L_R + (u32)(rr * 1024), dvo[(i) & 7], rs_wo, (u32)((h * 128 + rr) * 1024));
    }
  }
  if (chain) {
    const u32 o_efr = EF_OFF + (u32)(uc * 32768 + (tid >> 7) * 16384 + (tid & 127) * 16);
    u32x4 e4[8];
#pragma unroll
    for (int k = 0; k < 8; ++k) e4[k] = u32x4{0u, 0u, 0u, 0u};
    for (u32 spin = 0; ffn_ok; ++spin) {
#pragma unroll
      for (int k = 0; k < 8; ++k) ld128_sc1(e4[k], o_efr, rs_ws, (u32)(k * 2048));
      VMWAIT(0);
#pragma unroll
      for (int k = 0; k < 8; ++k) PIN(e4[k]);
      if (__all(tags_ok(e4, 8, epoch))) break;
      if (spin > SPIN_LIMIT) { fail = true; break; }
    }
    float s0 = 0.f, s1 = 0.f;
#pragma unroll
    for (int k = 0; k < 8; ++k) { s0 += __uint_as_float(e4[k].x); s1 += __uint_as_float(e4[k].z); }
    *(LAS u32x2*)(L + L_HS + tid * 8) = u32x2{__float_as_uint(s0), __float_as_uint(s1)};
  }
  const int lb = w >> 1, lp4 = 4 * (w & 1) + (l & 3), lss = l >> 2;
  u32 o_e3r = E3_OFF + (u32)(((h * 8 + j) * 4 + lb) * 32 * 128 + lss * 128 + lp4 * 16);
  u32 o_str = ST_OFF + (u32)(((h * 4 + lb) * 32 + (l & 31)) * 16);
  u32 a_wuvr = L_U + (u32)((4 * g + q_) * 256) + (u32)(((2 * w + (p_ >> 1)) ^ (8 * (g & 1) + 2 * q_)) * 16) + (u32)(8 * (p_ & 1));
  u32 a_latr = L_LAT + (u32)((g * 4 + (idx & 3)) * 16);
  u32 a_ctxw = L_CTX + (u32)((((w >> 1) * 4 + g) * 4 + (idx & 3)) * 16 + 8 * (w & 1));
  u32 a_ctxr = L_CTX + (u32)((g * 4 + (idx & 3)) * 16);
  u32 o_e5s = E5S_OFF + (u32)(((16 * w + 4 * (idx >> 2) + g) * 256 + bid) * 64 + (idx & 3) * 16);
  u32 a_latw;
  {
    const int e = ((l >> 5) & 1) * 4 + ((l >> 4) & 1) * 2 + ((l >> 3) & 1), k = e >> 1;
    const int c = 16 * (2 * (lp4 >> 2) + (k >> 1)) + 4 * (lp4 & 3) + 2 * (k & 1) + (e & 1);
    a_latw = L_LAT + (u32)(((((c >> 5) * 4 + ((c >> 2) & 3)) * 4 + lb) * 8 + 4 * ((c >> 4) & 1) + (c & 3)) * 2);
  }
  PIN(o_e3r); PIN(o_str); PIN(a_wuvr); PIN(a_latr); PIN(a_ctxw); PIN(a_ctxr); PIN(o_e5s); PIN(a_latw);
  {
    u32x4 e4[2]; u32x4 st;
    for (u32 spin = 0;; ++spin) {
      ld128_sc1(st, o_str, rs_ws, 0u);
      ld128_sc1(e4[0], o_e3r, rs_ws, 0u); ld128_sc1(e4[1], o_e3r, rs_ws, 2048u);
      VMWAIT(0);
      PIN(e4[0]); PIN(e4[1]); PIN(st);
      const u32 mx = max(max(max(e4[0].x, e4[0].y), max(e4[0].z, e4[0].w)), max(max(e4[1].x, e4[1].y), max(e4[1].z, e4[1].w)));
      if (__all(mx != SENT && st.y == epoch && st.w == epoch)) break;
      if (spin > SPIN_LIMIT) { fail = true; break; }
    }
    const float lse = __uint_as_float(st.x);
    const float M = half_max32(lse);
    const float e = __builtin_amdgcn_exp2f(lse - M);
    const float S = half_sum32(e);
    const float wgt = e * __builtin_amdgcn_rcpf(S);
    const float w0 = __builtin_bit_cast(float, __builtin_amdgcn_ds_bpermute(lss * 4, __builtin_bit_cast(int, wgt)));
    const float w1 = __builtin_bit_cast(float, __builtin_amdgcn_ds_bpermute((16 + lss) * 4, __builtin_bit_cast(int, wgt)));
    float v[8];
    v[0] = fmaf(w1, bf2f_lo(e4[1].x), w0 * bf2f_lo(e4[0].x)); v[1] = fmaf(w1, bf2f_hi(e4[1].x), w0 * bf2f_hi(e4[0].x));
    v[2] = fmaf(w1, bf2f_lo(e4[1].y), w0 * bf2f_lo(e4[0].y)); v[3] = fmaf(w1, bf2f_hi(e4[1].y), w0 * bf2f_hi(e4[0].y));
    v[4] = fmaf(w1, bf2f_lo(e4[1].z), w0 * bf2f_lo(e4[0].z)); v[5] = fmaf(w1, bf2f_hi(e4[1].z), w0 * bf2f_hi(e4[0].z));
    v[6] = fmaf(w1, bf2f_lo(e4[1].w), w0 * bf2f_lo(e4[0].w)); v[7] = fmaf(w1, bf2f_hi(e4[1].w), w0 * bf2f_hi(e4[0].w));
#pragma unroll
    for (int i = 0; i < 4; ++i) { auto s = __builtin_amdgcn_permlane32_swap(__float_as_uint(v[i]), __float_as_uint(v[i + 4]), false, false); v[i] = __uint_as_float(s[0]) + __uint_as_float(s[1]); }
#pragma unroll
    for (int i = 0; i < 2; ++i) { auto s = __builtin_amdgcn_permlane16_swap(__float_as_uint(v[i]), __float_as_uint(v[i + 2]), false, false); v[i] = __uint_as_float(s[0]) + __uint_as_float(s[1]); }
    { const bool b3 = (l & 8) != 0; const float snd = b3 ? v[0] : v[1], keep = b3 ? v[1] : v[0]; v[0] = keep + dpp<XOR8>(snd); }
    {
      int t1 = __builtin_amdgcn_update_dpp(0, __builtin_bit_cast(int, v[0]), 0x114, 0xf, 0xa, false);
      t1 = __builtin_amdgcn_update_dpp(t1, __builtin_bit_cast(int, v[0]), 0x104, 0xf, 0x5, false);
      v[0] += __builtin_bit_cast(float, t1);
    }
    *(LAS u16*)(L + a_latw) = (u16)pack2(v[0], v[0]);
  }
  BAR();
  if (w < 2) {
    const u32x2 g0 = *(LAS u32x2*)(L + L_HS + tid * 8), u0 = *(LAS u32x2*)(L + L_HS + 1024 + tid * 8);
    const float ga = rbf(__uint_as_float(g0.x)), gb = rbf(__uint_as_float(g0.y));
    const float ua = rbf(__uint_as_float(u0.x)), ub = rbf(__uint_as_float(u0.y));
    const float sa = rbf(ga * __builtin_amdgcn_rcpf(1.0f + __expf(-ga))), sb = rbf(gb * __builtin_amdgcn_rcpf(1.0f + __expf(-gb)));
    const int f = 2 * (tid & 31), b = tid >> 5;
    *(LAS u32*)(L + L_HF + (u32)(((((f >> 5) * 4 + ((f >> 2) & 3)) * 4 + b) * 8 + 4 * ((f >> 4) & 1) + (f & 3)) * 2)) = pack2(sa * ua, sb * ub);
  }
  u32x4 wk3[4][4];
  {
    const u32x4 wa0 = trfrag(L, a_wuvr, 0, 4096), wa1 = trfrag(L, a_wuvr, 8192, 12288);
    const u32x4 lb0 = lds128(L, a_latr), lb1 = lds128(L, a_latr + 256);
#pragma unroll
    for (int mt = 0; mt < 4; ++mt) {
      wk3[mt][0] = trfrag(L, trb[mt], 0, 16384); wk3[mt][1] = trfrag(L, trb[mt], 32768, 49152);
      wk3[mt][2] = trfrag(L, trb2[mt], 0, 16384); wk3[mt][3] = trfrag(L, trb2[mt], 32768, 49152);
    }
    f32x4 c = mfma32(wa0, lb0, f32x4{0.f, 0.f, 0.f, 0.f});
    c = mfma32(wa1, lb1, c);
    if (idx < 4) { u32x2 v; v.x = pack2(c[0], c[1]); v.y = pack2(c[2], c[3]); *(LAS u32x2*)(L + a_ctxw) = v; }
  }
  BAR();
  {
    u32x4 cf[4];
#pragma unroll
    for (int ks = 0; ks < 4; ++ks) cf[ks] = lds128(L, a_ctxr + ks * 256);
    f32x4 o[4];
#pragma unroll
    for (int mt = 0; mt < 4; ++mt) o[mt] = f32x4{0.f, 0.f, 0.f, 0.f};
#pragma unroll
    for (int ks = 0; ks < 4; ++ks)
#pragma unroll
      for (int mt = 0; mt < 4; ++mt) o[mt] = mfma32(wk3[mt][ks], cf[ks], o[mt]);
    const int rep = idx >> 2;
    f32x4 v = rep == 0 ? o[0] : rep == 1 ? o[1] : rep == 2 ? o[2] : o[3];
    u32x4 s0;
    s0.x = min(__float_as_uint(v[0]), SENT - 1u); s0.y = min(__float_as_uint(v[1]), SENT - 1u); s0.z = min(__float_as_uint(v[2]), SENT - 1u); s0.w = min(__float_as_uint(v[3]), SENT - 1u);
    st128_sc1(s0, o_e5s, rs_ws, 0u);
  }
  if (w < 4) {
    const int row = 4 * g + q_;
    const u32 a0 = L_WDN + (u32)(row * 128) + (u32)(((2 * w + (p_ >> 1)) ^ (2 * ((row >> 1) & 3))) * 16) + (u32)(8 * (p_ & 1));
    const u32x4 wa0 = trfrag(L, a0, 0, 2048), wa1 = trfrag(L, a0, 4096, 6144);
    const u32x4 hb0 = lds128(L, L_HF + (u32)((g * 4 + (idx & 3)) * 16)), hb1 = lds128(L, L_HF + 256 + (u32)((g * 4 + (idx & 3)) * 16));
    f32x4 y = mfma32(wa0, hb0, f32x4{0.f, 0.f, 0.f, 0.f});
    y = mfma32(wa1, hb1, y);
    const u32 off = E5Y_OFF + (u32)(((16 * j + 4 * w + g) * 32 + uc) * 128 + (idx & 3) * 32);
    if (ffn_ok && idx < 4) {
      st128_sc1(u32x4{__float_as_uint(y[0]), epoch, __float_as_uint(y[1]), epoch}, off, rs_ws, 0u);
      st128_sc1(u32x4{__float_as_uint(y[2]), epoch, __float_as_uint(y[3]), epoch}, off, rs_ws, 16u);
    }
  }
  if (bid < 128) {
    u32x4 a4[2], y4;
    const u32 xfin = ((const u32*)x)[(l & 3) * 256 + bid * 2 + ((l >> 3) & 1)];
    const u32 o_r = E5S_OFF + (u32)(bid * 16384 + tid * 16);
    const u32 o_y = E5Y_OFF + (u32)(bid * 4096 + (tid & 255) * 16);
    const bool yv = tid < 224;
    for (u32 spin = 0;; ++spin) {
      ld128_sc1(y4, o_y, rs_ws, 0u);
      ld128_sc1(a4[0], o_r, rs_ws, 0u); ld128_sc1(a4[1], o_r, rs_ws, 8192u);
      VMWAIT(0);
      PIN(a4[0]); PIN(a4[1]); PIN(y4);
      const u32 mx = max(max(max(a4[0].x, a4[0].y), max(a4[0].z, a4[0].w)), max(max(a4[1].x, a4[1].y), max(a4[1].z, a4[1].w)));
      if (__all(mx != SENT && (!yv || (y4.y == epoch && y4.w == epoch)))) break;
      if (spin > SPIN_LIMIT) { fail = true; break; }
    }
    float v[4];
    v[0] = __uint_as_float(a4[0].x) + __uint_as_float(a4[1].x); v[1] = __uint_as_float(a4[0].y) + __uint_as_float(a4[1].y);
    v[2] = __uint_as_float(a4[0].z) + __uint_as_float(a4[1].z); v[3] = __uint_as_float(a4[0].w) + __uint_as_float(a4[1].w);
#pragma unroll
    for (int i = 0; i < 2; ++i) { auto s = __builtin_amdgcn_permlane32_swap(__float_as_uint(v[i]), __float_as_uint(v[i + 2]), false, false); v[i] = __uint_as_float(s[0]) + __uint_as_float(s[1]); }
    { auto s = __builtin_amdgcn_permlane16_swap(__float_as_uint(v[0]), __float_as_uint(v[1]), false, false); v[0] = __uint_as_float(s[0]) + __uint_as_float(s[1]); }
    v[0] += dpp<0x124>(v[0]); v[0] += dpp<0x128>(v[0]);
    float y0 = yv ? __uint_as_float(y4.x) : 0.f, y1 = yv ? __uint_as_float(y4.z) : 0.f;
    y0 += dpp<XOR8>(y0); y1 += dpp<XOR8>(y1);
    y0 = x16_sum(y0); y1 = x16_sum(y1); y0 = x32_sum(y0); y1 = x32_sum(y1);
    if ((l & 12) == 0) *(LAS float*)(L + L_FIN + (u32)((w * 16 + (l >> 4) * 4 + (l & 3)) * 4)) = v[0];
    if (l < 8 && w < 4) *(LAS u32x2*)(L + L_HS + (u32)((w * 8 + l) * 8)) = u32x2{__float_as_uint(y0), __float_as_uint(y1)};
    BAR();
    if (w == 0) {
      const int mi = (l >> 2) & 3, b = l & 3;
      float sv[8], yv4[4];
#pragma unroll
      for (int ww = 0; ww < 8; ++ww) sv[ww] = *(LAS float*)(L + L_FIN + (u32)((ww * 16 + mi * 4 + b) * 4));
#pragma unroll
      for (int ww = 0; ww < 4; ++ww) yv4[ww] = *(LAS float*)(L + L_HS + (u32)(((ww * 8 + b * 2 + (mi >> 1)) * 2 + (mi & 1)) * 4));
      const float s = ((sv[0] + sv[1]) + (sv[2] + sv[3])) + ((sv[4] + sv[5]) + (sv[6] + sv[7]));
      const float y = (yv4[0] + yv4[1]) + (yv4[2] + yv4[3]);
      const float sb = rbf(s), yb = rbf(y);
      const float gg = rbf(yb * __builtin_amdgcn_rcpf(1.0f + __expf(-yb)));
      const float xv_ = (mi & 1) ? bf2f_hi(xfin) : bf2f_lo(xfin);
      float o = xv_ + rbf(sb * gg);
      if (fail) o = __uint_as_float(0x7fc00000u);
      if (l < 16) out[b * 512 + 4 * bid + mi] = (u16)(pack2(o, 0.f));
      if (bid == 0 && l == 0) st32_sc1(epoch, EPOCH_OFF, rs_ws);
    }
  }
}

namespace simple {
__device__ __forceinline__ float bf2f(u16 b) { return __uint_as_float(((unsigned)b) << 16); }
__device__ __forceinline__ u16 f2bf(float f) { return __builtin_bit_cast(u16, (__bf16)f); }
constexpr int B = 4, H = 32, SK = 4096, D = 128, M = 512, DC = 512, F = 1792;
__device__ void layernorm_lds(const u16* __restrict__ x, float* xn) {
  const int tid = threadIdx.x, w = tid >> 6, l = tid & 63;
  float v[8]; float s = 0.f;
  for (int i = 0; i < 8; ++i) { v[i] = bf2f(x[w * M + l * 8 + i]); s += v[i]; }
  for (int o = 32; o; o >>= 1) s += __shfl_xor(s, o);
  const float mean = s * (1.f / M);
  float ss = 0.f;
  for (int i = 0; i < 8; ++i) { const float d = v[i] - mean; ss += d * d; }
  for (int o = 32; o; o >>= 1) ss += __shfl_xor(ss, o);
  const float var = rbf(ss * (1.f / M)), mb = rbf(mean), den = rbf(sqrtf(rbf(var + 1e-5f)));
  for (int i = 0; i < 8; ++i) xn[w * M + l * 8 + i] = rbf(rbf(v[i] - mb) / den);
  __syncthreads();
}
__global__ void k1(const u16* x, const u16* wq, const u16* wg, const u16* wu, float* q, float* hid) {
  __shared__ float xn[B * M]; __shared__ float red[4][4][64][2];
  layernorm_lds(x, xn);
  const int tid = threadIdx.x, c = tid & 63, kq = tid >> 6;
  if (blockIdx.x < 64) {
    const int n = blockIdx.x * 64 + c;
    float a[4] = {0, 0, 0, 0};
    for (int m = kq * 128; m < kq * 128 + 128; ++m) { const float w = bf2f(wq[(size_t)m * 4096 + n]); for (int b = 0; b < 4; ++b) a[b] += xn[b * M + m] * w; }
    for (int b = 0; b < 4; ++b) red[kq][b][c][0] = a[b];
    __syncthreads();
    if (kq == 0) for (int b = 0; b < 4; ++b) q[b * 4096 + n] = rbf(red[0][b][c][0] + red[1][b][c][0] + red[2][b][c][0] + red[3][b][c][0]);
  } else {
    const int n = (blockIdx.x - 64) * 64 + c;
    float a[4] = {0, 0, 0, 0}, u[4] = {0, 0, 0, 0};
    for (int m = kq * 128; m < kq * 128 + 128; ++m) { const float w = bf2f(wg[(size_t)m * F + n]), w2 = bf2f(wu[(size_t)m * F + n]); for (int b = 0; b < 4; ++b) { a[b] += xn[b * M + m] * w; u[b] += xn[b * M + m] * w2; } }
    for (int b = 0; b < 4; ++b) { red[kq][b][c][0] = a[b]; red[kq][b][c][1] = u[b]; }
    __syncthreads();
    if (kq == 0) for (int b = 0; b < 4; ++b) {
      const float g = rbf(red[0][b][c][0] + red[1][b][c][0] + red[2][b][c][0] + red[3][b][c][0]);
      const float uu = rbf(red[0][b][c][1] + red[1][b][c][1] + red[2][b][c][1] + red[3][b][c][1]);
      hid[b * F + n] = rbf(rbf(g / (1.f + __expf(-g))) * uu);
    }
  }
}
__global__ void k2(const float* q, const u16* wuk, float* qa) {
  const int h = blockIdx.x, c = blockIdx.y * 64 + threadIdx.x;
  float a[4] = {0, 0, 0, 0};
  for (int d = 0; d < D; ++d) { const float w = bf2f(wuk[((size_t)c * H + h) * D + d]); for (int b = 0; b < 4; ++b) a[b] += q[b * 4096 + h * D + d] * w; }
  for (int b = 0; b < 4; ++b) qa[(b * H + h) * DC + c] = rbf(a[b]);
}
__global__ void k3(const float* qa, const u16* ckv, float* po, float* pm, float* pl) {
  __shared__ float s[H][64];
  const int sp = blockIdx.x, b = blockIdx.y, tid = threadIdx.x;
  const u16* kv = ckv + ((size_t)b * SK + sp * 64) * DC;
  for (int i = tid; i < H * 64; i += 256) { const int h = i >> 6, k = i & 63; float a = 0; for (int c = 0; c < DC; ++c) a += qa[(b * H + h) * DC + c] * bf2f(kv[k * DC + c]); s[h][k] = a * 0.08838834764831845f; }
  __syncthreads();
  if (tid < H) { float m = -INFINITY; for (int k = 0; k < 64; ++k) m = fmaxf(m, s[tid][k]); float l = 0; for (int k = 0; k < 64; ++k) { const float p = __expf(s[tid][k] - m); l += p; s[tid][k] = rbf(p); } pm[(b * H + tid) * 64 + sp] = m; pl[(b * H + tid) * 64 + sp] = l; }
  __syncthreads();
  for (int i = tid; i < H * DC; i += 256) { const int h = i >> 9, c = i & 511; float a = 0; for (int k = 0; k < 64; ++k) a += s[h][k] * bf2f(kv[k * DC + c]); po[((size_t)(b * H + h) * 64 + sp) * DC + c] = a; }
}
__global__ void k4(const float* po, const float* pm, const float* pl, const u16* wuv, float* ctx) {
  __shared__ float lat[DC];
  const int h = blockIdx.x, b = blockIdx.y, tid = threadIdx.x;
  float mx = -INFINITY; for (int s = 0; s < 64; ++s) mx = fmaxf(mx, pm[(b * H + h) * 64 + s]);
  float L = 0, a = 0; for (int s = 0; s < 64; ++s) { const float w = __expf(pm[(b * H + h) * 64 + s] - mx); L += w * pl[(b * H + h) * 64 + s]; a += w * po[((size_t)(b * H + h) * 64 + s) * DC + tid]; }
  lat[tid] = rbf(a / L);
  __syncthreads();
  if (tid < D) { float c = 0; for (int k = 0; k < DC; ++k) c += lat[k] * bf2f(wuv[((size_t)k * H + h) * D + tid]); ctx[b * 4096 + h * D + tid] = rbf(c); }
}
__global__ void k5(const u16* x, const float* ctx, const float* hid, const u16* wo, const u16* wd, u16* out) {
  __shared__ float red[4][4][64][2];
  const int tid = threadIdx.x, c = tid & 63, kq = tid >> 6, n = blockIdx.x * 64 + c;
  float a[4] = {0, 0, 0, 0}, y[4] = {0, 0, 0, 0};
  for (int k = kq * 1024; k < kq * 1024 + 1024; ++k) { const float w = bf2f(wo[(size_t)k * M + n]); for (int b = 0; b < 4; ++b) a[b] += ctx[b * 4096 + k] * w; }
  for (int k = kq * 448; k < kq * 448 + 448; ++k) { const float w = bf2f(wd[(size_t)k * M + n]); for (int b = 0; b < 4; ++b) y[b] += hid[b * F + k] * w; }
  for (int b = 0; b < 4; ++b) { red[kq][b][c][0] = a[b]; red[kq][b][c][1] = y[b]; }
  __syncthreads();
  if (kq == 0) for (int b = 0; b < 4; ++b) {
    const float s = rbf(red[0][b][c][0] + red[1][b][c][0] + red[2][b][c][0] + red[3][b][c][0]);
    const float yy = rbf(red[0][b][c][1] + red[1][b][c][1] + red[2][b][c][1] + red[3][b][c][1]);
    out[b * M + n] = f2bf(bf2f(x[b * M + n]) + rbf(s * rbf(yy / (1.f + __expf(-yy)))));
  }
}
}

extern "C" void kernel_launch(void* const* d_in, const int* in_sizes, int n_in, void* d_out, int out_size, void* d_ws, size_t ws_size, hipStream_t stream) {
  const u16* x = (const u16*)d_in[0]; const u16* ckv = (const u16*)d_in[1]; const u16* wq = (const u16*)d_in[2]; const u16* wuk = (const u16*)d_in[3];
  const u16* wuv = (const u16*)d_in[4]; const u16* wo = (const u16*)d_in[5]; const u16* wg = (const u16*)d_in[7]; const u16* wu = (const u16*)d_in[8]; const u16* wd = (const u16*)d_in[9];
  int dev = 0, cus = 0;
  (void)hipGetDevice(&dev);
  (void)hipDeviceGetAttribute(&cus, hipDeviceAttributeMultiprocessorCount, dev);
  if (cus >= 256 && ws_size >= (size_t)WS_TOTAL) {
    fused_decode<<<256, 512, 0, stream>>>(x, ckv, wq, wuk, wuv, wo, wg, wu, wd, (u16*)d_out, (unsigned char*)d_ws);
    return;
  }
  float* ws = (float*)d_ws;
  float* q = ws; float* hid = q + 4 * 4096; float* qa = hid + 4 * 1792; float* pm = qa + 4 * 32 * 512; float* pl = pm + 4 * 32 * 64; float* ctx = pl + 4 * 32 * 64; float* po = ctx + 4 * 4096;
  simple::k1<<<92, 256, 0, stream>>>(x, wq, wg, wu, q, hid);
  simple::k2<<<dim3(32, 8), 64, 0, stream>>>(q, wuk, qa);
  simple::k3<<<dim3(64, 4), 256, 0, stream>>>(qa, ckv, po, pm, pl);
  simple::k4<<<dim3(32, 4), 512, 0, stream>>>(po, pm, pl, wuv, ctx);
  simple::k5<<<8, 256, 0, stream>>>(x, ctx, hid, wo, wd, (u16*)d_out);
}
```
